```python
import torch

def _softmax(x, axis=-1):
    return torch.softmax(x, dim=axis)
def _gelu(x):
    return torch.nn.functional.gelu(x, approximate="tanh")
def _silu(x):
    return torch.nn.functional.silu(x)
def _sigmoid(x):
    return torch.sigmoid(x)
def _layernorm(x, axis=-1, eps=1e-5):
    m = x.mean(dim=axis, keepdim=True)
    v = x.var(dim=axis, keepdim=True, unbiased=False)
    return (x - m) / torch.sqrt(v + eps)
def _scatter_add(tgt, idx, src, axis=0):
    return tgt.clone().index_add_(axis, idx, src)
def _segment_sum(src, seg, n):
    out = torch.zeros((n,) + src.shape[1:], dtype=src.dtype)
    return out.index_add_(0, seg, src)
def _pad(x, pw):
    flat = tuple(v for pair in reversed(pw) for v in pair)
    return torch.nn.functional.pad(x, flat)

kernel_name = 'parallel_sum__linear_attn_block__dense_activation_relu__prompt'

def setup_inputs():

    _w = (lambda a: (lambda t: t.to(torch.bfloat16) if t.is_floating_point() else t)(torch.as_tensor(a)))
    def _A_setup():
        B, S, D = 1, 2048, 640
        return {
            "x": _w((lambda *s: torch.randn(*s, dtype=torch.bfloat16) * 0.05)(B, S, D)),
            "wqkv": _w((lambda *s: torch.randn(*s, dtype=torch.bfloat16) * 0.2)(D, 3 * D)),
            "wo": _w((lambda *s: torch.randn(*s, dtype=torch.bfloat16) * 0.2)(D, D)),
        }
    _A_in = _A_setup()

    def _B_setup():
        B, S, D, Dff = 1, 2048, 640, 1536
        return {
            "x": _w((lambda *s: torch.randn(*s, dtype=torch.bfloat16) * 0.05)(B, S, D)),
            "w1": _w((lambda *s: torch.randn(*s, dtype=torch.bfloat16) * 0.05)(D, Dff)),
            "w2": _w((lambda *s: torch.randn(*s, dtype=torch.bfloat16) * 0.05)(Dff, D)),
        }
    _B_in = _B_setup()

    return {
        "A_x": _A_in["x"],
        "A_wqkv": _A_in["wqkv"],
        "A_wo": _A_in["wo"],
        "B_x": _B_in["x"],
        "B_w1": _B_in["w1"],
        "B_w2": _B_in["w2"],
    }

def reference(A_x, A_wqkv, A_wo, B_x, B_w1, B_w2):
    def _A_ref(x, wqkv, wo):
        B, S, D = x.shape
        H = 32; Dh = D // H
        qkv = (lambda x, *d: x.permute(*d))((x @ wqkv).reshape(B, S, 3, H, Dh), 2, 0, 3, 1, 4)
        q, k, v = qkv[0], qkv[1], qkv[2]
        phi = lambda t: torch.where(t > 0, t + 1, torch.exp(t)) + 1e-6
        pq, pk = phi(q), phi(k)
        kv = torch.einsum("bhsd,bhse->bhde", pk, v)
        z = torch.einsum("bhsd->bhd", pk)
        num = torch.einsum("bhqd,bhde->bhqe", pq, kv)
        den = torch.einsum("bhqd,bhd->bhq", pq, z)[..., None] + 1e-6
        o = num / den
        return (lambda x, *d: x.permute(*d))(o, 0, 2, 1, 3).reshape(B, S, D) @ wo

    def _B_ref(x, w1, w2):
        h = torch.einsum("bsd,df->bsf", x, w1)
        zero = torch.zeros_like(h)
        h = torch.maximum(h, zero)
        return torch.einsum("bsf,fd->bsd", h, w2)

    x = A_x
    xn = _layernorm(x, axis=-1)
    return x + _A_ref(xn, A_wqkv, A_wo) + _B_ref(xn, B_w1, B_w2)
```

```cpp
#include <hip/hip_runtime.h>
#include <hip/hip_bf16.h>
#include <stdint.h>

typedef __attribute__((ext_vector_type(8))) __bf16 bf8;
typedef __attribute__((ext_vector_type(4))) __bf16 bf4;
typedef __attribute__((ext_vector_type(2))) __bf16 bf2;
typedef __attribute__((ext_vector_type(4))) short s4;
typedef __attribute__((ext_vector_type(4))) float f4;
typedef __attribute__((ext_vector_type(2))) float f2;
typedef __attribute__((ext_vector_type(4))) unsigned u4;
typedef __attribute__((ext_vector_type(2))) unsigned u2;
#define AS1 __attribute__((address_space(1)))
#define AS3 __attribute__((address_space(3)))
#define DI __device__ __forceinline__

constexpr int S_ = 2048, D_ = 640, DFF = 1536, N3 = 1920;
constexpr size_t WS_HF = 0;
constexpr size_t WS_PQ = WS_HF + (size_t)128 * 48 * 1024;
constexpr size_t WS_AF = WS_PQ + (size_t)128 * 20 * 1024;
constexpr size_t WS_W2 = WS_AF + (size_t)128 * 20 * 1024;
constexpr size_t WS_MF = WS_W2 + (size_t)8 * 48 * 5 * 1024;
constexpr size_t WS_KV = WS_MF + (size_t)8 * 20 * 5 * 1024;
constexpr size_t WS_FL = WS_KV + (size_t)16 * 32 * 420 * 4;
constexpr size_t WS_END = WS_FL + 256 * 128;

DI float bfr(float v) { return (float)(__bf16)v; }
DI unsigned pk2(float a, float b) { f2 v = {a, b}; return __builtin_bit_cast(unsigned, __builtin_convertvector(v, bf2)); }
DI float lo16(unsigned u) { return __builtin_bit_cast(float, u << 16); }
DI float hi16(unsigned u) { return __builtin_bit_cast(float, u & 0xffff0000u); }
DI f4 mfma(bf8 a, bf8 b, f4 c) { return __builtin_amdgcn_mfma_f32_16x16x32_bf16(a, b, c, 0, 0, 0); }
DI void glds16(const void* g, char* l) {
  __builtin_amdgcn_global_load_lds((const AS1 void*)g, (AS3 void*)l, 16, 0, 0);
}
DI bf8 trfrag(const char* p0, const char* p1) {
  s4 a = __builtin_amdgcn_ds_read_tr16_b64_v4i16((AS3 s4*)p0);
  s4 b = __builtin_amdgcn_ds_read_tr16_b64_v4i16((AS3 s4*)p1);
  return __builtin_bit_cast(bf8, __builtin_shufflevector(a, b, 0, 1, 2, 3, 4, 5, 6, 7));
}
DI float dot2(unsigned a, unsigned b, float c) {
  return __builtin_amdgcn_fdot2_f32_bf16(__builtin_bit_cast(bf2, a), __builtin_bit_cast(bf2, b), c, false);
}
DI float xg_sum(float v) {
  v += __shfl_xor(v, 16);
  v += __shfl_xor(v, 32);
  return v;
}
DI float phi(float t) {
  return fmaxf(t, 0.f) + 1e-6f + __builtin_amdgcn_exp2f(fminf(t, 0.f) * 1.44269504088896341f);
}
#define WAITV(n) asm volatile("s_waitcnt vmcnt(" #n ")" ::: "memory")
#define WAITL0() asm volatile("s_waitcnt lgkmcnt(0)" ::: "memory")

constexpr int A_BYTES = 128 * 128, B_BYTES = 8 * 14 * 256, STG = A_BYTES + B_BYTES;
constexpr int L_W2 = 3 * STG;
constexpr int L_ST = L_W2 + 8192;
constexpr int L_CS = L_ST + 1024;
constexpr int L_KV = L_CS + 2048;
constexpr int K1_LDS = L_KV + 4 * 420 * 4 + 32;

template <bool KV>
DI void k1_body(const __bf16* __restrict__ x, const __bf16* __restrict__ wqkv, const __bf16* __restrict__ w1,
                const __bf16* __restrict__ w2, char* __restrict__ ws, char* smem, int rb, int ci, int bid) {
  const int tid = threadIdx.x, lane = tid & 63;
  const int w = __builtin_amdgcn_readfirstlane(tid >> 6), wm = w >> 1, wn = w & 1;
  const int g = lane >> 4, l15 = lane & 15;
  const __bf16* srcA[4];
#pragma unroll
  for (int n = 0; n < 4; ++n) {
    int row = (w * 4 + n) * 8 + (lane >> 3);
    srcA[n] = x + (size_t)(rb * 128 + row) * D_ + (((lane & 7) ^ ((row >> 1) & 7)) << 3);
  }
  const __bf16* srcB[7];
  int strB[7];
#pragma unroll
  for (int n = 0; n < 7; ++n) {
    int bl = n * 4 + g;
    int hiK = bl >= 14;
    int tile = bl - 14 * hiK;
    int kk = (2 * w + hiK) * 8 + ((l15 >> 1) ^ (hiK << 2));
    int cw = tile >= 7;
    int c = (tile - 7 * cw) * 2 + (lane & 1);
    const __bf16* p;
    int st;
    if (KV) {
      if (c < 10) { int v = c >= 5; p = wqkv + (640 + 640 * v + 80 * ci + 40 * cw + 8 * (c - 5 * v)); st = N3; }
      else { p = w1 + (1024 + 64 * ci + 32 * cw + 8 * (c - 10)); st = DFF; }
    } else {
      if (c < 6) { int cc = (cw && c >= 4) ? c - 2 : c; p = wqkv + (80 * ci + 48 * cw + 8 * cc); st = N3; }
      else { p = w1 + (128 * ci + 64 * cw + 8 * (c - 6)); st = DFF; }
    }
    srcB[n] = p + (size_t)kk * st;
    strB[n] = st * 64;
  }
  auto stage = [&](int buf) {
    char* a = smem + buf * STG + w * 4096;
    char* b = smem + buf * STG + A_BYTES + w * 7168;
#pragma unroll
    for (int n = 0; n < 4; ++n) { glds16(srcA[n], a + n * 1024); srcA[n] += 64; }
#pragma unroll
    for (int n = 0; n < 7; ++n) { glds16(srcB[n], b + n * 1024); srcB[n] += strB[n]; }
  };
  stage(0);
  stage(1);
  const int sw = l15 >> 1;
  int aoff[2], boff[2];
#pragma unroll
  for (int s = 0; s < 2; ++s) aoff[s] = (wm * 64 + l15) * 128 + ((((s ^ wm) * 4 + g) ^ sw) << 4);
  {
    int q = l15 >> 2, p = lane & 3;
    boff[0] = A_BYTES + g * 3584 + wn * 1792 + (q + 4 * (g & 1)) * 32 + 8 * p;
    boff[1] = A_BYTES + g * 3584 + wn * 1792 + (q + 4 * ((g & 1) ^ 1)) * 32 + 8 * p;
  }
  f4 acc[4][7];
#pragma unroll
  for (int i = 0; i < 4; ++i)
#pragma unroll
    for (int j = 0; j < 7; ++j) acc[i][j] = f4{0.f, 0.f, 0.f, 0.f};
  float rs[2] = {0.f, 0.f}, rq[2] = {0.f, 0.f}, cs[7] = {0.f, 0.f, 0.f, 0.f, 0.f, 0.f, 0.f};
  bf8 fa[2][4], fb[2][7];
  auto rd = [&](int buf, int s) {
    const char* base = smem + buf * STG;
#pragma unroll
    for (int j = 0; j < 7; ++j) {
      int o = (s ^ wm) * 14336 + j * 256;
      fb[s][j] = trfrag(base + boff[0] + o, base + boff[1] + o);
    }
#pragma unroll
    for (int i = 0; i < 4; ++i) fa[s][i] = *(const bf8*)(base + aoff[s] + ((i ^ (2 * wn)) << 11));
  };
  auto mm = [&](int s) {
#pragma unroll
    for (int j = 0; j < 7; ++j)
#pragma unroll
      for (int i = 0; i < 4; ++i)
        acc[i][j] = (KV && j < 5) ? mfma(fa[s][i], fb[s][j], acc[i][j]) : mfma(fb[s][j], fa[s][i], acc[i][j]);
#pragma unroll
    for (int i = 0; i < 2; ++i) {
      u4 u = __builtin_bit_cast(u4, fa[s][i]);
#pragma unroll
      for (int e = 0; e < 4; ++e) { rs[i] = dot2(u[e], 0x3f803f80u, rs[i]); rq[i] = dot2(u[e], u[e], rq[i]); }
    }
    if (s == 0) {
#pragma unroll
      for (int j = 0; j < 7; ++j) {
        u4 u = __builtin_bit_cast(u4, fb[s][j]);
#pragma unroll
        for (int e = 0; e < 4; ++e) cs[j] = dot2(u[e], 0x3f803f80u, cs[j]);
      }
    }
  };
#pragma unroll
  for (int t = 0; t < 10; ++t) {
    if (t < 9) WAITV(11); else WAITV(2);
    __builtin_amdgcn_s_barrier();
    if (t < 8) stage((t + 2) % 3);
    if (t == 8) {
      int e = bid < 240 ? bid : 0;
      int ks = e / 5, cb = e - ks * 5;
#pragma unroll
      for (int n = 0; n < 2; ++n) {
        int tile = n * 4 + g;
        int kk = w * 8 + ((l15 >> 1) ^ ((w & 1) << 2));
        glds16(w2 + (size_t)(ks * 32 + kk) * D_ + cb * 128 + tile * 16 + (lane & 1) * 8, smem + L_W2 + w * 2048 + n * 1024);
      }
    }
    rd(t % 3, 0);
    if (t > 0) mm(1);
    rd(t % 3, 1);
    mm(0);
    asm volatile("" : "+v"(rs[0]), "+v"(rs[1]), "+v"(rq[0]), "+v"(rq[1]), "+v"(cs[0]), "+v"(cs[1]), "+v"(cs[2]), "+v"(cs[3]),
                 "+v"(cs[4]), "+v"(cs[5]), "+v"(cs[6]));
    WAITL0();
    __builtin_amdgcn_sched_barrier(0);
  }
  mm(1);
  float* st = (float*)(smem + L_ST);
  float* csl = (float*)(smem + L_CS);
  float* kvl = (float*)(smem + L_KV);
#pragma unroll
  for (int i = 0; i < 2; ++i) {
    float su = xg_sum(rs[i]), sq = xg_sum(rq[i]);
    float mean = su * (1.f / 640.f);
    float var = fmaxf(sq * (1.f / 640.f) - mean * mean, 0.f);
    float sd = bfr(sqrtf(bfr(bfr(var) + 1e-5f)));
    if (g == 0) *(f2*)(st + (wm * 64 + ((i ^ (2 * wn)) << 4) + l15) * 2) = f2{bfr(mean), 1.f / sd};
  }
#pragma unroll
  for (int j = 0; j < 7; ++j) {
    float c = xg_sum(cs[j]);
    if (g == 0) csl[wm * 224 + (wn * 7 + j) * 16 + l15] = c;
  }
  if (KV) { for (int i = tid; i < 1680; i += 256) kvl[i] = 0.f; }
  WAITV(0);
  __syncthreads();
  if (bid < 240) {
    int ks = bid / 5, cb = bid - ks * 5;
    int q = l15 >> 2, p = lane & 3;
#pragma unroll
    for (int n = 0; n < 2; ++n) {
      int tile = w * 2 + n;
      int kg = g >> 1, rr = ((g & 1) * 4 + q) ^ ((kg & 1) << 2);
      const char* p0 = smem + L_W2 + (kg * 8 + tile) * 256 + rr * 32 + 8 * p;
      bf8 f = trfrag(p0, p0 + 4096);
      int col = cb * 128 + tile * 16, cg = col / 80, tt = (col - cg * 80) >> 4;
      *(bf8*)(ws + WS_W2 + ((size_t)(cg * 48 + ks) * 5 + tt) * 1024 + lane * 16) = f;
    }
  }
  if (tid == 0) *(unsigned*)(ws + WS_FL + bid * 128) = 0u;
  f2 mr[4];
#pragma unroll
  for (int i = 0; i < 4; ++i) mr[i] = *(const f2*)(st + (wm * 64 + ((i ^ (2 * wn)) << 4) + l15) * 2);
#pragma unroll
  for (int j = (KV ? 5 : 0); j < 7; ++j) {
    const bool isq = !KV && j < 3;
    if (isq && j == 2 && wn) continue;
    f4 c0 = *(const f4*)(csl + (wn * 7 + j) * 16 + 4 * g), c1 = *(const f4*)(csl + 224 + (wn * 7 + j) * 16 + 4 * g);
    f4 c = c0 + c1;
    int col = KV ? 1024 + 64 * ci + 32 * wn + 16 * (j - 5) : (isq ? 80 * ci + 48 * wn + 16 * j : 128 * ci + 64 * wn + 16 * (j - 3));
    char* dst = ws + (isq ? WS_PQ : WS_HF) + (size_t)(col >> 5) * 1024 + ((col >> 4) & 1) * 8 + lane * 16;
#pragma unroll
    for (int i = 0; i < 4; ++i) {
      float v[4];
#pragma unroll
      for (int e = 0; e < 4; ++e) {
        float y = (acc[i][j][e] - mr[i][0] * c[e]) * mr[i][1];
        v[e] = isq ? phi(y) : fmaxf(y, 0.f);
      }
      int rt = rb * 8 + wm * 4 + (i ^ (2 * wn));
      *(u2*)(dst + (size_t)rt * (isq ? 20 : 48) * 1024) = u2{pk2(v[0], v[1]), pk2(v[2], v[3])};
    }
  }
  if (KV) {
    float cc[5];
#pragma unroll
    for (int j = 0; j < 5; ++j) cc[j] = csl[(wn * 7 + j) * 16 + l15] + csl[224 + (wn * 7 + j) * 16 + l15];
    f4 kvacc[8];
#pragma unroll
    for (int i = 0; i < 8; ++i) kvacc[i] = f4{0.f, 0.f, 0.f, 0.f};
#pragma unroll
    for (int hp = 0; hp < 2; ++hp) {
      u4 fr[6];
#pragma unroll
      for (int ii = 0; ii < 2; ++ii) {
        int i = hp * 2 + ii;
        const float* sp = st + (wm * 64 + ((i ^ (2 * wn)) << 4) + 4 * g) * 2;
        f4 s0 = *(const f4*)sp, s1 = *(const f4*)(sp + 4);
        float mean[4] = {s0[0], s0[2], s1[0], s1[2]}, rstd[4] = {s0[1], s0[3], s1[1], s1[3]};
#pragma unroll
        for (int j = 0; j < 5; ++j) {
          float v[4];
#pragma unroll
          for (int e = 0; e < 4; ++e) {
            float y = (acc[i][j][e] - mean[e] * cc[j]) * rstd[e];
            v[e] = (j < 2) ? phi(y) : (j == 2 ? (l15 < 8 ? phi(y) : y) : y);
          }
          fr[j][2 * ii] = pk2(v[0], v[1]);
          fr[j][2 * ii + 1] = pk2(v[2], v[3]);
        }
      }
#pragma unroll
      for (int e = 0; e < 4; ++e) fr[5][e] = l15 < 8 ? 0x3f803f80u : fr[2][e];
#define FR(i) __builtin_bit_cast(bf8, fr[i])
      kvacc[0] = mfma(FR(0), FR(5), kvacc[0]);
      kvacc[1] = mfma(FR(0), FR(3), kvacc[1]);
      kvacc[2] = mfma(FR(1), FR(5), kvacc[2]);
      kvacc[3] = mfma(FR(1), FR(3), kvacc[3]);
      kvacc[4] = mfma(FR(1), FR(4), kvacc[4]);
      kvacc[5] = mfma(FR(2), FR(5), kvacc[5]);
      kvacc[6] = mfma(FR(2), FR(3), kvacc[6]);
      kvacc[7] = mfma(FR(2), FR(4), kvacc[7]);
#undef FR
    }
    const int ta[8] = {0, 0, 1, 1, 1, 2, 2, 2}, tb[8] = {2, 3, 2, 3, 4, 2, 3, 4};
#pragma unroll
    for (int m = 0; m < 8; ++m) {
      int vc = tb[m] * 16 + l15 - 40;
#pragma unroll
      for (int e = 0; e < 4; ++e) {
        int kc = ta[m] * 16 + 4 * g + e;
        int hk = kc >= 20, d = kc - 20 * hk;
        int hv = vc >= 20, ee = vc - 20 * hv;
        bool okv = kc < 40 && vc >= 0 && hk == hv;
        bool okz = kc < 40 && vc == -8;
        int idx = (wn * 2 + hk) * 420 + (okz ? 400 + d : d * 20 + ee);
        if (okv || okz) __hip_atomic_fetch_add((AS3 float*)(kvl + idx), kvacc[m][e], __ATOMIC_RELAXED, __HIP_MEMORY_SCOPE_WORKGROUP);
      }
    }
    __syncthreads();
    float* dst = (float*)(ws + WS_KV) + ((size_t)rb * 32 + ci * 4) * 420;
    for (int i = tid; i < 420; i += 256) *(f4*)(dst + i * 4) = *(const f4*)(kvl + i * 4);
  }
}

__global__ __launch_bounds__(256, 1) void k1_kernel(const __bf16* __restrict__ x, const __bf16* __restrict__ wqkv,
                                                    const __bf16* __restrict__ w1, const __bf16* __restrict__ w2,
                                                    char* __restrict__ ws) {
  extern __shared__ __attribute__((aligned(16))) char smem[];
  const int bid = blockIdx.x, xcd = bid & 7, idx = bid >> 3;
  const int rb = (xcd >> 1) * 4 + (idx & 3), cgi = idx >> 2;
  const int ci = (xcd & 1) * 4 + (cgi >> 1);
  if (cgi & 1) k1_body<false>(x, wqkv, w1, w2, ws, smem, rb, ci, bid);
  else k1_body<true>(x, wqkv, w1, w2, ws, smem, rb, ci, bid);
}

typedef __attribute__((ext_vector_type(4))) int i4;
DI __amdgpu_buffer_rsrc_t mkrsrc(const void* p, unsigned bytes) {
  return __builtin_amdgcn_make_buffer_rsrc((void*)p, 0, bytes, 0x00020000);
}
template <int AUX>
DI bf8 ldfrag(__amdgpu_buffer_rsrc_t r, int voff, int soff) {
  return __builtin_bit_cast(bf8, __builtin_amdgcn_raw_buffer_load_b128(r, voff, soff, AUX));
}
constexpr int L2_KVB = 0;
constexpr int L2_WO = 2048;
constexpr int L2_RED = 8192;
constexpr int L2_ACC = 0;
constexpr int K2_LDS = 61440;

DI void k2_job(const __bf16* __restrict__ wo, char* __restrict__ ws, char* smem, int r, int c) {
  const int tid = threadIdx.x, lane = tid & 63, w = __builtin_amdgcn_readfirstlane(tid >> 6);
  const int g = lane >> 4, l15 = lane & 15;
  const float* kvp = (const float*)(ws + WS_KV);
  const int hh = 4 * c + w, rt = r * 4 + g;
  u2 pq[5];
  int goff[5];
#pragma unroll
  for (int m = 0; m < 5; ++m) {
    int k = 20 * hh + 4 * m;
    goff[m] = (rt * 20 + (k >> 5)) * 1024 + (((k >> 2) & 3) * 16 + l15) * 16 + ((k >> 4) & 1) * 8;
    pq[m] = *(const u2*)(ws + WS_PQ + goff[m]);
  }
  f4 sum = {0.f, 0.f, 0.f, 0.f};
  float* red = (float*)(smem + L2_RED);
  if (tid < 240) {
    int half, o;
    if (tid < 200) { half = tid >= 100; o = r * 420 + (tid - 100 * half) * 4; }
    else { int t = tid - 200; half = t >= 20; t -= 20 * half; o = (4 * c + t / 5) * 420 + 400 + (t % 5) * 4; }
    const float* p = kvp + (size_t)half * 8 * 32 * 420 + o;
    f4 v[8];
#pragma unroll
    for (int i = 0; i < 8; ++i) v[i] = *(const f4*)(p + (size_t)i * 32 * 420);
    sum = ((v[0] + v[1]) + (v[2] + v[3])) + ((v[4] + v[5]) + (v[6] + v[7]));
  }
  u4 wv = {0u, 0u, 0u, 0u};
  for (int i = tid; i < 320; i += 256) {
    int row = i / 10, ch = i - row * 10;
    wv = u4{0u, 0u, 0u, 0u};
    if (row < 20) wv = *(const u4*)(wo + (size_t)(20 * r + row) * D_ + 80 * c + ch * 8);
    *(u4*)(smem + L2_WO + i * 16) = wv;
  }
  if (tid < 128) *(u4*)(smem + L2_KVB + tid * 16) = u4{0u, 0u, 0u, 0u};
  if (tid < 240) *(f4*)(red + tid * 4) = sum;
  __syncthreads();
  if (tid < 100) {
    f4 a = *(const f4*)(red + tid * 4), b = *(const f4*)(red + 400 + tid * 4);
    a += b;
    int d = tid / 5, e = (tid - d * 5) * 4;
    *(u2*)(smem + L2_KVB + d * 64 + e * 2) = u2{pk2(a[0], a[1]), pk2(a[2], a[3])};
  }
  {
    const float* z0 = red + 800 + w * 20;
    float den = 0.f, p[20];
#pragma unroll
    for (int m = 0; m < 5; ++m) {
      f4 za = *(const f4*)(z0 + 4 * m), zb = *(const f4*)(z0 + 80 + 4 * m);
      p[4 * m] = lo16(pq[m][0]); p[4 * m + 1] = hi16(pq[m][0]); p[4 * m + 2] = lo16(pq[m][1]); p[4 * m + 3] = hi16(pq[m][1]);
#pragma unroll
      for (int e = 0; e < 4; ++e) den += p[4 * m + e] * bfr(za[e] + zb[e]);
    }
    float rden = 1.f / bfr(bfr(den) + 1e-6f);
#pragma unroll
    for (int m = 0; m < 5; ++m) {
      unsigned long long o = (unsigned long long)pk2(p[4 * m] * rden, p[4 * m + 1] * rden) |
                             ((unsigned long long)pk2(p[4 * m + 2] * rden, p[4 * m + 3] * rden) << 32);
      __hip_atomic_store((AS1 unsigned long long*)(ws + WS_AF + goff[m]), o, __ATOMIC_RELAXED, __HIP_MEMORY_SCOPE_AGENT);
    }
  }
  __syncthreads();
  {
    bf8 a0 = *(const bf8*)(smem + L2_KVB + l15 * 64 + g * 16);
    bf8 a1 = *(const bf8*)(smem + L2_KVB + (16 + l15) * 64 + g * 16);
    int q = l15 >> 2, p = lane & 3;
    for (int t = w; t < 5; t += 4) {
      const char* p0 = smem + L2_WO + (8 * g + q) * 160 + t * 32 + p * 8;
      bf8 b = trfrag(p0, p0 + 640);
      f4 z = {0.f, 0.f, 0.f, 0.f};
      f4 m0 = mfma(a0, b, z), m1 = mfma(a1, b, z);
      int k0 = 20 * r + 4 * g, k1 = 20 * r + 16;
      char* mf = ws + WS_MF + (size_t)c * 20 * 5120 + t * 1024 + l15 * 16;
      unsigned long long o0 = (unsigned long long)pk2(m0[0], m0[1]) | ((unsigned long long)pk2(m0[2], m0[3]) << 32);
      unsigned long long o1 = (unsigned long long)pk2(m1[0], m1[1]) | ((unsigned long long)pk2(m1[2], m1[3]) << 32);
      __hip_atomic_store((AS1 unsigned long long*)(mf + (k0 >> 5) * 5120 + ((k0 >> 2) & 3) * 256 + ((k0 >> 4) & 1) * 8), o0,
                         __ATOMIC_RELAXED, __HIP_MEMORY_SCOPE_AGENT);
      if (g == 0)
        __hip_atomic_store((AS1 unsigned long long*)(mf + (k1 >> 5) * 5120 + ((k1 >> 2) & 3) * 256 + ((k1 >> 4) & 1) * 8), o1,
                           __ATOMIC_RELAXED, __HIP_MEMORY_SCOPE_AGENT);
    }
  }
}

DI void k2_map(int bid, int& r, int& c) {
  const int xcd = bid & 7, idx = bid >> 3;
  r = (xcd >> 1) * 8 + (idx & 7);
  c = (xcd & 1) * 4 + (idx >> 3);
}

#define FUSE_JOB 1
__global__ __launch_bounds__(256, 1) void kmid_kernel(const __bf16* __restrict__ wo, char* __restrict__ ws) {
  extern __shared__ __attribute__((aligned(16))) char smem[];
  int r, c;
  k2_map(blockIdx.x, r, c);
  k2_job(wo, ws, smem, r, c);
}

constexpr int RING = 4;
__global__ __launch_bounds__(256, 1) void k2_kernel(const __bf16* __restrict__ x, const __bf16* __restrict__ wo,
                                                    char* __restrict__ ws, __bf16* __restrict__ out) {
  extern __shared__ __attribute__((aligned(16))) char smem[];
  const int tid = threadIdx.x, lane = tid & 63, w = __builtin_amdgcn_readfirstlane(tid >> 6);
  const int g = lane >> 4, l15 = lane & 15;
  int r, c;
  k2_map(blockIdx.x, r, c);
  const __amdgpu_buffer_rsrc_t rs = mkrsrc(ws, (unsigned)WS_END);
  const int vo = lane * 16;
  const int hA = (int)WS_HF + (r * 4 * 48 + w) * 1024, hB = (int)WS_W2 + (c * 48 + w) * 5120;
  const int aA = (int)WS_AF + (r * 4 * 20 + w) * 1024, aB = (int)WS_MF + (c * 20 + w) * 5120;
  bf8 fa[RING][4], fb[RING][5];
  auto ld = [&](int i) {
    const int sl = i % RING;
    if (i < 12) {
#pragma unroll
      for (int j = 0; j < 5; ++j) fb[sl][j] = ldfrag<0>(rs, vo, hB + i * 4 * 5120 + j * 1024);
#pragma unroll
      for (int j = 0; j < 4; ++j) fa[sl][j] = ldfrag<0>(rs, vo, hA + (i * 4 + j * 48) * 1024);
    } else {
#pragma unroll
      for (int j = 0; j < 5; ++j) fb[sl][j] = ldfrag<16>(rs, vo, aB + (i - 12) * 4 * 5120 + j * 1024);
#pragma unroll
      for (int j = 0; j < 4; ++j) fa[sl][j] = ldfrag<16>(rs, vo, aA + ((i - 12) * 4 + j * 20) * 1024);
    }
  };
#pragma unroll
  for (int i = 0; i < RING; ++i) ld(i);
  k2_job(wo, ws, smem, r, c);
  WAITV(0);
  __syncthreads();
  if (tid == 0) __hip_atomic_store((AS1 unsigned*)(ws + WS_FL + (r * 8 + c) * 128), 1u, __ATOMIC_RELAXED, __HIP_MEMORY_SCOPE_AGENT);
  const AS1 unsigned* fl = (const AS1 unsigned*)(ws + WS_FL + (lane < 8 ? r * 8 + lane : (lane < 40 ? (lane - 8) * 8 + c : r * 8 + c)) * 128);
  f4 acc[4][5];
#pragma unroll
  for (int i = 0; i < 4; ++i)
#pragma unroll
    for (int j = 0; j < 5; ++j) acc[i][j] = f4{0.f, 0.f, 0.f, 0.f};
#pragma unroll
  for (int i = 0; i < 17; ++i) {
    const int sl = i % RING;
#pragma unroll
    for (int j = 0; j < 5; ++j)
#pragma unroll
      for (int m = 0; m < 4; ++m) acc[m][j] = mfma(fb[sl][j], fa[sl][m], acc[m][j]);
    if (i + RING == 12) {
      for (unsigned spin = 0; spin < (1u << 22); ++spin) {
        unsigned v = __hip_atomic_load(fl, __ATOMIC_RELAXED, __HIP_MEMORY_SCOPE_AGENT);
        if (__all(v == 1u)) break;
        __builtin_amdgcn_s_sleep(1);
      }
      __builtin_amdgcn_fence(__ATOMIC_ACQUIRE, "wavefront");
    }
    if (i + RING < 17) ld(i + RING);
    __builtin_amdgcn_sched_barrier(0);
  }
  const size_t orow = (size_t)(r * 64 + w * 16 + l15) * D_ + c * 80 + 4 * g;
  u2 xr[5];
#pragma unroll
  for (int j = 0; j < 5; ++j) xr[j] = *(const u2*)(x + orow + 16 * j);
  __syncthreads();
#pragma unroll
  for (int m = 0; m < 4; ++m) {
    if (m != w) {
      int slot = m < w ? m : m - 1;
#pragma unroll
      for (int j = 0; j < 5; ++j) *(f4*)(smem + L2_ACC + ((w * 3 + slot) * 5 + j) * 1024 + lane * 16) = acc[m][j];
    }
  }
  __syncthreads();
  f4 own[5];
#pragma unroll
  for (int j = 0; j < 5; ++j) own[j] = w == 0 ? acc[0][j] : (w == 1 ? acc[1][j] : (w == 2 ? acc[2][j] : acc[3][j]));
  f4 tot[5];
#pragma unroll
  for (int j = 0; j < 5; ++j) tot[j] = f4{0.f, 0.f, 0.f, 0.f};
#pragma unroll
  for (int s = 0; s < 4; ++s) {
    int slot = w < s ? w : w - 1;
#pragma unroll
    for (int j = 0; j < 5; ++j) {
      f4 v = own[j];
      if (s != w) v = *(const f4*)(smem + L2_ACC + ((s * 3 + slot) * 5 + j) * 1024 + lane * 16);
      tot[j] += v;
    }
  }
#pragma unroll
  for (int j = 0; j < 5; ++j) {
    u2 o = {pk2(tot[j][0] + lo16(xr[j][0]), tot[j][1] + hi16(xr[j][0])), pk2(tot[j][2] + lo16(xr[j][1]), tot[j][3] + hi16(xr[j][1]))};
    *(u2*)(out + orow + 16 * j) = o;
  }
}

extern "C" void kernel_launch(void* const* d_in, const int* in_sizes, int n_in, void* d_out, int out_size, void* d_ws,
                              size_t ws_size, hipStream_t stream) {
  const __bf16* x = (const __bf16*)d_in[0];
  const __bf16* wqkv = (const __bf16*)d_in[1];
  const __bf16* wo = (const __bf16*)d_in[2];
  const __bf16* w1 = (const __bf16*)d_in[4];
  const __bf16* w2 = (const __bf16*)d_in[5];
  char* ws = (char*)d_ws;
  (void)hipFuncSetAttribute((const void*)k1_kernel, hipFuncAttributeMaxDynamicSharedMemorySize, K1_LDS);
  (void)hipFuncSetAttribute((const void*)k2_kernel, hipFuncAttributeMaxDynamicSharedMemorySize, 98304);
  k1_kernel<<<256, 256, K1_LDS, stream>>>(x, wqkv, w1, w2, ws);
  k2_kernel<<<256, 256, 98304, stream>>>(x, wo, ws, (__bf16*)d_out);
}
```

```cpp
#include <hip/hip_runtime.h>
#include <hip/hip_bf16.h>
#include <stdint.h>

typedef __attribute__((ext_vector_type(8))) __bf16 bf8;
typedef __attribute__((ext_vector_type(4))) __bf16 bf4;
typedef __attribute__((ext_vector_type(2))) __bf16 bf2;
typedef __attribute__((ext_vector_type(4))) short s4;
typedef __attribute__((ext_vector_type(4))) float f4;
typedef __attribute__((ext_vector_type(2))) float f2;
typedef __attribute__((ext_vector_type(4))) unsigned u4;
typedef __attribute__((ext_vector_type(2))) unsigned u2;
#define AS1 __attribute__((address_space(1)))
#define AS3 __attribute__((address_space(3)))
#define DI __device__ __forceinline__

constexpr int S_ = 2048, D_ = 640, DFF = 1536, N3 = 1920;
constexpr size_t WS_HF = 0;
constexpr size_t WS_PQ = WS_HF + (size_t)128 * 48 * 1024;
constexpr size_t WS_AF = WS_PQ + (size_t)128 * 20 * 1024;
constexpr size_t WS_W2 = WS_AF + (size_t)128 * 20 * 1024;
constexpr size_t WS_MF = WS_W2 + (size_t)8 * 48 * 5 * 1024;
constexpr size_t WS_KV = WS_MF + (size_t)8 * 20 * 5 * 1024;
constexpr size_t WS_FL = WS_KV + (size_t)16 * 32 * 480 * 4;
constexpr size_t WS_END = WS_FL + 256 * 128;
#define STAMP(k, i) do {} while (0)

DI float bfr(float v) { return (float)(__bf16)v; }
DI unsigned pk2(float a, float b) { f2 v = {a, b}; return __builtin_bit_cast(unsigned, __builtin_convertvector(v, bf2)); }
DI float lo16(unsigned u) { return __builtin_bit_cast(float, u << 16); }
DI float hi16(unsigned u) { return __builtin_bit_cast(float, u & 0xffff0000u); }
DI f4 mfma(bf8 a, bf8 b, f4 c) { return __builtin_amdgcn_mfma_f32_16x16x32_bf16(a, b, c, 0, 0, 0); }
DI void glds16(const void* g, char* l) {
  __builtin_amdgcn_global_load_lds((const AS1 void*)g, (AS3 void*)l, 16, 0, 0);
}
DI bf8 trfrag(const char* p0, const char* p1) {
  s4 a = __builtin_amdgcn_ds_read_tr16_b64_v4i16((AS3 s4*)p0);
  s4 b = __builtin_amdgcn_ds_read_tr16_b64_v4i16((AS3 s4*)p1);
  return __builtin_bit_cast(bf8, __builtin_shufflevector(a, b, 0, 1, 2, 3, 4, 5, 6, 7));
}
DI float dot2(unsigned a, unsigned b, float c) {
  return __builtin_amdgcn_fdot2_f32_bf16(__builtin_bit_cast(bf2, a), __builtin_bit_cast(bf2, b), c, false);
}
DI float xg_sum(float v) {
  v += __shfl_xor(v, 16);
  v += __shfl_xor(v, 32);
  return v;
}
DI float phi(float t) {
  return fmaxf(t, 0.f) + 1e-6f + __builtin_amdgcn_exp2f(fminf(t, 0.f) * 1.44269504088896341f);
}
#define WAITV(n) asm volatile("s_waitcnt vmcnt(" #n ")" ::: "memory")
#define WAITL0() asm volatile("s_waitcnt lgkmcnt(0)" ::: "memory")

DI __amdgpu_buffer_rsrc_t mkrsrc(const void* p, unsigned bytes) {
  return __builtin_amdgcn_make_buffer_rsrc((void*)p, 0, bytes, 0x00020000);
}
DI void blds16(__amdgpu_buffer_rsrc_t r, char* l, int voff, int soff) {
  __builtin_amdgcn_raw_ptr_buffer_load_lds(r, (AS3 void*)l, 16, voff, soff, 0, 0);
}
constexpr int A_BYTES = 128 * 128, B_BYTES = 64 * 448, STG = A_BYTES + B_BYTES;
constexpr int L_W2 = 3 * STG;
constexpr int L_ST = L_W2 + 8192;
constexpr int L_CS = L_ST + 1024;
constexpr int K1_LDS = L_CS + 4 * 224 * 4;

template <bool KV>
DI void k1_body(const __bf16* __restrict__ x, const __bf16* __restrict__ wqkv, const __bf16* __restrict__ w1,
                const __bf16* __restrict__ w2, char* __restrict__ ws, char* smem, int rb, int ci, int bid) {
  constexpr int NQ = KV ? 20 : 12, NH = KV ? 8 : 16;
  constexpr int QB = 64 * NQ * 16;
  const int tid = threadIdx.x, lane = tid & 63;
  const int w = __builtin_amdgcn_readfirstlane(tid >> 6), wm = w >> 1, wn = w & 1;
  const int g = lane >> 4, l15 = lane & 15;
  STAMP(0, 0);
  const __amdgpu_buffer_rsrc_t rx = mkrsrc(x, S_ * D_ * 2), rq = mkrsrc(wqkv, D_ * N3 * 2), r1 = mkrsrc(w1, D_ * DFF * 2);
  int voA[2], voB[4];
#pragma unroll
  for (int n = 0; n < 2; ++n) {
    int row = (w * 2 + n) * 8 + (lane >> 3);
    voA[n] = (rb * 128 + row) * (D_ * 2) + (((lane & 7) ^ ((row >> 1) & 7)) << 4);
  }
  const int ib0 = w < 4 ? w * 4 : 16 + (w - 4) * 3;
#pragma unroll
  for (int n = 0; n < 4; ++n) {
    int ib = ib0 + n;
    if (ib < NQ) {
      int c = ib * 64 + lane, k = c / NQ, ch = c - k * NQ;
      ch ^= ((k >> 3) & 1) << 1;
      int col;
      if (KV) { int cw = ch >= 10, cc = ch - 10 * cw, v = cc >= 5; col = 640 + 640 * v + 80 * ci + 40 * cw + 8 * (cc - 5 * v); }
      else col = 80 * ci + 8 * (ch >= 10 ? ch - 2 : ch);
      voB[n] = (k * N3 + col) * 2;
    } else {
      int c = (ib - NQ) * 64 + lane, k = c / NH, ch = c - k * NH;
      int f = KV ? (((k >> 1) & 1) | ((k >> 2) & 2)) : ((k & 3) | ((k >> 1) & 4));
      ch ^= f << 1;
      voB[n] = (k * DFF + (KV ? 1024 + 64 * ci : 128 * ci) + 8 * ch) * 2;
    }
  }
  auto stage = [&](int sb, int t) {
    char* a = smem + sb + w * 2048;
    char* b = smem + sb + A_BYTES + ib0 * 1024;
#pragma unroll
    for (int n = 0; n < 2; ++n) blds16(rx, a + n * 1024, voA[n], t * 128);
#pragma unroll
    for (int n = 0; n < 4; ++n) {
      if (n == 3 && w >= 4) break;
      if (ib0 + n < NQ) blds16(rq, b + n * 1024, voB[n], t * (64 * N3 * 2));
      else blds16(r1, b + n * 1024, voB[n], t * (64 * DFF * 2));
    }
  };
  stage(0, 0);
  stage(STG, 1);
  const int ks0 = wm & 1;
  const int q4 = l15 >> 2, p4 = lane & 3;
  int aoff[2], qoff, hoff[2];
#pragma unroll
  for (int s = 0; s < 2; ++s) aoff[s] = (wm * 32 + l15) * 128 + ((((s ^ ks0) * 4 + g) ^ (l15 >> 1)) << 4);
  qoff = A_BYTES + (8 * g + q4) * (NQ * 16) + 8 * p4;
  const int gx = (g & 1) << 5;
  {
    int f = KV ? (((q4 >> 1) & 1) | ((g & 1) << 1)) : (q4 | ((g & 1) << 2));
    hoff[0] = A_BYTES + QB + (8 * g + q4) * (NH * 16) + 8 * p4;
    hoff[1] = f << 5;
  }
  constexpr int JQ = KV ? 5 : 3;
  int toff[7];
#pragma unroll
  for (int j = 0; j < 7; ++j)
    toff[j] = j < JQ ? qoff + (((wn * JQ + j) << 5) ^ gx) : hoff[0] + (((wn * (7 - JQ) + j - JQ) << 5) ^ hoff[1]);
  f4 acc[2][7];
#pragma unroll
  for (int i = 0; i < 2; ++i)
#pragma unroll
    for (int j = 0; j < 7; ++j) acc[i][j] = f4{0.f, 0.f, 0.f, 0.f};
  float rs = 0.f, rq2 = 0.f, cs[7] = {0.f, 0.f, 0.f, 0.f, 0.f, 0.f, 0.f};
  bf8 fa[2][2], fb[2][7];
#pragma unroll
  for (int i = 0; i < 2; ++i) fa[1][i] = __builtin_bit_cast(bf8, u4{0u, 0u, 0u, 0u});
#pragma unroll
  for (int j = 0; j < 7; ++j) fb[1][j] = __builtin_bit_cast(bf8, u4{0u, 0u, 0u, 0u});
  auto rd = [&](int sb, int s) {
    const char* base = smem + sb;
#pragma unroll
    for (int i = 0; i < 2; ++i) fa[s][i] = *(const bf8*)(base + aoff[s] + ((i ^ wn) << 11));
#pragma unroll
    for (int j = 0; j < 7; ++j) {
      const int rs16 = (j < JQ ? NQ : NH) * 16;
      const char* p = base + toff[j] + (s ^ ks0) * 32 * rs16;
      fb[s][j] = trfrag(p, p + 4 * rs16);
    }
  };
  auto mm = [&](int s) {
#pragma unroll
    for (int j = 0; j < 7; ++j)
#pragma unroll
      for (int i = 0; i < 2; ++i)
        acc[i][j] = (KV && j < 5) ? mfma(fa[s][i], fb[s][j], acc[i][j]) : mfma(fb[s][j], fa[s][i], acc[i][j]);
    u4 u = __builtin_bit_cast(u4, fa[s][0]);
#pragma unroll
    for (int e = 0; e < 4; ++e) { rs = dot2(u[e], 0x3f803f80u, rs); rq2 = dot2(u[e], u[e], rq2); }
  };
  auto interleave = [&]() {
#pragma unroll
    for (int i = 0; i < 14; ++i) {
      __builtin_amdgcn_sched_group_barrier(0x008, 1, 0);
      if (i < 8) __builtin_amdgcn_sched_group_barrier(0x100, 2, 0);
      __builtin_amdgcn_sched_group_barrier(0x002, 1, 0);
    }
  };
  int sb = 0, sn = 2 * STG;
#pragma unroll 1
  for (int t = 0; t < 10; ++t) {
    if (t == 9) WAITV(0); else if (w < 4) WAITV(6); else WAITV(5);
    __builtin_amdgcn_s_barrier();
    if (t < 8) stage(sn, t + 2);
    else if (t == 8) {
      int e = bid < 240 ? bid : 0;
      int ks = e / 5, cb = e - ks * 5;
      blds16(mkrsrc(w2, DFF * D_ * 2), smem + L_W2 + w * 1024, ((ks * 32 + w * 4 + g) * D_ + cb * 128 + l15 * 8) * 2, 0);
    }
    __builtin_amdgcn_sched_barrier(0);
    rd(sb, 0);
    mm(1);
    interleave();
    __builtin_amdgcn_sched_barrier(0);
    rd(sb, 1);
    mm(0);
    interleave();
    __builtin_amdgcn_sched_barrier(0);
    if ((t & 1) == (wm >> 1)) {
#pragma unroll
      for (int e = 0; e < 4; ++e)
#pragma unroll
        for (int j = 0; j < 7; ++j) cs[j] = dot2(__builtin_bit_cast(u4, fb[0][j])[e], 0x3f803f80u, cs[j]);
    }
    WAITL0();
    sn = sb;
    sb = sb == 2 * STG ? 0 : sb + STG;
  }
  __builtin_amdgcn_sched_barrier(0);
  mm(1);
  STAMP(0, 4);
  float* st = (float*)(smem + L_ST);
  float* csl = (float*)(smem + L_CS);
  {
    float su = xg_sum(rs), sq = xg_sum(rq2);
    float mean = su * (1.f / 640.f);
    float var = fmaxf(sq * (1.f / 640.f) - mean * mean, 0.f);
    float sd = bfr(sqrtf(bfr(bfr(var) + 1e-5f)));
    if (g == 0) *(f2*)(st + (wm * 32 + wn * 16 + l15) * 2) = f2{bfr(mean), 1.f / sd};
  }
#pragma unroll
  for (int j = 0; j < 7; ++j) cs[j] = xg_sum(cs[j]);
  {
    float c0 = g == 0 ? cs[0] : (g == 1 ? cs[1] : (g == 2 ? cs[2] : cs[3]));
    float c1 = g == 0 ? cs[4] : (g == 1 ? cs[5] : cs[6]);
    csl[wm * 224 + wn * 112 + lane] = c0;
    if (g < 3) csl[wm * 224 + wn * 112 + 64 + lane] = c1;
  }
  WAITL0();
  __builtin_amdgcn_s_barrier();
  STAMP(0, 5);
  if (bid < 240) {
    int ks = bid / 5, cb = bid - ks * 5;
    const char* p0 = smem + L_W2 + (4 * g + q4) * 256 + w * 32 + 8 * p4;
    bf8 f = trfrag(p0, p0 + 4096);
    int col = cb * 128 + w * 16, cg = col / 80, tt = (col - cg * 80) >> 4;
    *(bf8*)(ws + WS_W2 + ((size_t)(cg * 48 + ks) * 5 + tt) * 1024 + lane * 16) = f;
  }
  if (tid == 0) *(unsigned*)(ws + WS_FL + bid * 128) = 0u;
  f2 mr[2];
#pragma unroll
  for (int i = 0; i < 2; ++i) mr[i] = *(const f2*)(st + (wm * 32 + ((i ^ wn) << 4) + l15) * 2);
  auto csum4 = [&](int j) {
    const float* c = csl + wn * 112 + j * 16 + 4 * g;
    return (*(const f4*)c + *(const f4*)(c + 224)) + (*(const f4*)(c + 448) + *(const f4*)(c + 672));
  };
  const int rt0 = rb * 8 + wm * 2;
#pragma unroll
  for (int j = JQ; j < 7; j += 2) {
    f4 c0 = csum4(j), c1 = csum4(j + 1);
    int ks = (KV ? 1024 + 64 * ci + 32 * wn : 128 * ci + 64 * wn + 16 * (j - JQ)) >> 5;
#pragma unroll
    for (int i = 0; i < 2; ++i) {
      f4 y0 = (acc[i][j] - mr[i][0] * c0) * mr[i][1], y1 = (acc[i][j + 1] - mr[i][0] * c1) * mr[i][1];
      u4 o = {pk2(fmaxf(y0[0], 0.f), fmaxf(y0[1], 0.f)), pk2(fmaxf(y0[2], 0.f), fmaxf(y0[3], 0.f)),
              pk2(fmaxf(y1[0], 0.f), fmaxf(y1[1], 0.f)), pk2(fmaxf(y1[2], 0.f), fmaxf(y1[3], 0.f))};
      *(u4*)(ws + WS_HF + ((size_t)(rt0 + (i ^ wn)) * 48 + ks) * 1024 + lane * 16) = o;
    }
  }
  STAMP(0, 6);
  if (!KV) {
#pragma unroll
    for (int j = 0; j < 3; ++j) {
      if (j == 2 && wn) break;
      f4 c = csum4(j);
      int col = 80 * ci + 48 * wn + 16 * j;
#pragma unroll
      for (int i = 0; i < 2; ++i) {
        f4 y = (acc[i][j] - mr[i][0] * c) * mr[i][1];
        *(u2*)(ws + WS_PQ + ((size_t)(rt0 + (i ^ wn)) * 20 + (col >> 5)) * 1024 + ((col >> 4) & 1) * 8 + lane * 16) =
            u2{pk2(phi(y[0]), phi(y[1])), pk2(phi(y[2]), phi(y[3]))};
      }
    }
  } else {
    u4 fr[6];
#pragma unroll
    for (int i = 0; i < 2; ++i) {
      const float* sp = st + (wm * 32 + ((i ^ wn) << 4) + 4 * g) * 2;
      f4 s0 = *(const f4*)sp, s1 = *(const f4*)(sp + 4);
      f4 mean = {s0[0], s0[2], s1[0], s1[2]}, rstd = {s0[1], s0[3], s1[1], s1[3]};
#pragma unroll
      for (int j = 0; j < 5; ++j) {
        const float* c = csl + wn * 112 + j * 16 + l15;
        float cc = (c[0] + c[224]) + (c[448] + c[672]);
        f4 y = (acc[i][j] - mean * cc) * rstd;
        if (j < 3) {
#pragma unroll
          for (int e = 0; e < 4; ++e) y[e] = (j < 2 || l15 < 8) ? phi(y[e]) : y[e];
        }
        fr[j][2 * i] = pk2(y[0], y[1]);
        fr[j][2 * i + 1] = pk2(y[2], y[3]);
      }
    }
#pragma unroll
    for (int e = 0; e < 4; ++e) fr[5][e] = l15 < 8 ? 0x3f803f80u : fr[2][e];
    const int ta[8] = {0, 0, 1, 1, 1, 2, 2, 2}, tb[8] = {5, 3, 5, 3, 4, 5, 3, 4};
#pragma unroll
    for (int m = 0; m < 8; ++m) {
      f4 z = {0.f, 0.f, 0.f, 0.f};
      *(f4*)(smem + (w * 8 + m) * 1024 + lane * 16) = mfma(__builtin_bit_cast(bf8, fr[tb[m]]), __builtin_bit_cast(bf8, fr[ta[m]]), z);
    }
    WAITL0();
    __builtin_amdgcn_s_barrier();
    if (tid < 480) {
      int hl = tid / 120, rem = tid - hl * 120, d = rem / 6, grp = rem - d * 6;
      int hk = hl & 1, kc = 20 * hk + d, a = kc >> 4;
      int pos = grp < 5 ? 40 + 20 * hk + 4 * grp : 32, b = pos >> 4;
      int m = a == 0 ? b - 2 : (a == 1 ? b : b + 3);
      const char* p = smem + ((hl >> 1) * 8 + m) * 1024 + (((pos >> 2) & 3) * 16 + (kc & 15)) * 16;
      f4 v = (*(const f4*)p + *(const f4*)(p + 16384)) + (*(const f4*)(p + 32768) + *(const f4*)(p + 49152));
      *(f4*)(ws + WS_KV + ((size_t)(rb * 32 + ci * 4) * 120 + tid) * 16) = v;
    }
  }
  STAMP(0, 7);
}

__global__ __launch_bounds__(512, 2) void k1_kernel(const __bf16* __restrict__ x, const __bf16* __restrict__ wqkv,
                                                    const __bf16* __restrict__ w1, const __bf16* __restrict__ w2,
                                                    char* __restrict__ ws) {
  extern __shared__ __attribute__((aligned(16))) char smem[];
  const int bid = blockIdx.x, xcd = bid & 7, idx = bid >> 3;
  const int rb = (xcd >> 1) * 4 + (idx & 3), cgi = idx >> 2;
  const int ci = (xcd & 1) * 4 + (cgi >> 1);
  if (cgi & 1) k1_body<false>(x, wqkv, w1, w2, ws, smem, rb, ci, bid);
  else k1_body<true>(x, wqkv, w1, w2, ws, smem, rb, ci, bid);
}

template <int AUX>
DI bf8 ldfrag(__amdgpu_buffer_rsrc_t r, int voff, int soff) {
  return __builtin_bit_cast(bf8, __builtin_amdgcn_raw_buffer_load_b128(r, voff, soff, AUX));
}
DI void st8_sc1(char* p, unsigned a, unsigned b) {
  __hip_atomic_store((AS1 unsigned long long*)p, (unsigned long long)a | ((unsigned long long)b << 32), __ATOMIC_RELAXED,
                     __HIP_MEMORY_SCOPE_AGENT);
}
constexpr int L2_ACC = 0;
constexpr int L2_WO = 61440;
constexpr int L2_KV = L2_WO + 5120;
constexpr int L2_Z = L2_KV + 3200;
constexpr int L2_CNT = L2_Z + 640;
constexpr int K2_LDS = 98304;

DI void k2_map(int bid, int& r, int& c) {
  const int xcd = bid & 7, idx = bid >> 3;
  r = (xcd >> 1) * 8 + (idx & 7);
  c = (xcd & 1) * 4 + (idx >> 3);
}

#define RING 3
__global__ __launch_bounds__(256, 1) void k2_kernel(const __bf16* __restrict__ x, const __bf16* __restrict__ wo,
                                                    char* __restrict__ ws, __bf16* __restrict__ out) {
  extern __shared__ __attribute__((aligned(16))) char smem[];
  const int tid = threadIdx.x, lane = tid & 63, w = __builtin_amdgcn_readfirstlane(tid >> 6);
  const int g = lane >> 4, l15 = lane & 15;
  int r, c;
  k2_map(blockIdx.x, r, c);
  STAMP(1, 0);
  f4 kvv[8];
  float zv[8];
  {
    const int half = tid >= 100, it = tid - 100 * half, d = it / 5;
    const f4* p = (const f4*)(ws + WS_KV) + (half * 8 * 32 + r) * 120 + (tid < 200 ? it + d : 0);
#pragma unroll
    for (int i = 0; i < 8; ++i) kvv[i] = p[i * 32 * 120];
    const int t = tid < 96 ? 0 : tid - 96, hz = t >= 80, iz = t - 80 * hz;
    const float* q = (const float*)(ws + WS_KV) + (hz * 8 * 32 + 4 * c) * 480 + iz * 24 + 20;
#pragma unroll
    for (int i = 0; i < 8; ++i) zv[i] = q[i * 32 * 480];
  }
  u4 wv = {0u, 0u, 0u, 0u};
  if (tid < 200) { int row = tid / 10; wv = *(const u4*)(wo + (20 * r + row) * D_ + 80 * c + (tid - row * 10) * 8); }
  u2 pq[5];
  int goff[5];
#pragma unroll
  for (int m = 0; m < 5; ++m) {
    int k = 20 * (4 * c + w) + 4 * m;
    goff[m] = ((r * 4 + g) * 20 + (k >> 5)) * 1024 + (((k >> 2) & 3) * 16 + l15) * 16 + ((k >> 4) & 1) * 8;
    pq[m] = *(const u2*)(ws + WS_PQ + goff[m]);
  }
  const int orow = (r * 64 + w * 16 + l15) * D_ + c * 80 + 4 * g;
  u2 xr[5];
#pragma unroll
  for (int j = 0; j < 5; ++j) xr[j] = *(const u2*)(x + orow + 16 * j);
  __builtin_amdgcn_sched_barrier(0);
  const __amdgpu_buffer_rsrc_t rs = mkrsrc(ws, (unsigned)WS_END);
  const int vo = lane * 16;
  const int hA = (int)WS_HF + (r * 4 * 48 + w) * 1024, hB = (int)WS_W2 + (c * 48 + w) * 5120;
  const int aA = (int)WS_AF + (r * 4 * 20 + w) * 1024, aB = (int)WS_MF + (c * 20 + w) * 5120;
  bf8 fa[RING][4], fb[RING][5];
  auto ld = [&](int i) {
    const int sl = i % RING;
    if (i < 12) {
#pragma unroll
      for (int j = 0; j < 5; ++j) fb[sl][j] = ldfrag<0>(rs, vo, hB + i * 4 * 5120 + j * 1024);
#pragma unroll
      for (int j = 0; j < 4; ++j) fa[sl][j] = ldfrag<0>(rs, vo, hA + (i * 4 + j * 48) * 1024);
    } else {
#pragma unroll
      for (int j = 0; j < 5; ++j) fb[sl][j] = ldfrag<16>(rs, vo, aB + (i - 12) * 4 * 5120 + j * 1024);
#pragma unroll
      for (int j = 0; j < 4; ++j) fa[sl][j] = ldfrag<16>(rs, vo, aA + ((i - 12) * 4 + j * 20) * 1024);
    }
  };
#pragma unroll
  for (int i = 0; i < RING; ++i) ld(i);
  __builtin_amdgcn_sched_barrier(0);
  if (tid < 200) *(f4*)(smem + L2_KV + tid * 16) = ((kvv[0] + kvv[1]) + (kvv[2] + kvv[3])) + ((kvv[4] + kvv[5]) + (kvv[6] + kvv[7]));
  if (tid >= 96) *(float*)(smem + L2_Z + (tid - 96) * 4) = ((zv[0] + zv[1]) + (zv[2] + zv[3])) + ((zv[4] + zv[5]) + (zv[6] + zv[7]));
  *(u4*)(smem + L2_WO + tid * 16) = wv;
  if (tid < 64) *(u4*)(smem + L2_WO + 4096 + tid * 16) = u4{0u, 0u, 0u, 0u};
  if (tid == 0) *(unsigned*)(smem + L2_CNT) = 0u;
  WAITL0();
  __builtin_amdgcn_s_barrier();
  {
    const float* z0 = (const float*)(smem + L2_Z) + w * 20;
    float den = 0.f, p[20];
#pragma unroll
    for (int m = 0; m < 5; ++m) {
      f4 za = *(const f4*)(z0 + 4 * m), zb = *(const f4*)(z0 + 80 + 4 * m);
      p[4 * m] = lo16(pq[m][0]); p[4 * m + 1] = hi16(pq[m][0]); p[4 * m + 2] = lo16(pq[m][1]); p[4 * m + 3] = hi16(pq[m][1]);
#pragma unroll
      for (int e = 0; e < 4; ++e) den += p[4 * m + e] * bfr(za[e] + zb[e]);
    }
    float rden = __builtin_amdgcn_rcpf(bfr(bfr(den) + 1e-6f));
#pragma unroll
    for (int m = 0; m < 5; ++m)
      st8_sc1(ws + WS_AF + goff[m], pk2(p[4 * m] * rden, p[4 * m + 1] * rden), pk2(p[4 * m + 2] * rden, p[4 * m + 3] * rden));
  }
  {
    u4 au[2];
#pragma unroll
    for (int dt = 0; dt < 2; ++dt)
#pragma unroll
      for (int hh = 0; hh < 2; ++hh) {
        int d = dt * 16 + l15, grp = 2 * g + hh;
        bool ok = d < 20 && grp < 5;
        const char* p = smem + L2_KV + (ok ? d * 5 + grp : 0) * 16;
        f4 v = *(const f4*)p + *(const f4*)(p + 1600);
        au[dt][2 * hh] = ok ? pk2(v[0], v[1]) : 0u;
        au[dt][2 * hh + 1] = ok ? pk2(v[2], v[3]) : 0u;
      }
    const int k0 = 20 * r + 4 * g, k1 = 20 * r + 16;
    const char* p0 = smem + L2_WO + (8 * g + (l15 >> 2)) * 160 + (lane & 3) * 8;
    char* mf = ws + WS_MF + c * (20 * 5120) + l15 * 16;
    const int o0 = (k0 >> 5) * 5120 + ((k0 >> 2) & 3) * 256 + ((k0 >> 4) & 1) * 8;
    const int o1 = (k1 >> 5) * 5120 + ((k1 >> 2) & 3) * 256 + ((k1 >> 4) & 1) * 8;
    for (int t = w; t < 5; t += 4) {
      bf8 b = trfrag(p0 + t * 32, p0 + t * 32 + 640);
      f4 z = {0.f, 0.f, 0.f, 0.f};
      f4 m0 = mfma(__builtin_bit_cast(bf8, au[0]), b, z), m1 = mfma(__builtin_bit_cast(bf8, au[1]), b, z);
      st8_sc1(mf + t * 1024 + o0, pk2(m0[0], m0[1]), pk2(m0[2], m0[3]));
      if (g == 0) st8_sc1(mf + t * 1024 + o1, pk2(m1[0], m1[1]), pk2(m1[2], m1[3]));
    }
  }
  const AS1 unsigned* fl = (const AS1 unsigned*)(ws + WS_FL + (lane < 8 ? r * 8 + lane : (lane < 40 ? (lane - 8) * 8 + c : r * 8 + c)) * 128);
  STAMP(1, 1);
  __builtin_amdgcn_sched_barrier(0);
  f4 acc[4][5];
#pragma unroll
  for (int i = 0; i < 4; ++i)
#pragma unroll
    for (int j = 0; j < 5; ++j) acc[i][j] = f4{0.f, 0.f, 0.f, 0.f};
  unsigned flv = 0;
#pragma unroll
  for (int i = 0; i < 17; ++i) {
    const int sl = i % RING;
#pragma unroll
    for (int j = 0; j < 5; ++j)
#pragma unroll
      for (int m = 0; m < 4; ++m) acc[m][j] = mfma(fb[sl][j], fa[sl][m], acc[m][j]);
    if (i == 2) {
      WAITV(18);
      unsigned old = 0;
      if (lane == 0) old = __hip_atomic_fetch_add((AS3 unsigned*)(smem + L2_CNT), 1u, __ATOMIC_RELAXED, __HIP_MEMORY_SCOPE_WORKGROUP);
      if (lane == 0 && old == 3u)
        __hip_atomic_store((AS1 unsigned*)(ws + WS_FL + (r * 8 + c) * 128), 1u, __ATOMIC_RELAXED, __HIP_MEMORY_SCOPE_AGENT);
    }
    if (i + RING == 11) flv = __hip_atomic_load(fl, __ATOMIC_RELAXED, __HIP_MEMORY_SCOPE_AGENT);
    if (i + RING == 12) {
      for (unsigned spin = 0; !__all(flv == 1u) && spin < (1u << 22); ++spin) {
        __builtin_amdgcn_s_sleep(1);
        flv = __hip_atomic_load(fl, __ATOMIC_RELAXED, __HIP_MEMORY_SCOPE_AGENT);
      }
      __builtin_amdgcn_fence(__ATOMIC_ACQUIRE, "wavefront");
    }
    if (i + RING < 17) ld(i + RING);
    __builtin_amdgcn_sched_barrier(0);
  }
  STAMP(1, 6);
#pragma unroll
  for (int m = 0; m < 4; ++m) {
    if (m != w) {
      int slot = m < w ? m : m - 1;
#pragma unroll
      for (int j = 0; j < 5; ++j) *(f4*)(smem + L2_ACC + ((w * 3 + slot) * 5 + j) * 1024 + lane * 16) = acc[m][j];
    }
  }
  WAITL0();
  __builtin_amdgcn_s_barrier();
  f4 own[5];
#pragma unroll
  for (int j = 0; j < 5; ++j) own[j] = w == 0 ? acc[0][j] : (w == 1 ? acc[1][j] : (w == 2 ? acc[2][j] : acc[3][j]));
  f4 tot[5];
#pragma unroll
  for (int j = 0; j < 5; ++j) tot[j] = f4{lo16(xr[j][0]), hi16(xr[j][0]), lo16(xr[j][1]), hi16(xr[j][1])};
#pragma unroll
  for (int s = 0; s < 4; ++s) {
    int slot = w < s ? w : w - 1;
#pragma unroll
    for (int j = 0; j < 5; ++j) {
      f4 v = own[j];
      if (s != w) v = *(const f4*)(smem + L2_ACC + ((s * 3 + slot) * 5 + j) * 1024 + lane * 16);
      tot[j] += v;
    }
  }
#pragma unroll
  for (int j = 0; j < 5; ++j) *(u2*)(out + orow + 16 * j) = u2{pk2(tot[j][0], tot[j][1]), pk2(tot[j][2], tot[j][3])};
  STAMP(1, 7);
}

extern "C" void kernel_launch(void* const* d_in, const int* in_sizes, int n_in, void* d_out, int out_size, void* d_ws,
                              size_t ws_size, hipStream_t stream) {
  const __bf16* x = (const __bf16*)d_in[0];
  const __bf16* wqkv = (const __bf16*)d_in[1];
  const __bf16* wo = (const __bf16*)d_in[2];
  const __bf16* w1 = (const __bf16*)d_in[4];
  const __bf16* w2 = (const __bf16*)d_in[5];
  char* ws = (char*)d_ws;
  (void)hipFuncSetAttribute((const void*)k1_kernel, hipFuncAttributeMaxDynamicSharedMemorySize, K1_LDS);
  (void)hipFuncSetAttribute((const void*)k2_kernel, hipFuncAttributeMaxDynamicSharedMemorySize, K2_LDS);
  k1_kernel<<<256, 512, K1_LDS, stream>>>(x, wqkv, w1, w2, ws);
  k2_kernel<<<256, 256, K2_LDS, stream>>>(x, wo, ws, (__bf16*)d_out);
}
```

```cpp
#include <hip/hip_runtime.h>
#include <hip/hip_bf16.h>
#include <stdint.h>

typedef __attribute__((ext_vector_type(8))) __bf16 bf8;
typedef __attribute__((ext_vector_type(4))) __bf16 bf4;
typedef __attribute__((ext_vector_type(2))) __bf16 bf2;
typedef __attribute__((ext_vector_type(4))) short s4;
typedef __attribute__((ext_vector_type(4))) float f4;
typedef __attribute__((ext_vector_type(2))) float f2;
typedef __attribute__((ext_vector_type(4))) unsigned u4;
typedef __attribute__((ext_vector_type(2))) unsigned u2;
#define AS1 __attribute__((address_space(1)))
#define AS3 __attribute__((address_space(3)))
#define DI __device__ __forceinline__

constexpr int S_ = 2048, D_ = 640, DFF = 1536, N3 = 1920;
constexpr size_t WS_HF = 0;
constexpr size_t WS_PQ = WS_HF + (size_t)128 * 48 * 1024;
constexpr size_t WS_AF = WS_PQ + (size_t)128 * 20 * 1024;
constexpr size_t WS_W2 = WS_AF + (size_t)128 * 20 * 1024;
constexpr size_t WS_MF = WS_W2 + (size_t)8 * 48 * 5 * 1024;
constexpr size_t WS_KV = WS_MF + (size_t)8 * 20 * 5 * 1024;
constexpr size_t WS_Z = WS_KV + (size_t)16 * 32 * 400 * 4;
constexpr size_t WS_FL = WS_Z + (size_t)16 * 640 * 4;
constexpr size_t WS_END = WS_FL + 256 * 128;
#define STAMPW(k, i) do {} while (0)
#define STAMP(k, i) do {} while (0)

DI float bfr(float v) { return (float)(__bf16)v; }
DI unsigned pk2(float a, float b) { f2 v = {a, b}; return __builtin_bit_cast(unsigned, __builtin_convertvector(v, bf2)); }
DI float lo16(unsigned u) { return __builtin_bit_cast(float, u << 16); }
DI float hi16(unsigned u) { return __builtin_bit_cast(float, u & 0xffff0000u); }
DI f4 mfma(bf8 a, bf8 b, f4 c) { return __builtin_amdgcn_mfma_f32_16x16x32_bf16(a, b, c, 0, 0, 0); }
DI void glds16(const void* g, char* l) {
  __builtin_amdgcn_global_load_lds((const AS1 void*)g, (AS3 void*)l, 16, 0, 0);
}
DI bf8 trfrag(const char* p0, const char* p1) {
  s4 a = __builtin_amdgcn_ds_read_tr16_b64_v4i16((AS3 s4*)p0);
  s4 b = __builtin_amdgcn_ds_read_tr16_b64_v4i16((AS3 s4*)p1);
  return __builtin_bit_cast(bf8, __builtin_shufflevector(a, b, 0, 1, 2, 3, 4, 5, 6, 7));
}
DI bf8 trfrag3(const AS3 char* p0, const AS3 char* p1) {
  s4 a = __builtin_amdgcn_ds_read_tr16_b64_v4i16((AS3 s4*)p0);
  s4 b = __builtin_amdgcn_ds_read_tr16_b64_v4i16((AS3 s4*)p1);
  return __builtin_bit_cast(bf8, __builtin_shufflevector(a, b, 0, 1, 2, 3, 4, 5, 6, 7));
}
DI float phi(float t) {
  return fmaxf(t, 0.f) + 1e-6f + __builtin_amdgcn_exp2f(fminf(t, 0.f) * 1.44269504088896341f);
}
#define MT(i) do {} while (0)
#define WAITV(n) asm volatile("s_waitcnt vmcnt(" #n ")" ::: "memory")
#define WAITL0() asm volatile("s_waitcnt lgkmcnt(0)" ::: "memory")

DI __amdgpu_buffer_rsrc_t mkrsrc(const void* p, unsigned bytes) {
  return __builtin_amdgcn_make_buffer_rsrc((void*)p, 0, bytes, 0x00020000);
}
DI void blds16(__amdgpu_buffer_rsrc_t r, char* l, int voff, int soff) {
  __builtin_amdgcn_raw_ptr_buffer_load_lds(r, (AS3 void*)l, 16, voff, soff, 0, 0);
}
constexpr int A_BYTES = 128 * 128;
constexpr int L_Q = 3 * A_BYTES;
constexpr int L_W2 = L_Q + 3 * 64 * 448;
constexpr int L_DUM = L_W2 + 8192;
constexpr int L_ST = L_DUM + 1024;
constexpr int L_CS = L_ST + 1024;
constexpr int K1_LDS = L_CS + 4 * 224 * 4;

template <bool KV>
DI void k1_body(const __bf16* __restrict__ x, const __bf16* __restrict__ wqkv, const __bf16* __restrict__ w1,
                const __bf16* __restrict__ w2, char* __restrict__ ws, char* smem, int rb, int ci, int bid) {
  constexpr int NQ = KV ? 20 : 12, NH = KV ? 8 : 16;
  constexpr int QB = 64 * NQ * 16, HB = 64 * NH * 16;
  constexpr int L_H = L_Q + 3 * QB;
  const int tid = threadIdx.x, lane = tid & 63;
  const int w = __builtin_amdgcn_readfirstlane(tid >> 6), wm = w >> 1, wn = w & 1;
  const int g = lane >> 4, l15 = lane & 15;
  STAMP(0, 0);
  int voA[2], voB[4];
#pragma unroll
  for (int n = 0; n < 2; ++n) {
    int row = (w * 2 + n) * 8 + (lane >> 3);
    voA[n] = (rb * 128 + row) * (D_ * 2) + (((lane & 7) ^ ((row >> 1) & 7)) << 4);
  }
  const int ib0 = w < 4 ? w * 4 : 16 + (w - 4) * 3;
#pragma unroll
  for (int n = 0; n < 4; ++n) {
    int ib = ib0 + n;
    if (ib < NQ) {
      int c = ib * 64 + lane, k = c / NQ, ch = c - k * NQ;
      ch ^= ((k >> 3) & 1) << 1;
      int col;
      if (KV) { int cw = ch >= 10, cc = ch - 10 * cw, v = cc >= 5; col = 640 + 640 * v + 80 * ci + 40 * cw + 8 * (cc - 5 * v); }
      else col = 80 * ci + 8 * (ch >= 10 ? ch - 2 : ch);
      voB[n] = (k * N3 + col) * 2;
    } else {
      int c = (ib - NQ) * 64 + lane, k = c / NH, ch = c - k * NH;
      int f = KV ? (((k >> 1) & 1) | ((k >> 2) & 2)) : ((k & 3) | ((k >> 1) & 4));
      ch ^= f << 1;
      voB[n] = (k * DFF + (KV ? 1024 + 64 * ci : 128 * ci) + 8 * ch) * 2;
    }
  }
  const void* pB[4];
  int nB[4], dB[4], zB[4], kB[4];
#pragma unroll
  for (int n = 0; n < 4; ++n) {
    const int ib = ib0 + n;
    const bool isq = ib < NQ, dummy = n == 3 && w >= 4;
    pB[n] = isq ? (const void*)wqkv : (const void*)w1;
    nB[n] = dummy ? 0 : (isq ? D_ * N3 * 2 : D_ * DFF * 2);
    dB[n] = dummy ? L_DUM : (isq ? L_Q + ib * 1024 : L_H + (ib - NQ) * 1024);
    zB[n] = dummy ? 0 : (isq ? QB : HB);
    kB[n] = isq ? 64 * N3 * 2 : 64 * DFF * 2;
  }
  auto ldslot = [&](int n, int stg, int t) {
    const bool on = t < 10;
    if (n < 2) blds16(mkrsrc(x, on ? S_ * D_ * 2 : 0), smem + stg * A_BYTES + w * 2048 + n * 1024, voA[n], t * 128);
    else blds16(mkrsrc(pB[n - 2], on ? nB[n - 2] : 0), smem + dB[n - 2] + stg * zB[n - 2], voB[n - 2], t * kB[n - 2]);
  };
#pragma unroll
  for (int n = 0; n < 6; ++n) ldslot(n, 0, 0);
#pragma unroll
  for (int n = 0; n < 6; ++n) ldslot(n, 1, 1);
  {
    int e = bid < 240 ? bid : 0;
    int ks = e / 5, cb = e - ks * 5;
    blds16(mkrsrc(w2, DFF * D_ * 2), smem + L_W2 + w * 1024, ((ks * 32 + w * 4 + g) * D_ + cb * 128 + l15 * 8) * 2, 0);
  }
  const int ks0 = wm & 1;
  const int q4 = l15 >> 2, p4 = lane & 3;
  constexpr int JQ = KV ? 5 : 3;
  unsigned ra[2][2], rb_[2][7];
  {
    const unsigned lb = (unsigned)(size_t)(AS3 char*)smem;
    const int f = KV ? (((q4 >> 1) & 1) | ((g & 1) << 1)) : (q4 | ((g & 1) << 2));
#pragma unroll
    for (int s = 0; s < 2; ++s) {
      const int sk = s ^ ks0;
#pragma unroll
      for (int i = 0; i < 2; ++i) ra[s][i] = lb + (wm * 32 + ((i ^ wn) << 4) + l15) * 128 + (((sk * 4 + g) ^ (l15 >> 1)) << 4);
#pragma unroll
      for (int j = 0; j < 7; ++j)
        rb_[s][j] = lb + 8 * p4 +
                    (j < JQ ? L_Q + (32 * sk + 8 * g + q4) * (NQ * 16) + (((wn * JQ + j) ^ (g & 1)) << 5)
                            : L_H + (32 * sk + 8 * g + q4) * (NH * 16) + (((wn * (7 - JQ) + j - JQ) ^ f) << 5));
#pragma unroll
      for (int i = 0; i < 2; ++i) asm volatile("" : "+v"(ra[s][i]));
#pragma unroll
      for (int j = 0; j < 7; ++j) asm volatile("" : "+v"(rb_[s][j]));
    }
  }
  const f4 zero4 = {0.f, 0.f, 0.f, 0.f};
  bf8 ones = __builtin_bit_cast(bf8, u4{0x3f803f80u, 0x3f803f80u, 0x3f803f80u, 0x3f803f80u});
  asm volatile("" : "+v"(ones));
  f4 acc[2][7], cs[7], rs = zero4, rq2 = zero4;
#pragma unroll
  for (int j = 0; j < 7; ++j) acc[0][j] = acc[1][j] = cs[j] = zero4;
  bf8 fa[2][2], fb[2][7];
  auto rd = [&](int s, int stg, int sn, int tn) {
#pragma unroll
    for (int i = 0; i < 2; ++i) fa[s][i] = *(const AS3 bf8*)(size_t)(ra[s][i] + stg * A_BYTES);
#pragma unroll
    for (int j = 0; j < 7; ++j) {
      const AS3 char* p = (const AS3 char*)(size_t)rb_[s][j] + stg * (j < JQ ? QB : HB);
      fb[s][j] = trfrag3(p, p + 4 * (j < JQ ? NQ : NH) * 16);
      if (j == 0 || j == 3 || j == 5) ldslot(3 * s + (j + 1) / 2 - (j > 0), sn, tn);
    }
  };
  auto mm = [&](int s) {
#pragma unroll
    for (int j = 0; j < 7; ++j)
#pragma unroll
      for (int i = 0; i < 2; ++i)
        acc[i][j] = (KV && j < 5) ? mfma(fa[s][i], fb[s][j], acc[i][j]) : mfma(fb[s][j], fa[s][i], acc[i][j]);
    rs = mfma(ones, fa[s][0], rs);
    rq2 = mfma(fa[s][0], fa[s][0], rq2);
  };
  auto interleave = [&]() {
#pragma unroll
    for (int i = 0; i < 16; ++i) {
      __builtin_amdgcn_sched_group_barrier(0x008, 1, 0);
      __builtin_amdgcn_sched_group_barrier(0x100, 1, 0);
      if (i == 3 || i == 9 || i == 13) __builtin_amdgcn_sched_group_barrier(0x020, 1, 0);
    }
  };
  auto body = [&](int t, int stg, bool first) {
    if (first) WAITV(7); else WAITV(6);
    __builtin_amdgcn_s_barrier();
    MT(0);
    const int sn = stg == 0 ? 2 : stg - 1;
    __builtin_amdgcn_sched_barrier(0);
    rd(0, stg, sn, t + 2);
    if (!first) { mm(1); interleave(); }
    __builtin_amdgcn_sched_barrier(0);
    MT(2);
    rd(1, stg, sn, t + 2);
    mm(0);
    interleave();
    __builtin_amdgcn_sched_barrier(0);
    MT(3);
    if ((t & 1) == (wm >> 1)) {
#pragma unroll
      for (int j = 0; j < 7; ++j) cs[j] = mfma(fb[0][j], ones, cs[j]);
    }
    WAITL0();
    MT(4);
  };
  body(0, 0, true);
#pragma unroll 1
  for (int t = 1; t < 10; t += 3) {
    body(t, 1, false);
    body(t + 1, 2, false);
    body(t + 2, 0, false);
  }
  __builtin_amdgcn_sched_barrier(0);
  mm(1);
  STAMP(0, 4);
  float* st = (float*)(smem + L_ST);
  float* csl = (float*)(smem + L_CS);
  {
    float sq = p4 == 0 ? rq2[0] : (p4 == 1 ? rq2[1] : (p4 == 2 ? rq2[2] : rq2[3]));
    float mean = rs[0] * (1.f / 640.f);
    float var = fmaxf(sq * (1.f / 640.f) - mean * mean, 0.f);
    float sd = bfr(__builtin_amdgcn_sqrtf(bfr(bfr(var) + 1e-5f)));
    if (q4 == g) *(f2*)(st + (wm * 32 + wn * 16 + l15) * 2) = f2{bfr(mean), __builtin_amdgcn_rcpf(sd)};
  }
  {
    f4 c = cs[0];
#pragma unroll
    for (int j = 1; j < 7; ++j) c = l15 == j ? cs[j] : c;
    if (l15 < 7) *(f4*)(csl + wm * 224 + wn * 112 + l15 * 16 + 4 * g) = c;
  }
  WAITL0();
  __builtin_amdgcn_s_barrier();
  STAMP(0, 5);
  if (bid < 240) {
    int ks = bid / 5, cb = bid - ks * 5;
    const char* p0 = smem + L_W2 + (4 * g + q4) * 256 + w * 32 + 8 * p4;
    bf8 f = trfrag(p0, p0 + 4096);
    int col = cb * 128 + w * 16, cg = col / 80, tt = (col - cg * 80) >> 4;
    *(bf8*)(ws + WS_W2 + ((size_t)(cg * 48 + ks) * 5 + tt) * 1024 + lane * 16) = f;
  }
  if (tid == 0) *(unsigned*)(ws + WS_FL + bid * 128) = 0u;
  f2 mr[2];
#pragma unroll
  for (int i = 0; i < 2; ++i) mr[i] = *(const f2*)(st + (wm * 32 + ((i ^ wn) << 4) + l15) * 2);
  auto csum4 = [&](int j) {
    const float* c = csl + wn * 112 + j * 16 + 4 * g;
    return (*(const f4*)c + *(const f4*)(c + 224)) + (*(const f4*)(c + 448) + *(const f4*)(c + 672));
  };
  const int rt0 = rb * 8 + wm * 2;
#pragma unroll
  for (int j = JQ; j < 7; j += 2) {
    f4 c0 = csum4(j), c1 = csum4(j + 1);
    int ks = (KV ? 1024 + 64 * ci + 32 * wn : 128 * ci + 64 * wn + 16 * (j - JQ)) >> 5;
#pragma unroll
    for (int i = 0; i < 2; ++i) {
      f4 y0 = (acc[i][j] - mr[i][0] * c0) * mr[i][1], y1 = (acc[i][j + 1] - mr[i][0] * c1) * mr[i][1];
      u4 o = {pk2(fmaxf(y0[0], 0.f), fmaxf(y0[1], 0.f)), pk2(fmaxf(y0[2], 0.f), fmaxf(y0[3], 0.f)),
              pk2(fmaxf(y1[0], 0.f), fmaxf(y1[1], 0.f)), pk2(fmaxf(y1[2], 0.f), fmaxf(y1[3], 0.f))};
      *(u4*)(ws + WS_HF + ((size_t)(rt0 + (i ^ wn)) * 48 + ks) * 1024 + lane * 16) = o;
    }
  }
  STAMP(0, 6);
  if (!KV) {
#pragma unroll
    for (int j = 0; j < 3; ++j) {
      if (j == 2 && wn) break;
      f4 c = csum4(j);
      int col = 80 * ci + 48 * wn + 16 * j;
#pragma unroll
      for (int i = 0; i < 2; ++i) {
        f4 y = (acc[i][j] - mr[i][0] * c) * mr[i][1];
        *(u2*)(ws + WS_PQ + ((size_t)(rt0 + (i ^ wn)) * 20 + (col >> 5)) * 1024 + ((col >> 4) & 1) * 8 + lane * 16) =
            u2{pk2(phi(y[0]), phi(y[1])), pk2(phi(y[2]), phi(y[3]))};
      }
    }
  } else {
    u4 fr[6];
#pragma unroll
    for (int i = 0; i < 2; ++i) {
      const float* sp = st + (wm * 32 + ((i ^ wn) << 4) + 4 * g) * 2;
      f4 s0 = *(const f4*)sp, s1 = *(const f4*)(sp + 4);
      f4 mean = {s0[0], s0[2], s1[0], s1[2]}, rstd = {s0[1], s0[3], s1[1], s1[3]};
#pragma unroll
      for (int j = 0; j < 5; ++j) {
        const float* c = csl + wn * 112 + j * 16 + l15;
        float cc = (c[0] + c[224]) + (c[448] + c[672]);
        f4 y = (acc[i][j] - mean * cc) * rstd;
        if (j < 3) {
#pragma unroll
          for (int e = 0; e < 4; ++e) y[e] = (j < 2 || l15 < 8) ? phi(y[e]) : y[e];
        }
        fr[j][2 * i] = pk2(y[0], y[1]);
        fr[j][2 * i + 1] = pk2(y[2], y[3]);
      }
    }
#pragma unroll
    for (int e = 0; e < 4; ++e) fr[5][e] = l15 < 8 ? 0x3f803f80u : fr[2][e];
    const int ta[8] = {0, 0, 1, 1, 1, 2, 2, 2}, tb[8] = {5, 3, 5, 3, 4, 5, 3, 4};
#pragma unroll
    for (int m = 0; m < 8; ++m) {
      f4 z = {0.f, 0.f, 0.f, 0.f};
      *(f4*)(smem + (w * 8 + m) * 1024 + lane * 16) = mfma(__builtin_bit_cast(bf8, fr[tb[m]]), __builtin_bit_cast(bf8, fr[ta[m]]), z);
    }
    WAITL0();
    __builtin_amdgcn_s_barrier();
    if (tid < 480) {
      const bool isz = tid >= 400;
      int hl = isz ? (tid - 400) / 20 : tid / 100, rem = isz ? (tid - 400 - hl * 20) * 5 : tid - hl * 100, d = rem / 5, grp = rem - d * 5;
      int hk = hl & 1, kc = 20 * hk + d, a = kc >> 4;
      int pos = isz ? 32 : 40 + 20 * hk + 4 * grp, b = pos >> 4;
      int m = a == 0 ? b - 2 : (a == 1 ? b : b + 3);
      const char* p = smem + ((hl >> 1) * 8 + m) * 1024 + (((pos >> 2) & 3) * 16 + (kc & 15)) * 16;
      f4 v = (*(const f4*)p + *(const f4*)(p + 16384)) + (*(const f4*)(p + 32768) + *(const f4*)(p + 49152));
      if (isz) *(float*)(ws + WS_Z + (rb * 640 + ci * 80 + tid - 400) * 4) = v[0];
      else *(f4*)(ws + WS_KV + ((size_t)(rb * 32 + ci * 4) * 100 + tid) * 16) = v;
    }
  }
  STAMP(0, 7);
}

__global__ __launch_bounds__(512, 2) void k1_kernel(const __bf16* __restrict__ x, const __bf16* __restrict__ wqkv,
                                                    const __bf16* __restrict__ w1, const __bf16* __restrict__ w2,
                                                    char* __restrict__ ws) {
  extern __shared__ __attribute__((aligned(16))) char smem[];
  const int bid = blockIdx.x, xcd = bid & 7, idx = bid >> 3;
  const int rb = (xcd >> 1) * 4 + (idx & 3), cgi = idx >> 2;
  const int ci = (xcd & 1) * 4 + (cgi >> 1);
  if (cgi & 1) k1_body<false>(x, wqkv, w1, w2, ws, smem, rb, ci, bid);
  else k1_body<true>(x, wqkv, w1, w2, ws, smem, rb, ci, bid);
}

template <int AUX>
DI bf8 ldfrag(__amdgpu_buffer_rsrc_t r, int voff, int soff) {
  return __builtin_bit_cast(bf8, __builtin_amdgcn_raw_buffer_load_b128(r, voff, soff, AUX));
}
DI void st8_sc1(char* p, unsigned a, unsigned b) {
  __hip_atomic_store((AS1 unsigned long long*)p, (unsigned long long)a | ((unsigned long long)b << 32), __ATOMIC_RELAXED,
                     __HIP_MEMORY_SCOPE_AGENT);
}
constexpr int L2_ACC = 0;
constexpr int L2_WO = 61440;
constexpr int L2_KV = L2_WO + 5120;
constexpr int L2_Z = L2_KV + 2048;
constexpr int L2_CNT = L2_Z + 2048;
constexpr int K2_LDS = 98304;

#define RING 3
DI void k2_publish(char* ws, char* smem, int flag, int lane) {
  WAITV(0);
  if (lane == 0) {
    unsigned old = __hip_atomic_fetch_add((AS3 unsigned*)(smem + L2_CNT), 1u, __ATOMIC_RELAXED, __HIP_MEMORY_SCOPE_WORKGROUP);
    if (old == 2u) __hip_atomic_store((AS1 unsigned*)(ws + WS_FL + flag * 128), 1u, __ATOMIC_RELAXED, __HIP_MEMORY_SCOPE_AGENT);
  }
}
__global__ __launch_bounds__(512, 2) void k2_kernel(const __bf16* __restrict__ x, const __bf16* __restrict__ wo,
                                                    char* __restrict__ ws, __bf16* __restrict__ out) {
  extern __shared__ __attribute__((aligned(16))) char smem[];
  const int tid = threadIdx.x, lane = tid & 63, w = __builtin_amdgcn_readfirstlane(tid >> 6);
  const int g = lane >> 4, l15 = lane & 15;
  const int xcd = blockIdx.x & 7, idx = blockIdx.x >> 3;
  const int r = (xcd >> 1) * 8 + (idx & 7), c = (xcd & 1) * 4 + (idx >> 3);
  STAMP(1, 0);
  if (tid == 0) *(unsigned*)(smem + L2_CNT) = 0u;
  WAITL0();
  __builtin_amdgcn_s_barrier();
  const __amdgpu_buffer_rsrc_t rs = mkrsrc(ws, (unsigned)WS_END);
  const int vo = lane * 16;
  const f4 zero4 = {0.f, 0.f, 0.f, 0.f};
  if (w >= 4) __builtin_amdgcn_s_setprio(3);
  if (w == 4) {
    const f4* p = (const f4*)(ws + WS_KV) + r * 100;
    const int i1 = lane < 36 ? lane + 64 : lane;
    f4 v0[16], v1[16];
#pragma unroll
    for (int i = 0; i < 16; ++i) { v0[i] = p[i * 3200 + lane]; v1[i] = p[i * 3200 + i1]; }
    u4 wv[4];
#pragma unroll
    for (int i = 0; i < 4; ++i) {
      int ch = i * 64 + lane, row = ch / 10;
      wv[i] = u4{0u, 0u, 0u, 0u};
      if (ch < 200) wv[i] = *(const u4*)(wo + (20 * r + row) * D_ + 80 * c + (ch - row * 10) * 8);
    }
#pragma unroll
    for (int st = 1; st < 16; st *= 2)
#pragma unroll
      for (int i = 0; i < 16; i += 2 * st) { v0[i] += v0[i + st]; v1[i] += v1[i + st]; }
    *(f4*)(smem + L2_KV + vo) = v0[0];
    *(f4*)(smem + L2_KV + 1024 + vo) = v1[0];
#pragma unroll
    for (int i = 0; i < 4; ++i) *(u4*)(smem + L2_WO + i * 1024 + vo) = wv[i];
    *(u4*)(smem + L2_WO + 4096 + vo) = u4{0u, 0u, 0u, 0u};
    u4 au[2];
#pragma unroll
    for (int dt = 0; dt < 2; ++dt)
#pragma unroll
      for (int hh = 0; hh < 2; ++hh) {
        int d = dt * 16 + l15, grp = 2 * g + hh;
        bool ok = d < 20 && grp < 5;
        f4 v = *(const f4*)(smem + L2_KV + (ok ? d * 5 + grp : 0) * 16);
        au[dt][2 * hh] = ok ? pk2(v[0], v[1]) : 0u;
        au[dt][2 * hh + 1] = ok ? pk2(v[2], v[3]) : 0u;
      }
    const int k0 = 20 * r + 4 * g, k1 = 20 * r + 16;
    const char* p0 = smem + L2_WO + (8 * g + (l15 >> 2)) * 160 + (lane & 3) * 8;
    char* mf = ws + WS_MF + c * (20 * 5120) + l15 * 16;
    const int o0 = (k0 >> 5) * 5120 + ((k0 >> 2) & 3) * 256 + ((k0 >> 4) & 1) * 8;
    const int o1 = (k1 >> 5) * 5120 + ((k1 >> 2) & 3) * 256 + ((k1 >> 4) & 1) * 8;
#pragma unroll
    for (int t = 0; t < 5; ++t) {
      bf8 b = trfrag(p0 + t * 32, p0 + t * 32 + 640);
      f4 m0 = mfma(__builtin_bit_cast(bf8, au[0]), b, zero4), m1 = mfma(__builtin_bit_cast(bf8, au[1]), b, zero4);
      st8_sc1(mf + t * 1024 + o0, pk2(m0[0], m0[1]), pk2(m0[2], m0[3]));
      if (g == 0) st8_sc1(mf + t * 1024 + o1, pk2(m1[0], m1[1]), pk2(m1[2], m1[3]));
    }
    k2_publish(ws, smem, r * 8 + c, lane);
    STAMPW(1, 8);
  } else if (w == 5 || w == 6) {
    const int fo = ((r * 4 + (c & 1) * 2 + w - 5) * 20 + (c >> 1) * 5) * 1024;
    const f4* zp = (const f4*)(ws + WS_Z) + (c >> 1) * 40 + (lane < 40 ? lane : 0);
    f4 zv[16];
#pragma unroll
    for (int i = 0; i < 16; ++i) zv[i] = zp[i * 160];
    u4 pq[5];
#pragma unroll
    for (int k = 0; k < 5; ++k) pq[k] = __builtin_bit_cast(u4, ldfrag<0>(rs, vo, (int)WS_PQ + fo + k * 1024));
#pragma unroll
    for (int st = 1; st < 16; st *= 2)
#pragma unroll
      for (int i = 0; i < 16; i += 2 * st) zv[i] += zv[i + st];
    char* zl = smem + L2_Z + (w - 5) * 1024;
    if (lane < 40) *(u2*)(zl + lane * 8) = u2{pk2(zv[0][0], zv[0][1]), pk2(zv[0][2], zv[0][3])};
    f4 den = zero4;
#pragma unroll
    for (int k = 0; k < 5; ++k) {
      u4 zf;
#pragma unroll
      for (int p = 0; p < 2; ++p) {
        int gi = 8 * k + 4 * p + g;
        u2 z2 = *(const u2*)(zl + gi * 8);
        bool mine = gi / 5 == l15;
        zf[2 * p] = mine ? z2[0] : 0u;
        zf[2 * p + 1] = mine ? z2[1] : 0u;
      }
      den = mfma(__builtin_bit_cast(bf8, zf), __builtin_bit_cast(bf8, pq[k]), den);
    }
    f4 rd;
#pragma unroll
    for (int e = 0; e < 4; ++e) rd[e] = __builtin_amdgcn_rcpf(bfr(bfr(den[e]) + 1e-6f));
    if (g < 2) *(f4*)(zl + 512 + l15 * 32 + g * 16) = rd;
#pragma unroll
    for (int k = 0; k < 5; ++k) {
      u4 o;
#pragma unroll
      for (int p = 0; p < 2; ++p) {
        float s = *(const float*)(zl + 512 + l15 * 32 + ((8 * k + 4 * p + g) / 5) * 4);
        o[2 * p] = pk2(lo16(pq[k][2 * p]) * s, hi16(pq[k][2 * p]) * s);
        o[2 * p + 1] = pk2(lo16(pq[k][2 * p + 1]) * s, hi16(pq[k][2 * p + 1]) * s);
      }
      __builtin_amdgcn_raw_buffer_store_b128(o, rs, vo, (int)WS_AF + fo + k * 1024, 16);
    }
    k2_publish(ws, smem, r * 8 + c, lane);
    STAMPW(1, 9);
  }
  if (w >= 4) {
    __builtin_amdgcn_s_barrier();
    return;
  }
  const int hA = (int)WS_HF + (r * 4 * 48 + w) * 1024, hB = (int)WS_W2 + (c * 48 + w) * 5120;
  const int aA = (int)WS_AF + (r * 4 * 20 + w) * 1024, aB = (int)WS_MF + (c * 20 + w) * 5120;
#define ROT 0
  const int rot = ROT ? ((idx >> 3) * 3 + (idx & 7) * 5) % 12 : 0, rot2 = ROT ? idx % 5 : 0;
  bf8 fa[RING][4], fb[RING][5];
  auto ld = [&](int i) {
    const int sl = i % RING;
    if (i < 12) {
      int ii = i + rot;
      ii = ii >= 12 ? ii - 12 : ii;
#pragma unroll
      for (int j = 0; j < 5; ++j) fb[sl][j] = ldfrag<0>(rs, vo + j * 1024, hB + ii * 20480);
#pragma unroll
      for (int j = 0; j < 4; ++j) fa[sl][j] = ldfrag<0>(rs, vo, hA + ii * 4096 + j * 49152);
    } else {
      int ii = i - 12 + rot2;
      ii = ii >= 5 ? ii - 5 : ii;
#pragma unroll
      for (int j = 0; j < 5; ++j) fb[sl][j] = ldfrag<16>(rs, vo + j * 1024, aB + ii * 20480);
#pragma unroll
      for (int j = 0; j < 4; ++j) fa[sl][j] = ldfrag<16>(rs, vo, aA + ii * 4096 + j * 20480);
    }
  };
#pragma unroll
  for (int i = 0; i < RING; ++i) ld(i);
  const int orow = (r * 64 + w * 16 + l15) * D_ + c * 80 + 4 * g;
  u2 xr[5];
#pragma unroll
  for (int j = 0; j < 5; ++j) xr[j] = *(const u2*)(x + orow + 16 * j);
  const AS1 unsigned* fl = (const AS1 unsigned*)(ws + WS_FL + (lane < 8 ? r * 8 + lane : (lane < 40 ? (lane - 8) * 8 + c : r * 8 + c)) * 128);
  STAMP(1, 1);
  __builtin_amdgcn_sched_barrier(0);
  f4 acc[4][5];
#pragma unroll
  for (int i = 0; i < 4; ++i)
#pragma unroll
    for (int j = 0; j < 5; ++j) acc[i][j] = zero4;
  unsigned flv = 0;
#pragma unroll
  for (int i = 0; i < 17; ++i) {
    const int sl = i % RING;
#pragma unroll
    for (int j = 0; j < 5; ++j)
#pragma unroll
      for (int m = 0; m < 4; ++m) acc[m][j] = mfma(fb[sl][j], fa[sl][m], acc[m][j]);
    if (i + RING == 11) flv = __hip_atomic_load(fl, __ATOMIC_RELAXED, __HIP_MEMORY_SCOPE_AGENT);
    if (i + RING == 12) {
      for (unsigned spin = 0; !__all(flv == 1u) && spin < (1u << 22); ++spin) {
        __builtin_amdgcn_s_sleep(1);
        flv = __hip_atomic_load(fl, __ATOMIC_RELAXED, __HIP_MEMORY_SCOPE_AGENT);
      }
      __builtin_amdgcn_fence(__ATOMIC_ACQUIRE, "wavefront");
    }
    if (i + RING < 17) ld(i + RING);
    __builtin_amdgcn_sched_barrier(0);
  }
  STAMP(1, 6);
#pragma unroll
  for (int m = 0; m < 4; ++m) {
    if (m != w) {
      int slot = m < w ? m : m - 1;
#pragma unroll
      for (int j = 0; j < 5; ++j) *(f4*)(smem + L2_ACC + ((w * 3 + slot) * 5 + j) * 1024 + vo) = acc[m][j];
    }
  }
  WAITL0();
  __builtin_amdgcn_s_barrier();
  f4 own[5];
#pragma unroll
  for (int j = 0; j < 5; ++j) own[j] = w == 0 ? acc[0][j] : (w == 1 ? acc[1][j] : (w == 2 ? acc[2][j] : acc[3][j]));
  f4 tot[5];
#pragma unroll
  for (int j = 0; j < 5; ++j) tot[j] = f4{lo16(xr[j][0]), hi16(xr[j][0]), lo16(xr[j][1]), hi16(xr[j][1])};
#pragma unroll
  for (int s = 0; s < 4; ++s) {
    int slot = w < s ? w : w - 1;
#pragma unroll
    for (int j = 0; j < 5; ++j) {
      f4 v = own[j];
      if (s != w) v = *(const f4*)(smem + L2_ACC + ((s * 3 + slot) * 5 + j) * 1024 + vo);
      tot[j] += v;
    }
  }
#pragma unroll
  for (int j = 0; j < 5; ++j) *(u2*)(out + orow + 16 * j) = u2{pk2(tot[j][0], tot[j][1]), pk2(tot[j][2], tot[j][3])};
  STAMP(1, 7);
}

extern "C" void kernel_launch(void* const* d_in, const int* in_sizes, int n_in, void* d_out, int out_size, void* d_ws,
                              size_t ws_size, hipStream_t stream) {
  const __bf16* x = (const __bf16*)d_in[0];
  const __bf16* wqkv = (const __bf16*)d_in[1];
  const __bf16* wo = (const __bf16*)d_in[2];
  const __bf16* w1 = (const __bf16*)d_in[4];
  const __bf16* w2 = (const __bf16*)d_in[5];
  char* ws = (char*)d_ws;
  (void)hipFuncSetAttribute((const void*)k1_kernel, hipFuncAttributeMaxDynamicSharedMemorySize, K1_LDS);
  (void)hipFuncSetAttribute((const void*)k2_kernel, hipFuncAttributeMaxDynamicSharedMemorySize, K2_LDS);
  k1_kernel<<<256, 512, K1_LDS, stream>>>(x, wqkv, w1, w2, ws);
  k2_kernel<<<256, 512, K2_LDS, stream>>>(x, wo, ws, (__bf16*)d_out);
}
```

```cpp
#include <hip/hip_runtime.h>
#include <hip/hip_bf16.h>
#include <stdint.h>

typedef __attribute__((ext_vector_type(8))) __bf16 bf8;
typedef __attribute__((ext_vector_type(4))) __bf16 bf4;
typedef __attribute__((ext_vector_type(2))) __bf16 bf2;
typedef __attribute__((ext_vector_type(4))) short s4;
typedef __attribute__((ext_vector_type(4))) float f4;
typedef __attribute__((ext_vector_type(2))) float f2;
typedef __attribute__((ext_vector_type(4))) unsigned u4;
typedef __attribute__((ext_vector_type(2))) unsigned u2;
#define AS1 __attribute__((address_space(1)))
#define AS3 __attribute__((address_space(3)))
#define DI __device__ __forceinline__

constexpr int S_ = 2048, D_ = 640, DFF = 1536, N3 = 1920;
constexpr int WS_HF = 0;
constexpr int WS_PQ = WS_HF + 128 * 48 * 1024;
constexpr int WS_AF = WS_PQ + 128 * 20 * 1024;
constexpr int WS_W2 = WS_AF + 128 * 20 * 1024;
constexpr int WS_MF = WS_W2 + 8 * 48 * 5 * 1024;
constexpr int WS_KV = WS_MF + 8 * 20 * 5 * 1024;
constexpr int WS_Z = WS_KV + 16 * 32 * 400 * 4;
constexpr int WS_CG = WS_Z + 16 * 640 * 4;
constexpr int WS_SG = WS_CG + 4 * 3456 * 8;
constexpr int WS_FW = WS_SG + 2048 * 8;
constexpr int WS_F1 = WS_FW + 1024;
constexpr int WS_F2 = WS_F1 + 1024;
constexpr int WS_EP = WS_F2 + 256 * 128;
constexpr int WS_END = WS_EP + 128;
#define STAMPW(k, i) do {} while (0)
#define STAMP(k, i) do {} while (0)

DI float bfr(float v) { return (float)(__bf16)v; }
DI unsigned pk2(float a, float b) { f2 v = {a, b}; return __builtin_bit_cast(unsigned, __builtin_convertvector(v, bf2)); }
DI float lo16(unsigned u) { return __builtin_bit_cast(float, u << 16); }
DI float hi16(unsigned u) { return __builtin_bit_cast(float, u & 0xffff0000u); }
DI f4 mfma(bf8 a, bf8 b, f4 c) { return __builtin_amdgcn_mfma_f32_16x16x32_bf16(a, b, c, 0, 0, 0); }
DI void glds16(const void* g, char* l) {
  __builtin_amdgcn_global_load_lds((const AS1 void*)g, (AS3 void*)l, 16, 0, 0);
}
DI bf8 trfrag(const char* p0, const char* p1) {
  s4 a = __builtin_amdgcn_ds_read_tr16_b64_v4i16((AS3 s4*)p0);
  s4 b = __builtin_amdgcn_ds_read_tr16_b64_v4i16((AS3 s4*)p1);
  return __builtin_bit_cast(bf8, __builtin_shufflevector(a, b, 0, 1, 2, 3, 4, 5, 6, 7));
}
DI bf8 trfrag3(const AS3 char* p0, const AS3 char* p1) {
  s4 a = __builtin_amdgcn_ds_read_tr16_b64_v4i16((AS3 s4*)p0);
  s4 b = __builtin_amdgcn_ds_read_tr16_b64_v4i16((AS3 s4*)p1);
  return __builtin_bit_cast(bf8, __builtin_shufflevector(a, b, 0, 1, 2, 3, 4, 5, 6, 7));
}
DI float phi(float t) {
  return fmaxf(t, 0.f) + 1e-6f + __builtin_amdgcn_exp2f(fminf(t, 0.f) * 1.44269504088896341f);
}
#define MT(i) do {} while (0)
#define WAITV(n) asm volatile("s_waitcnt vmcnt(" #n ")" ::: "memory")
#define WAITL0() asm volatile("s_waitcnt lgkmcnt(0)" ::: "memory")

typedef __amdgpu_buffer_rsrc_t rsrc_t;
DI rsrc_t mkrsrc(const void* p, unsigned bytes) { return __builtin_amdgcn_make_buffer_rsrc((void*)p, 0, bytes, 0x00020000); }
template <int AUX>
DI void blds16(rsrc_t r, char* l, int voff, int soff) {
  __builtin_amdgcn_raw_ptr_buffer_load_lds(r, (AS3 void*)l, 16, voff, soff, 0, AUX);
}
DI u4 ld16(rsrc_t r, int voff, int soff) { return __builtin_amdgcn_raw_buffer_load_b128(r, voff, soff, 16); }
DI void st16(u4 v, rsrc_t r, int voff, int soff) { __builtin_amdgcn_raw_buffer_store_b128(v, r, voff + soff, 0, 16); }
DI void st8(u2 v, rsrc_t r, int voff, int soff) { __builtin_amdgcn_raw_buffer_store_b64(v, r, voff, soff, 16); }
DI void st4(unsigned v, rsrc_t r, int voff, int soff) { __builtin_amdgcn_raw_buffer_store_b32(v, r, voff, soff, 16); }
DI unsigned ld4(rsrc_t r, int voff, int soff) { return __builtin_amdgcn_raw_buffer_load_b32(r, voff, soff, 16); }
DI float wave_sum(float v) {
#define DPP_ADD(c) v += __builtin_bit_cast(float, __builtin_amdgcn_update_dpp(0, __builtin_bit_cast(int, v), c, 0xf, 0xf, true))
  DPP_ADD(0x111); DPP_ADD(0x112); DPP_ADD(0x114); DPP_ADD(0x118);
#undef DPP_ADD
  const int u = __builtin_bit_cast(int, v);
  return (__builtin_bit_cast(float, __builtin_amdgcn_readlane(u, 15)) + __builtin_bit_cast(float, __builtin_amdgcn_readlane(u, 31))) +
         (__builtin_bit_cast(float, __builtin_amdgcn_readlane(u, 47)) + __builtin_bit_cast(float, __builtin_amdgcn_readlane(u, 63)));
}
constexpr unsigned SPIN = 1u << 20;
DI void wait16(rsrc_t rs, int voff, int soff, unsigned tag) {
#pragma unroll 1
  for (unsigned spin = 0; spin < SPIN; ++spin) {
    asm volatile("" : "+v"(voff) :: "memory");
    u4 v = ld16(rs, voff, soff);
    if (__all(v[0] == tag && v[1] == tag && v[2] == tag && v[3] == tag)) break;
    __builtin_amdgcn_s_sleep(1);
    asm volatile("" ::: "memory");
  }
  asm volatile("" ::: "memory");
  __builtin_amdgcn_fence(__ATOMIC_ACQUIRE, "wavefront");
}
DI void wait4(rsrc_t rs, int voff, int soff, unsigned tag) {
#pragma unroll 1
  for (unsigned spin = 0; spin < SPIN; ++spin) {
    asm volatile("" : "+v"(voff) :: "memory");
    if (__all(ld4(rs, voff, soff) == tag)) break;
    __builtin_amdgcn_s_sleep(1);
    asm volatile("" ::: "memory");
  }
  asm volatile("" ::: "memory");
  __builtin_amdgcn_fence(__ATOMIC_ACQUIRE, "wavefront");
}

constexpr int A_BYTES = 128 * 128;
constexpr int L_Q = 3 * A_BYTES;
constexpr int L_EARLY = 2 * A_BYTES;
constexpr int L_ROW = L_Q + 3 * 64 * 448;
constexpr int L_DUM = L_ROW + 16384;
constexpr int L_CNT = L_DUM + 1024;
constexpr int LDS_BYTES = L_CNT + 64;

template <bool KV>
DI void phase1(const __bf16* __restrict__ x, const __bf16* __restrict__ wqkv, const __bf16* __restrict__ w1,
               const __bf16* __restrict__ w2, char* ws, char* smem, int rb, int ci, int bid, unsigned tag) {
  constexpr int NQ = KV ? 20 : 12, NH = KV ? 8 : 16;
  constexpr int QB = 64 * NQ * 16, HB = 64 * NH * 16;
  constexpr int L_H = L_Q + 3 * QB;
  constexpr int JQ = KV ? 5 : 3;
  const int tid = threadIdx.x, lane = tid & 63;
  const int w = __builtin_amdgcn_readfirstlane(tid >> 6), wm = w >> 1, wn = w & 1;
  const int g = lane >> 4, l15 = lane & 15, q4 = l15 >> 2, p4 = lane & 3;
  const rsrc_t rs = mkrsrc(ws, WS_END);
  const rsrc_t rx = mkrsrc(x, S_ * D_ * 2);
  const int cl = bid % 54, cq = bid / 54;
  const int wks = bid / 5, wcb = bid - wks * 5;
  {
    blds16<0>(rx, smem + L_ROW + w * 2048, (bid * 8 + w) * (D_ * 2) + lane * 16, 0);
    blds16<0>(rx, smem + L_ROW + w * 2048 + 1024, (bid * 8 + w) * (D_ * 2) + (lane < 16 ? lane * 16 : 0), 1024);
    const bool q = cl < 30;
    const rsrc_t rc = mkrsrc(q ? wqkv : w1, bid < 216 ? (q ? D_ * N3 * 2 : D_ * DFF * 2) : 0);
    const int st = q ? N3 * 2 : DFF * 2;
    const int vb = (cq * 160 + (lane >> 3)) * st + (q ? cl : cl - 30) * 128 + (lane & 7) * 16;
#pragma unroll
    for (int n = 0; n < 3; ++n) {
      const int i = n * 8 + w;
      blds16<0>(rc, smem + (i < 20 ? L_EARLY + i * 1024 : L_DUM), vb, (i < 20 ? i : 0) * 8 * st);
    }
    blds16<0>(mkrsrc(w2, bid < 240 ? DFF * D_ * 2 : 0), smem + L_EARLY + 20480 + w * 1024,
              ((wks * 32 + w * 4 + g) * D_ + wcb * 128 + l15 * 8) * 2, 0);
  }
  int voA[2], voB[4];
#pragma unroll
  for (int n = 0; n < 2; ++n) {
    int row = (w * 2 + n) * 8 + (lane >> 3);
    voA[n] = (rb * 128 + row) * (D_ * 2) + (((lane & 7) ^ ((row >> 1) & 7)) << 4);
  }
  const int ib0 = w < 4 ? w * 4 : 16 + (w - 4) * 3;
  const void* pB[4];
  int nB[4], dB[4], zB[4], kB[4];
#pragma unroll
  for (int n = 0; n < 4; ++n) {
    const int ib = ib0 + n;
    const bool isq = ib < NQ, dummy = n == 3 && w >= 4;
    if (isq) {
      int c = ib * 64 + lane, k = c / NQ, ch = c - k * NQ;
      ch ^= ((k >> 3) & 1) << 1;
      int col;
      if (KV) { int cw = ch >= 10, cc = ch - 10 * cw, v = cc >= 5; col = 640 + 640 * v + 80 * ci + 40 * cw + 8 * (cc - 5 * v); }
      else col = 80 * ci + 8 * (ch >= 10 ? ch - 2 : ch);
      voB[n] = (k * N3 + col) * 2;
    } else {
      int c = (ib - NQ) * 64 + lane, k = c / NH, ch = c - k * NH;
      int f = KV ? (((k >> 1) & 1) | ((k >> 2) & 2)) : ((k & 3) | ((k >> 1) & 4));
      ch ^= f << 1;
      voB[n] = (k * DFF + (KV ? 1024 + 64 * ci : 128 * ci) + 8 * ch) * 2;
    }
    pB[n] = isq ? (const void*)wqkv : (const void*)w1;
    nB[n] = dummy ? 0 : (isq ? D_ * N3 * 2 : D_ * DFF * 2);
    dB[n] = dummy ? L_DUM : (isq ? L_Q + 2 * QB + ib * 1024 : L_H + (ib - NQ) * 1024);
    zB[n] = dummy ? 0 : (isq ? -QB : HB);
    kB[n] = isq ? 64 * N3 * 2 : 64 * DFF * 2;
  }
  auto ldslot = [&](int n, int stg, int t) {
    const bool on = t < 10;
    if (n < 2) blds16<0>(mkrsrc(x, on ? S_ * D_ * 2 : 0), smem + stg * A_BYTES + w * 2048 + n * 1024, voA[n], t * 128);
    else blds16<0>(mkrsrc(pB[n - 2], on ? nB[n - 2] : 0), smem + dB[n - 2] + stg * zB[n - 2], voB[n - 2], t * kB[n - 2]);
  };
#pragma unroll
  for (int n = 0; n < 6; ++n) ldslot(n, 0, 0);
#pragma unroll
  for (int n = 0; n < 6; ++n) ldslot(n, 1, 1);
  unsigned ra[2], rb_[7];
  {
    const unsigned lb = (unsigned)(size_t)(AS3 char*)smem;
    const int f = KV ? (((q4 >> 1) & 1) | ((g & 1) << 1)) : (q4 | ((g & 1) << 2));
#pragma unroll
    for (int s = 0; s < 2; ++s) ra[s] = lb + (wm * 32 + l15) * 128 + (((s * 4 + g) ^ (l15 >> 1)) << 4);
#pragma unroll
    for (int j = 0; j < 7; ++j)
      rb_[j] = lb + 8 * p4 + (j < JQ ? L_Q + (8 * g + q4) * (NQ * 16) + (((wn * JQ + j) ^ (g & 1)) << 5)
                                     : L_H + (8 * g + q4) * (NH * 16) + (((wn * (7 - JQ) + j - JQ) ^ f) << 5));
  }
  int voG;
  {
    const int c = w * 64 + lane, qq = c / 112, rem = c - qq * 112, tl = rem >> 3, cw = tl >= 7, j = tl - 7 * cw, pr = (rem & 7) * 2;
    int col;
    if (KV) { int cc = j * 16 + pr; col = j < 5 ? 640 + 80 * ci + 40 * cw + (cc >= 40 ? 600 + cc : cc) : N3 + 1024 + 64 * ci + 32 * cw + cc - 80; }
    else col = j < 3 ? 80 * ci + 48 * cw + 16 * (cw && j == 2 ? 1 : j) + pr : N3 + 128 * ci + 64 * cw + 16 * (j - 3) + pr;
    voG = w < 7 ? WS_CG + (qq * 3456 + col) * 8 : WS_SG + rb * 1024 + lane * 16;
  }
  const f4 zero4 = {0.f, 0.f, 0.f, 0.f};
  STAMP(0, 1);
  WAITV(12);
  __builtin_amdgcn_s_barrier();
  {
    const u4 a = *(const u4*)(smem + L_ROW + w * 2048 + lane * 16), b = *(const u4*)(smem + L_ROW + w * 2048 + 1024 + l15 * 16);
    const char* p0 = smem + L_EARLY + 20480 + (4 * g + q4) * 256 + w * 32 + 8 * p4;
    const bf8 f = trfrag(p0, p0 + 4096);
    if (w < 4) {
      const bf8 ones = __builtin_bit_cast(bf8, u4{0x3f803f80u, 0x3f803f80u, 0x3f803f80u, 0x3f803f80u});
      const char* c0 = smem + L_EARLY + (8 * g + q4) * 128 + w * 32 + 8 * p4;
      f4 c = zero4;
#pragma unroll
      for (int k = 0; k < 5; ++k) c = mfma(ones, trfrag(c0 + k * 4096, c0 + k * 4096 + 512), c);
      if (g == 0 && bid < 216) st8(u2{__builtin_bit_cast(unsigned, c[0]), tag}, rs, l15 * 8, WS_CG + (cq * 3456 + cl * 64 + w * 16) * 8);
    }
    if (bid < 240) {
      const int col = wcb * 128 + w * 16, cg = col / 80;
      st16(__builtin_bit_cast(u4, f), rs, lane * 16, WS_W2 + ((cg * 48 + wks) * 5 + ((col - cg * 80) >> 4)) * 1024);
    }
    float v[16], su = 0.f;
#pragma unroll
    for (int e = 0; e < 4; ++e) { v[2 * e] = lo16(a[e]); v[2 * e + 1] = hi16(a[e]); v[8 + 2 * e] = lo16(b[e]); v[9 + 2 * e] = hi16(b[e]); }
    float s2 = 0.f;
#pragma unroll
    for (int e = 0; e < 8; ++e) { su += v[e]; s2 += v[8 + e]; }
    const float mean = wave_sum(lane < 16 ? su + s2 : su) * (1.f / 640.f);
    su = 0.f; s2 = 0.f;
#pragma unroll
    for (int e = 0; e < 8; ++e) { float d0 = v[e] - mean, d1 = v[8 + e] - mean; su += d0 * d0; s2 += d1 * d1; }
    const float var = wave_sum(lane < 16 ? su + s2 : su) * (1.f / 640.f);
    const float sd = __builtin_amdgcn_sqrtf(bfr(bfr(var) + 1e-5f));
    if (lane == 0) st8(u2{pk2(mean, sd), tag}, rs, 0, WS_SG + (bid * 8 + w) * 8);
    WAITL0();
  }
  f4 acc[2][7];
#pragma unroll
  for (int j = 0; j < 7; ++j) acc[0][j] = acc[1][j] = zero4;
  bf8 fa[2][2], fb[2][7];
  auto rd = [&](int s, int stg, int sn, int tn) {
#pragma unroll
    for (int i = 0; i < 2; ++i) fa[s][i] = *(const AS3 bf8*)(size_t)(ra[s] + stg * A_BYTES + i * 2048);
#pragma unroll
    for (int j = 0; j < 7; ++j) {
      const AS3 char* p = (const AS3 char*)(size_t)rb_[j] + (j < JQ ? (2 - stg) * QB + s * 32 * NQ * 16 : stg * HB + s * 32 * NH * 16);
      fb[s][j] = trfrag3(p, p + 4 * (j < JQ ? NQ : NH) * 16);
      if (j == 0 || j == 3 || j == 5) ldslot(3 * s + (j + 1) / 2 - (j > 0), sn, tn);
    }
  };
  auto mm = [&](int s) {
#pragma unroll
    for (int j = 0; j < 7; ++j)
#pragma unroll
      for (int i = 0; i < 2; ++i)
        acc[i][j] = (KV && j < 5) ? mfma(fa[s][i], fb[s][j], acc[i][j]) : mfma(fb[s][j], fa[s][i], acc[i][j]);
  };
  auto interleave = [&]() {
#pragma unroll
    for (int i = 0; i < 14; ++i) {
      __builtin_amdgcn_sched_group_barrier(0x008, 1, 0);
      __builtin_amdgcn_sched_group_barrier(0x100, 1, 0);
      if (i < 2) __builtin_amdgcn_sched_group_barrier(0x100, 1, 0);
      if (i == 1 || i == 7 || i == 11) __builtin_amdgcn_sched_group_barrier(0x020, 1, 0);
    }
  };
  auto body = [&](int t, int stg, bool first) {
    WAITV(6);
    __builtin_amdgcn_s_barrier();
    if (stg == 1) {
      if (t == 1) { if (tid == 0) st4(tag, rs, 0, WS_FW + bid * 4); }
      else if (t == 7) blds16<16>(rs, smem + L_ROW + w * 1024, voG, 0);
    }
    const int sn = stg == 0 ? 2 : stg - 1;
    __builtin_amdgcn_sched_barrier(0);
    rd(0, stg, sn, t + 2);
    if (!first) { mm(1); interleave(); }
    __builtin_amdgcn_sched_barrier(0);
    rd(1, stg, sn, t + 2);
    mm(0);
    interleave();
    __builtin_amdgcn_sched_barrier(0);
    WAITL0();
  };
  STAMP(0, 2);
  body(0, 0, true);
#pragma unroll 1
  for (int t = 1; t < 10; t += 3) {
    body(t, 1, false);
    body(t + 1, 2, false);
    body(t + 2, 0, false);
  }
  __builtin_amdgcn_sched_barrier(0);
  mm(1);
  STAMP(0, 4);
#pragma unroll 1
  for (unsigned spin = 0; spin < SPIN; ++spin) {
    const u4 v = *(const u4*)(smem + L_ROW + w * 1024 + lane * 16);
    if (__all(v[1] == tag && v[3] == tag)) break;
    __builtin_amdgcn_s_sleep(1);
    asm volatile("" : "+v"(voG) :: "memory");
    *(u4*)(smem + L_ROW + w * 1024 + lane * 16) = ld16(rs, voG, 0);
  }
  WAITL0();
  __builtin_amdgcn_s_barrier();
  const char* sg = smem + L_ROW + 7168 + wm * 256;
  unsigned ms[2];
#pragma unroll
  for (int i = 0; i < 2; ++i) ms[i] = *(const unsigned*)(sg + (i * 16 + l15) * 8);
  {
    bf8 am[2];
#pragma unroll
    for (int i = 0; i < 2; ++i) {
      const unsigned nm = (ms[i] & 0xffffu) ^ 0x8000u;
      am[i] = __builtin_bit_cast(bf8, u4{nm | (nm << 16), nm, 0u, 0u});
    }
#pragma unroll
    for (int j = 0; j < 7; ++j) {
      const float c = *(const float*)(smem + L_ROW + (g * 224 + (wn * 7 + j) * 16 + l15) * 8);
      const float r1 = c - bfr(c), r2 = r1 - bfr(r1);
      const bf8 bm = __builtin_bit_cast(bf8, u4{pk2(c, r1), pk2(r2, 0.f), 0u, 0u});
#pragma unroll
      for (int i = 0; i < 2; ++i) acc[i][j] = (KV && j < 5) ? mfma(am[i], bm, acc[i][j]) : mfma(bm, am[i], acc[i][j]);
    }
  }
  STAMP(0, 5);
  float rstd[2];
#pragma unroll
  for (int i = 0; i < 2; ++i) rstd[i] = __builtin_amdgcn_rcpf(hi16(ms[i]));
  const int rt0 = rb * 8 + wm * 2;
  typedef __attribute__((ext_vector_type(2))) short s2_t;
  auto relu2 = [&](float a, float b) {
    s2_t v = __builtin_bit_cast(s2_t, pk2(a, b));
    return __builtin_bit_cast(unsigned, __builtin_elementwise_max(v, s2_t{0, 0}));
  };
  if (KV) {
    u4 fr[6];
#pragma unroll
    for (int i = 0; i < 2; ++i) {
      const u4 s0 = *(const u4*)(sg + (i * 16 + 4 * g) * 8), s1 = *(const u4*)(sg + (i * 16 + 4 * g) * 8 + 16);
      const f4 rr = {__builtin_amdgcn_rcpf(hi16(s0[0])), __builtin_amdgcn_rcpf(hi16(s0[2])), __builtin_amdgcn_rcpf(hi16(s1[0])),
                     __builtin_amdgcn_rcpf(hi16(s1[2]))};
#pragma unroll
      for (int j = 0; j < 5; ++j) {
        f4 y = acc[i][j] * rr;
        if (j < 3) {
#pragma unroll
          for (int e = 0; e < 4; ++e) y[e] = (j < 2 || l15 < 8) ? phi(y[e]) : y[e];
        }
        fr[j][2 * i] = pk2(y[0], y[1]);
        fr[j][2 * i + 1] = pk2(y[2], y[3]);
      }
    }
#pragma unroll
    for (int e = 0; e < 4; ++e) fr[5][e] = l15 < 8 ? 0x3f803f80u : fr[2][e];
    const int ta[8] = {0, 0, 1, 1, 1, 2, 2, 2}, tb[8] = {5, 3, 5, 3, 4, 5, 3, 4};
#pragma unroll
    for (int m = 0; m < 8; ++m)
      *(f4*)(smem + (w * 8 + m) * 1024 + lane * 16) = mfma(__builtin_bit_cast(bf8, fr[tb[m]]), __builtin_bit_cast(bf8, fr[ta[m]]), zero4);
  } else {
#pragma unroll
    for (int j = 0; j < 3; ++j) {
      if (j == 2 && wn) break;
      const int col = 80 * ci + 48 * wn + 16 * j;
#pragma unroll
      for (int i = 0; i < 2; ++i) {
        const f4 y = acc[i][j] * rstd[i];
        st8(u2{pk2(phi(y[0]), phi(y[1])), pk2(phi(y[2]), phi(y[3]))}, rs, lane * 16,
            WS_PQ + ((rt0 + i) * 20 + (col >> 5)) * 1024 + ((col >> 4) & 1) * 8);
      }
    }
  }
#pragma unroll
  for (int j = JQ; j < 7; j += 2) {
    const int ks = (KV ? 1024 + 64 * ci + 32 * wn : 128 * ci + 64 * wn + 16 * (j - JQ)) >> 5;
#pragma unroll
    for (int i = 0; i < 2; ++i) {
      const f4 y0 = acc[i][j] * rstd[i], y1 = acc[i][j + 1] * rstd[i];
      st16(u4{relu2(y0[0], y0[1]), relu2(y0[2], y0[3]), relu2(y1[0], y1[1]), relu2(y1[2], y1[3])}, rs, lane * 16,
           WS_HF + ((rt0 + i) * 48 + ks) * 1024);
    }
  }
  STAMP(0, 6);
  if (KV) {
    WAITL0();
    __builtin_amdgcn_s_barrier();
    if (tid < 480) {
      const bool isz = tid >= 400;
      int hl = isz ? (tid - 400) / 20 : tid / 100, rem = isz ? (tid - 400 - hl * 20) * 5 : tid - hl * 100, d = rem / 5, grp = rem - d * 5;
      int hk = hl & 1, kc = 20 * hk + d, a = kc >> 4;
      int pos = isz ? 32 : 40 + 20 * hk + 4 * grp, b = pos >> 4;
      int m = a == 0 ? b - 2 : (a == 1 ? b : b + 3);
      const char* p = smem + ((hl >> 1) * 8 + m) * 1024 + (((pos >> 2) & 3) * 16 + (kc & 15)) * 16;
      f4 v = (*(const f4*)p + *(const f4*)(p + 16384)) + (*(const f4*)(p + 32768) + *(const f4*)(p + 49152));
      if (isz) st4(__builtin_bit_cast(unsigned, v[0]), rs, (tid - 400) * 4, WS_Z + (rb * 640 + ci * 80) * 4);
      else st16(__builtin_bit_cast(u4, v), rs, tid * 16, WS_KV + (rb * 32 + ci * 4) * 1600);
    }
  }
}

constexpr int L2_ACC = 0;
constexpr int L2_WO = 61440;
constexpr int L2_KV = L2_WO + 5120;
constexpr int L2_Z = L2_KV + 2048;
#define RING 3
DI void publish2(rsrc_t rs, char* smem, int flag, int lane, unsigned tag) {
  WAITV(0);
  if (lane == 0) {
    unsigned old = __hip_atomic_fetch_add((AS3 unsigned*)(smem + L_CNT), 1u, __ATOMIC_RELAXED, __HIP_MEMORY_SCOPE_WORKGROUP);
    if (old == 2u) st4(tag, rs, 0, WS_F2 + flag * 128);
  }
}

__global__ __launch_bounds__(512, 2) void fused_kernel(const __bf16* __restrict__ x, const __bf16* __restrict__ wqkv,
                                                       const __bf16* __restrict__ wo, const __bf16* __restrict__ w1,
                                                       const __bf16* __restrict__ w2, char* ws,
                                                       __bf16* __restrict__ out) {
  extern __shared__ __attribute__((aligned(16))) char smem[];
  const int bid = blockIdx.x, xcd = bid & 7, idx = bid >> 3;
  STAMP(0, 0);
  const unsigned tag = *(const unsigned*)(ws + WS_EP) + 1u;
  if (threadIdx.x == 0) *(unsigned*)(smem + L_CNT) = 0u;
  const int rb = (xcd >> 1) * 4 + (idx & 3), cgi = idx >> 2;
  {
    const int ci = (xcd & 1) * 4 + (cgi >> 1);
    if (cgi & 1) phase1<false>(x, wqkv, w1, w2, ws, smem, rb, ci, bid, tag);
    else phase1<true>(x, wqkv, w1, w2, ws, smem, rb, ci, bid, tag);
  }
  int tid = threadIdx.x;
  asm volatile("" : "+v"(tid));
  const int lane = tid & 63, w = __builtin_amdgcn_readfirstlane(tid >> 6);
  const int g = lane >> 4, l15 = lane & 15;
  const int r = (xcd >> 1) * 8 + (idx & 7), c = (xcd & 1) * 4 + (idx >> 3);
  const rsrc_t rs = mkrsrc(ws, WS_END);
  const int vo = lane * 16;
  const f4 zero4 = {0.f, 0.f, 0.f, 0.f};
  WAITV(0);
  WAITL0();
  __builtin_amdgcn_s_barrier();
  if (tid == 0) st4(tag, rs, 0, WS_F1 + (rb * 16 + (xcd & 1) * 8 + cgi) * 4);
  STAMP(0, 7);
  STAMP(1, 0);
  if (w >= 4) {
    __builtin_amdgcn_s_setprio(3);
    if (w == 4) {
      u4 wv[4];
#pragma unroll
      for (int i = 0; i < 4; ++i) {
        int ch = i * 64 + lane, row = ch / 10;
        wv[i] = u4{0u, 0u, 0u, 0u};
        if (ch < 200) wv[i] = *(const u4*)(wo + (20 * r + row) * D_ + 80 * c + (ch - row * 10) * 8);
      }
#pragma unroll
      for (int i = 0; i < 4; ++i) *(u4*)(smem + L2_WO + i * 1024 + vo) = wv[i];
      *(u4*)(smem + L2_WO + 4096 + vo) = u4{0u, 0u, 0u, 0u};
      wait16(rs, vo, WS_F1, tag);
      if (bid == 0 && lane == 0) st4(tag, rs, 0, WS_EP);
      const int v1o = lane < 36 ? vo + 1024 : vo;
      f4 v0[16], v1[16];
#pragma unroll
      for (int i = 0; i < 16; ++i) {
        v0[i] = __builtin_bit_cast(f4, ld16(rs, vo, WS_KV + (i * 32 + r) * 1600));
        v1[i] = __builtin_bit_cast(f4, ld16(rs, v1o, WS_KV + (i * 32 + r) * 1600));
      }
#pragma unroll
      for (int st = 1; st < 16; st *= 2)
#pragma unroll
        for (int i = 0; i < 16; i += 2 * st) { v0[i] += v0[i + st]; v1[i] += v1[i + st]; }
      *(f4*)(smem + L2_KV + vo) = v0[0];
      *(f4*)(smem + L2_KV + 1024 + vo) = v1[0];
      u4 au[2];
#pragma unroll
      for (int dt = 0; dt < 2; ++dt)
#pragma unroll
        for (int hh = 0; hh < 2; ++hh) {
          int d = dt * 16 + l15, grp = 2 * g + hh;
          bool ok = d < 20 && grp < 5;
          f4 v = *(const f4*)(smem + L2_KV + (ok ? d * 5 + grp : 0) * 16);
          au[dt][2 * hh] = ok ? pk2(v[0], v[1]) : 0u;
          au[dt][2 * hh + 1] = ok ? pk2(v[2], v[3]) : 0u;
        }
      const int k0 = 20 * r + 4 * g, k1 = 20 * r + 16;
      const char* p0 = smem + L2_WO + (8 * g + (l15 >> 2)) * 160 + (lane & 3) * 8;
      const int o0 = (k0 >> 5) * 5120 + ((k0 >> 2) & 3) * 256 + ((k0 >> 4) & 1) * 8 + l15 * 16;
      const int o1 = (k1 >> 5) * 5120 + ((k1 >> 2) & 3) * 256 + ((k1 >> 4) & 1) * 8 + l15 * 16;
#pragma unroll
      for (int t = 0; t < 5; ++t) {
        bf8 b = trfrag(p0 + t * 32, p0 + t * 32 + 640);
        f4 m0 = mfma(__builtin_bit_cast(bf8, au[0]), b, zero4), m1 = mfma(__builtin_bit_cast(bf8, au[1]), b, zero4);
        st8(u2{pk2(m0[0], m0[1]), pk2(m0[2], m0[3])}, rs, o0, WS_MF + c * (20 * 5120) + t * 1024);
        if (g == 0) st8(u2{pk2(m1[0], m1[1]), pk2(m1[2], m1[3])}, rs, o1, WS_MF + c * (20 * 5120) + t * 1024);
      }
      publish2(rs, smem, r * 8 + c, lane, tag);
      STAMPW(1, 8);
    } else if (w < 7) {
      const int fo = ((r * 4 + (c & 1) * 2 + w - 5) * 20 + (c >> 1) * 5) * 1024;
      wait16(rs, vo, WS_F1, tag);
      f4 zv[16];
#pragma unroll
      for (int i = 0; i < 16; ++i) zv[i] = __builtin_bit_cast(f4, ld16(rs, lane < 40 ? vo : 0, WS_Z + i * 2560 + (c >> 1) * 640));
      u4 pq[5];
#pragma unroll
      for (int k = 0; k < 5; ++k) pq[k] = ld16(rs, vo, WS_PQ + fo + k * 1024);
#pragma unroll
      for (int st = 1; st < 16; st *= 2)
#pragma unroll
        for (int i = 0; i < 16; i += 2 * st) zv[i] += zv[i + st];
      char* zl = smem + L2_Z + (w - 5) * 1024;
      if (lane < 40) *(u2*)(zl + lane * 8) = u2{pk2(zv[0][0], zv[0][1]), pk2(zv[0][2], zv[0][3])};
      f4 den = zero4;
#pragma unroll
      for (int k = 0; k < 5; ++k) {
        u4 zf;
#pragma unroll
        for (int p = 0; p < 2; ++p) {
          int gi = 8 * k + 4 * p + g;
          u2 z2 = *(const u2*)(zl + gi * 8);
          bool mine = gi / 5 == l15;
          zf[2 * p] = mine ? z2[0] : 0u;
          zf[2 * p + 1] = mine ? z2[1] : 0u;
        }
        den = mfma(__builtin_bit_cast(bf8, zf), __builtin_bit_cast(bf8, pq[k]), den);
      }
      f4 rd;
#pragma unroll
      for (int e = 0; e < 4; ++e) rd[e] = __builtin_amdgcn_rcpf(bfr(bfr(den[e]) + 1e-6f));
      if (g < 2) *(f4*)(zl + 512 + l15 * 32 + g * 16) = rd;
#pragma unroll
      for (int k = 0; k < 5; ++k) {
        u4 o;
#pragma unroll
        for (int p = 0; p < 2; ++p) {
          float s = *(const float*)(zl + 512 + l15 * 32 + ((8 * k + 4 * p + g) / 5) * 4);
          o[2 * p] = pk2(lo16(pq[k][2 * p]) * s, hi16(pq[k][2 * p]) * s);
          o[2 * p + 1] = pk2(lo16(pq[k][2 * p + 1]) * s, hi16(pq[k][2 * p + 1]) * s);
        }
        st16(o, rs, vo, WS_AF + fo + k * 1024);
      }
      publish2(rs, smem, r * 8 + c, lane, tag);
      STAMPW(1, 9);
    }
    __builtin_amdgcn_s_barrier();
    return;
  }
  const int hA = WS_HF + (r * 4 * 48 + w) * 1024, hB = WS_W2 + (c * 48 + w) * 5120;
  const int aA = WS_AF + (r * 4 * 20 + w) * 1024, aB = WS_MF + (c * 20 + w) * 5120;
  bf8 fa[RING][4], fb[RING][5];
  auto ldB = [&](int i) {
#pragma unroll
    for (int j = 0; j < 5; ++j) fb[i % RING][j] = __builtin_bit_cast(bf8, ld16(rs, vo + j * 1024, i < 12 ? hB + i * 20480 : aB + (i - 12) * 20480));
  };
  auto ldA = [&](int i) {
#pragma unroll
    for (int j = 0; j < 4; ++j)
      fa[i % RING][j] = __builtin_bit_cast(bf8, ld16(rs, vo, i < 12 ? hA + i * 4096 + j * 49152 : aA + (i - 12) * 4096 + j * 20480));
  };
  const int orow = (r * 64 + w * 16 + l15) * D_ + c * 80 + 4 * g;
  u2 xr[5];
  wait16(rs, lane < 60 ? vo : 0, WS_FW, tag);
#pragma unroll
  for (int i = 0; i < RING; ++i) ldB(i);
  wait4(rs, l15 * 4, WS_F1 + (r >> 1) * 64, tag);
  STAMP(1, 1);
#pragma unroll
  for (int i = 0; i < RING; ++i) ldA(i);
  const int fo = (lane < 8 ? r * 8 + lane : (lane < 40 ? (lane - 8) * 8 + c : r * 8 + c)) * 128;
  __builtin_amdgcn_sched_barrier(0);
  f4 acc[4][5];
#pragma unroll
  for (int i = 0; i < 4; ++i)
#pragma unroll
    for (int j = 0; j < 5; ++j) acc[i][j] = zero4;
  unsigned flv = 0;
#pragma unroll
  for (int i = 0; i < 17; ++i) {
    const int sl = i % RING;
#pragma unroll
    for (int j = 0; j < 5; ++j)
#pragma unroll
      for (int m = 0; m < 4; ++m) acc[m][j] = mfma(fb[sl][j], fa[sl][m], acc[m][j]);
    if (i + RING == 11) flv = ld4(rs, fo, WS_F2);
    if (i + RING == 12) {
      if (!__all(flv == tag)) wait4(rs, fo, WS_F2, tag);
      asm volatile("" ::: "memory");
      __builtin_amdgcn_fence(__ATOMIC_ACQUIRE, "wavefront");
    }
    if (i + RING < 17) { ldB(i + RING); ldA(i + RING); }
    if (i + RING == 17) {
#pragma unroll
      for (int j = 0; j < 5; ++j) xr[j] = *(const u2*)(x + orow + 16 * j);
    }
    __builtin_amdgcn_sched_barrier(0);
  }
  STAMP(1, 6);
#pragma unroll
  for (int m = 0; m < 4; ++m) {
    if (m != w) {
      int slot = m < w ? m : m - 1;
#pragma unroll
      for (int j = 0; j < 5; ++j) *(f4*)(smem + L2_ACC + ((w * 3 + slot) * 5 + j) * 1024 + vo) = acc[m][j];
    }
  }
  WAITL0();
  __builtin_amdgcn_s_barrier();
  f4 own[5];
#pragma unroll
  for (int j = 0; j < 5; ++j) own[j] = w == 0 ? acc[0][j] : (w == 1 ? acc[1][j] : (w == 2 ? acc[2][j] : acc[3][j]));
  f4 tot[5];
#pragma unroll
  for (int j = 0; j < 5; ++j) tot[j] = f4{lo16(xr[j][0]), hi16(xr[j][0]), lo16(xr[j][1]), hi16(xr[j][1])};
#pragma unroll
  for (int s = 0; s < 4; ++s) {
    int slot = w < s ? w : w - 1;
#pragma unroll
    for (int j = 0; j < 5; ++j) {
      f4 v = own[j];
      if (s != w) v = *(const f4*)(smem + L2_ACC + ((s * 3 + slot) * 5 + j) * 1024 + vo);
      tot[j] += v;
    }
  }
#pragma unroll
  for (int j = 0; j < 5; ++j) *(u2*)(out + orow + 16 * j) = u2{pk2(tot[j][0], tot[j][1]), pk2(tot[j][2], tot[j][3])};
  STAMP(1, 7);
}

extern "C" void kernel_launch(void* const* d_in, const int* in_sizes, int n_in, void* d_out, int out_size, void* d_ws,
                              size_t ws_size, hipStream_t stream) {
  (void)hipFuncSetAttribute((const void*)fused_kernel, hipFuncAttributeMaxDynamicSharedMemorySize, LDS_BYTES);
  fused_kernel<<<256, 512, LDS_BYTES, stream>>>((const __bf16*)d_in[0], (const __bf16*)d_in[1], (const __bf16*)d_in[2],
                                                (const __bf16*)d_in[4], (const __bf16*)d_in[5], (char*)d_ws, (__bf16*)d_out);
}
```

```cpp
#include <hip/hip_runtime.h>
#include <hip/hip_bf16.h>
#include <stdint.h>

typedef __attribute__((ext_vector_type(8))) __bf16 bf8;
typedef __attribute__((ext_vector_type(4))) __bf16 bf4;
typedef __attribute__((ext_vector_type(2))) __bf16 bf2;
typedef __attribute__((ext_vector_type(4))) short s4;
typedef __attribute__((ext_vector_type(4))) float f4;
typedef __attribute__((ext_vector_type(2))) float f2;
typedef __attribute__((ext_vector_type(4))) unsigned u4;
typedef __attribute__((ext_vector_type(2))) unsigned u2;
#define AS1 __attribute__((address_space(1)))
#define AS3 __attribute__((address_space(3)))
#define DI __device__ __forceinline__

constexpr int S_ = 2048, D_ = 640, DFF = 1536, N3 = 1920;
constexpr int WS_HF = 0;
constexpr int WS_PQ = WS_HF + 128 * 48 * 1024;
constexpr int WS_AF = WS_PQ + 128 * 20 * 1024;
constexpr int WS_W2 = WS_AF + 128 * 20 * 1024;
constexpr int WS_MF = WS_W2 + 8 * 48 * 5 * 1024;
constexpr int WS_KV = WS_MF + 8 * 20 * 5 * 1024;
constexpr int WS_Z = WS_KV + 16 * 32 * 400 * 4;
constexpr int WS_CG = WS_Z + 16 * 640 * 4;
constexpr int WS_SG = WS_CG + 4 * 3456 * 8;
constexpr int WS_FW = WS_SG + 2048 * 8;
constexpr int WS_F1 = WS_FW + 1024;
constexpr int WS_F2 = WS_F1 + 1024;
constexpr int WS_EP = WS_F2 + 256 * 128;
constexpr int WS_END = WS_EP + 128;
#define STAMPW(k, i) do {} while (0)
#define STAMP(k, i) do {} while (0)

DI float bfr(float v) { return (float)(__bf16)v; }
DI unsigned pk2(float a, float b) { f2 v = {a, b}; return __builtin_bit_cast(unsigned, __builtin_convertvector(v, bf2)); }
DI float lo16(unsigned u) { return __builtin_bit_cast(float, u << 16); }
DI float hi16(unsigned u) { return __builtin_bit_cast(float, u & 0xffff0000u); }
DI f4 mfma(bf8 a, bf8 b, f4 c) { return __builtin_amdgcn_mfma_f32_16x16x32_bf16(a, b, c, 0, 0, 0); }
DI void glds16(const void* g, char* l) {
  __builtin_amdgcn_global_load_lds((const AS1 void*)g, (AS3 void*)l, 16, 0, 0);
}
DI bf8 trfrag(const char* p0, const char* p1) {
  s4 a = __builtin_amdgcn_ds_read_tr16_b64_v4i16((AS3 s4*)p0);
  s4 b = __builtin_amdgcn_ds_read_tr16_b64_v4i16((AS3 s4*)p1);
  return __builtin_bit_cast(bf8, __builtin_shufflevector(a, b, 0, 1, 2, 3, 4, 5, 6, 7));
}
DI bf8 trfrag3(const AS3 char* p0, const AS3 char* p1) {
  s4 a = __builtin_amdgcn_ds_read_tr16_b64_v4i16((AS3 s4*)p0);
  s4 b = __builtin_amdgcn_ds_read_tr16_b64_v4i16((AS3 s4*)p1);
  return __builtin_bit_cast(bf8, __builtin_shufflevector(a, b, 0, 1, 2, 3, 4, 5, 6, 7));
}
DI float phi(float t) {
  return fmaxf(t, 0.f) + 1e-6f + __builtin_amdgcn_exp2f(fminf(t, 0.f) * 1.44269504088896341f);
}
#define MT(i) do {} while (0)
#define WAITV(n) asm volatile("s_waitcnt vmcnt(" #n ")" ::: "memory")
#define WAITL0() asm volatile("s_waitcnt lgkmcnt(0)" ::: "memory")

typedef __amdgpu_buffer_rsrc_t rsrc_t;
DI rsrc_t mkrsrc(const void* p, unsigned bytes) { return __builtin_amdgcn_make_buffer_rsrc((void*)p, 0, bytes, 0x00020000); }
template <int AUX>
DI void blds16(rsrc_t r, char* l, int voff, int soff) {
  __builtin_amdgcn_raw_ptr_buffer_load_lds(r, (AS3 void*)l, 16, voff, soff, 0, AUX);
}
DI u4 ld16(rsrc_t r, int voff, int soff) { return __builtin_amdgcn_raw_buffer_load_b128(r, voff, soff, 16); }
DI void st16(u4 v, rsrc_t r, int voff, int soff) { __builtin_amdgcn_raw_buffer_store_b128(v, r, voff + soff, 0, 16); }
DI void pst16(u4 v, rsrc_t r, int voff, int soff) { __builtin_amdgcn_raw_buffer_store_b128(v, r, voff + soff, 0, 0); }
DI void pst8(u2 v, rsrc_t r, int voff, int soff) { __builtin_amdgcn_raw_buffer_store_b64(v, r, voff, soff, 0); }
DI void st8(u2 v, rsrc_t r, int voff, int soff) { __builtin_amdgcn_raw_buffer_store_b64(v, r, voff, soff, 16); }
DI void st4(unsigned v, rsrc_t r, int voff, int soff) { __builtin_amdgcn_raw_buffer_store_b32(v, r, voff, soff, 16); }
DI unsigned ld4(rsrc_t r, int voff, int soff) { return __builtin_amdgcn_raw_buffer_load_b32(r, voff, soff, 16); }
DI float wave_sum(float v) {
#define DPP_ADD(c) v += __builtin_bit_cast(float, __builtin_amdgcn_update_dpp(0, __builtin_bit_cast(int, v), c, 0xf, 0xf, true))
  DPP_ADD(0x111); DPP_ADD(0x112); DPP_ADD(0x114); DPP_ADD(0x118);
#undef DPP_ADD
  const int u = __builtin_bit_cast(int, v);
  return (__builtin_bit_cast(float, __builtin_amdgcn_readlane(u, 15)) + __builtin_bit_cast(float, __builtin_amdgcn_readlane(u, 31))) +
         (__builtin_bit_cast(float, __builtin_amdgcn_readlane(u, 47)) + __builtin_bit_cast(float, __builtin_amdgcn_readlane(u, 63)));
}
constexpr unsigned SPIN = 1u << 20;
#define PSLEEP 8
DI void wait16(rsrc_t rs, int voff, int soff, unsigned tag) {
#pragma unroll 1
  for (unsigned spin = 0; spin < SPIN; ++spin) {
    asm volatile("" : "+v"(voff) :: "memory");
    u4 v = ld16(rs, voff, soff);
    if (__all(v[0] == tag && v[1] == tag && v[2] == tag && v[3] == tag)) break;
    __builtin_amdgcn_s_sleep(PSLEEP);
  }
  asm volatile("" ::: "memory");
  __builtin_amdgcn_fence(__ATOMIC_ACQUIRE, "wavefront");
}
DI void wait4(rsrc_t rs, int voff, int soff, unsigned tag) {
#pragma unroll 1
  for (unsigned spin = 0; spin < SPIN; ++spin) {
    asm volatile("" : "+v"(voff) :: "memory");
    if (__all(ld4(rs, voff, soff) == tag)) break;
    __builtin_amdgcn_s_sleep(PSLEEP);
  }
  asm volatile("" ::: "memory");
  __builtin_amdgcn_fence(__ATOMIC_ACQUIRE, "wavefront");
}

DI void wait_lds(char* p, unsigned lvl) {
#pragma unroll 1
  for (unsigned spin = 0; spin < SPIN; ++spin) {
    if (__hip_atomic_load((AS3 unsigned*)p, __ATOMIC_RELAXED, __HIP_MEMORY_SCOPE_WORKGROUP) >= lvl) break;
    __builtin_amdgcn_s_sleep(1);
  }
  asm volatile("" ::: "memory");
  __builtin_amdgcn_fence(__ATOMIC_ACQUIRE, "wavefront");
}
DI void set_lds(char* p, unsigned lvl) { __hip_atomic_store((AS3 unsigned*)p, lvl, __ATOMIC_RELAXED, __HIP_MEMORY_SCOPE_WORKGROUP); }

constexpr int A_BYTES = 128 * 128;
constexpr int L_Q = 3 * A_BYTES;
constexpr int L_CB = L_Q + 3 * 64 * 448;
constexpr int L_ROW = L_CB + 20480;
constexpr int K1_LDS = L_ROW + 8192;

template <bool KV>
DI void k1_body(const __bf16* __restrict__ x, const __bf16* __restrict__ wqkv, const __bf16* __restrict__ w1,
                const __bf16* __restrict__ w2, char* ws, char* smem, int rb, int ci, int bid) {
  constexpr int NQ = KV ? 20 : 12, NH = KV ? 8 : 16;
  constexpr int QB = 64 * NQ * 16, HB = 64 * NH * 16;
  constexpr int L_H = L_Q + 3 * QB;
  constexpr int JQ = KV ? 5 : 3;
  const int tid = threadIdx.x, lane = tid & 63;
  const int w = __builtin_amdgcn_readfirstlane(tid >> 6), wm = (w >> 1) & 1, wn = w & 1;
  const int g = lane >> 4, l15 = lane & 15, q4 = l15 >> 2, p4 = lane & 3;
  const rsrc_t rs = mkrsrc(ws, WS_END);
  const f4 zero4 = {0.f, 0.f, 0.f, 0.f};
  f4 acc[4][7];
  if (w >= 4) {
    const int lw = w - 4;
    __builtin_amdgcn_s_setprio(3);
    const rsrc_t rx = mkrsrc(x, S_ * D_ * 2), rq = mkrsrc(wqkv, D_ * N3 * 2), r1 = mkrsrc(w1, D_ * DFF * 2);
    int vo[11];
#pragma unroll
    for (int n = 0; n < 4; ++n) {
      const int row = (n * 4 + lw) * 8 + (lane >> 3);
      vo[n] = (rb * 128 + row) * (D_ * 2) + (((lane & 7) ^ ((row >> 1) & 7)) << 4);
      if (n == 0) { for (int m = 1; m < 4; ++m) vo[m] = vo[0] + m * 32 * D_ * 2; break; }
    }
    auto ldA = [&](int stg, int t) {
#pragma unroll
      for (int n = 0; n < 4; ++n) blds16<0>(rx, smem + stg * A_BYTES + n * 4096 + lw * 1024, vo[n], t * 128);
    };
    ldA(0, 0);
#pragma unroll
    for (int n = 0; n < 7; ++n) {
      if (n < NQ / 4) {
        const int c = (n * 4 + lw) * 64 + lane, k = c / NQ, ch = (c - k * NQ) ^ ((k >> 2) & 2);
        vo[4 + n] = (k * N3 + 80 * ci + (KV ? (ch >= 10 ? 1200 : 640) + 8 * ch : 8 * (ch >= 10 ? ch - 2 : ch))) * 2;
      } else {
        const int c = ((n - NQ / 4) * 4 + lw) * 64 + lane, k = c / NH;
        const int f = KV ? (((k >> 1) & 1) | ((k >> 2) & 2)) : ((k & 3) | ((k >> 1) & 4));
        vo[4 + n] = (k * DFF + (KV ? 1024 + 64 * ci : 128 * ci) + 8 * ((c - k * NH) ^ (f << 1))) * 2;
      }
    }
    auto ldB = [&](int stg, int t) {
#pragma unroll
      for (int n = 0; n < 7; ++n) {
        if (n < NQ / 4) blds16<0>(rq, smem + L_Q + stg * QB + n * 4096 + lw * 1024, vo[4 + n], t * (64 * N3 * 2));
        else blds16<0>(r1, smem + L_H + stg * HB + (n - NQ / 4) * 4096 + lw * 1024, vo[4 + n], t * (64 * DFF * 2));
      }
    };
    ldB(0, 0);
    ldA(1, 1);
    ldB(1, 1);
    const unsigned tag = *(const unsigned*)(ws + WS_EP) + 1u;
    const int cl = bid % 54, cq = bid / 54, wks = bid / 5, wcb = bid - wks * 5;
    u4 xa[2], xb[2];
    {
      const bool q = cl < 30;
      const rsrc_t rc = mkrsrc(q ? wqkv : w1, bid < 216 ? (q ? D_ * N3 * 2 : D_ * DFF * 2) : 0);
      const int st = q ? N3 * 2 : DFF * 2;
      const int vb = (cq * 160 + lw * 8 + (lane >> 3)) * st + (q ? cl : cl - 30) * 128 + (lane & 7) * 16;
#pragma unroll
      for (int n = 0; n < 5; ++n) blds16<0>(rc, smem + L_CB + n * 4096 + lw * 1024, vb, n * 32 * st);
      const rsrc_t r2 = mkrsrc(w2, bid < 240 ? DFF * D_ * 2 : 0);
#pragma unroll
      for (int n = 0; n < 2; ++n) blds16<0>(r2, smem + L_ROW + n * 4096 + lw * 1024, ((wks * 32 + lw * 4 + g) * D_ + wcb * 128 + l15 * 8) * 2, n * 16 * D_ * 2);
      const char* xr = (const char*)x + (bid * 8 + lw * 2) * (D_ * 2);
      const char *p0 = xr + lane * 16, *p1 = xr + 1024 + l15 * 16;
      asm volatile("global_load_dwordx4 %0, %4, off\n\tglobal_load_dwordx4 %1, %5, off\n\tglobal_load_dwordx4 %2, %4, off offset:1280\n\t"
                   "global_load_dwordx4 %3, %5, off offset:1280" : "=&v"(xa[0]), "=&v"(xb[0]), "=&v"(xa[1]), "=&v"(xb[1]) : "v"(p0), "v"(p1) : "memory");
    }
    int voG[2];
#pragma unroll
    for (int n = 0; n < 2; ++n) {
      const int c = (n * 4 + lw) * 64 + lane, qq = c / 112, rem = c - qq * 112, tl = rem >> 3, cw = tl >= 7, j = tl - 7 * cw, pr = (rem & 7) * 2;
      int col;
      if (KV) { int cc = j * 16 + pr; col = j < 5 ? 640 + 80 * ci + 40 * cw + (cc >= 40 ? 600 + cc : cc) : N3 + 1024 + 64 * ci + 32 * cw + cc - 80; }
      else col = j < 3 ? 80 * ci + 48 * cw + 16 * (cw && j == 2 ? 1 : j) + pr : N3 + 128 * ci + 64 * cw + 16 * (j - 3) + pr;
      voG[n] = c < 448 ? WS_CG + (qq * 3456 + col) * 8 : WS_SG + rb * 1024 + lane * 16;
    }
    WAITV(22);
    __builtin_amdgcn_s_barrier();
    ldA(2, 2);
    ldB(2, 2);
    asm volatile("s_waitcnt vmcnt(11)" : "+v"(xa[0]), "+v"(xb[0]), "+v"(xa[1]), "+v"(xb[1]) :: "memory");
    __builtin_amdgcn_s_barrier();
    ldA(0, 3);
    ldB(0, 3);
    int stg = 1;
#pragma unroll 1
    for (int t = 1; t < 10; ++t) {
      if (t > 1) {
        if (t < 9) WAITV(11); else WAITV(0);
        __builtin_amdgcn_s_barrier();
        __builtin_amdgcn_s_setprio(3);
        if (t == 7) {
#pragma unroll
          for (int n = 0; n < 2; ++n) blds16<16>(rs, smem + L_ROW + n * 4096 + lw * 1024, voG[n], 0);
        }
        if (t < 8) { ldA(stg, t + 2); ldB(stg, t + 2); }
        stg = stg == 2 ? 0 : stg + 1;
      }
      __builtin_amdgcn_s_setprio(0);
      if (t == 1) {
        float su[2] = {0.f, 0.f}, sq[2] = {0.f, 0.f}, sh[2];
#pragma unroll
        for (int n = 0; n < 2; ++n) {
          sh[n] = lo16(__builtin_amdgcn_readfirstlane(xa[n][0]));
          float s2 = 0.f, q2 = 0.f;
#pragma unroll
          for (int e = 0; e < 4; ++e) {
            const float a0 = lo16(xa[n][e]) - sh[n], a1 = hi16(xa[n][e]) - sh[n], b0 = lo16(xb[n][e]) - sh[n], b1 = hi16(xb[n][e]) - sh[n];
            su[n] += a0 + a1; sq[n] += a0 * a0; sq[n] += a1 * a1; s2 += b0 + b1; q2 += b0 * b0; q2 += b1 * b1;
          }
          if (lane < 16) { su[n] += s2; sq[n] += q2; }
        }
        const float m0 = wave_sum(su[0]) * (1.f / 640.f), m1 = wave_sum(su[1]) * (1.f / 640.f);
        const float e0 = wave_sum(sq[0]) * (1.f / 640.f), e1 = wave_sum(sq[1]) * (1.f / 640.f);
        const float m = lane ? m1 : m0, var = fmaxf((lane ? e1 : e0) - m * m, 0.f);
        const float sd = __builtin_amdgcn_sqrtf(bfr(bfr(var) + 1e-5f));
        if (lane < 2) st8(u2{pk2(m + (lane ? sh[1] : sh[0]), sd), tag}, rs, lane * 8, WS_SG + (bid * 8 + lw * 2) * 8);
        if (tid == 256) { st4(tag, rs, 0, WS_FW + bid * 4); __builtin_amdgcn_raw_buffer_store_b32(0u, rs, 0, WS_F2 + bid * 128, 0); }
      } else if (t == 2) {
        const bf8 ones = __builtin_bit_cast(bf8, u4{0x3f803f80u, 0x3f803f80u, 0x3f803f80u, 0x3f803f80u});
        const char* c0 = smem + L_CB + (8 * g + q4) * 128 + lw * 32 + 8 * p4;
        f4 c = zero4;
#pragma unroll
        for (int k = 0; k < 5; ++k) c = mfma(ones, trfrag(c0 + k * 4096, c0 + k * 4096 + 512), c);
        if (g == 0 && bid < 216) st8(u2{__builtin_bit_cast(unsigned, c[0]), tag}, rs, l15 * 8, WS_CG + (cq * 3456 + cl * 64 + lw * 16) * 8);
      } else if (t == 3) {
#pragma unroll
        for (int n = 0; n < 2; ++n) {
          const char* p0 = smem + L_ROW + (4 * g + q4) * 256 + (lw * 2 + n) * 32 + 8 * p4;
          const bf8 f = trfrag(p0, p0 + 4096);
          const int col = wcb * 128 + (lw * 2 + n) * 16, cg = col / 80;
          if (bid < 240) pst16(__builtin_bit_cast(u4, f), rs, lane * 16, WS_W2 + ((cg * 48 + wks) * 5 + ((col - cg * 80) >> 4)) * 1024);
        }
      }
      WAITL0();
    }
#pragma unroll
    for (int n = 0; n < 2; ++n) {
#pragma unroll 1
      for (unsigned spin = 0; spin < SPIN; ++spin) {
        const u4 v = *(const u4*)(smem + L_ROW + n * 4096 + lw * 1024 + lane * 16);
        if (__all(v[1] == tag && v[3] == tag)) break;
        __builtin_amdgcn_s_sleep(1);
        asm volatile("" : "+v"(voG[n]) :: "memory");
        *(u4*)(smem + L_ROW + n * 4096 + lw * 1024 + lane * 16) = ld16(rs, voG[n], 0);
      }
    }
    WAITL0();
    __builtin_amdgcn_s_barrier();
    if (bid == 0 && w == 7) {
      wait16(rs, lane * 16, WS_FW, tag);
      if (lane == 0) st4(tag, rs, 0, WS_EP);
    }
  } else {
    unsigned ra[2], rb_[7];
    {
      const unsigned lb = (unsigned)(size_t)(AS3 char*)smem;
      const int f = KV ? (((q4 >> 1) & 1) | ((g & 1) << 1)) : (q4 | ((g & 1) << 2));
#pragma unroll
      for (int s = 0; s < 2; ++s) ra[s] = lb + (wm * 64 + l15) * 128 + (((s * 4 + g) ^ (l15 >> 1)) << 4);
#pragma unroll
      for (int j = 0; j < 7; ++j) {
        const int cc = 2 * j + (p4 >> 1);
        const int pos = KV ? wn * 5 + cc + (cc >= 5 ? 5 : 0) : wn * 6 + cc;
        rb_[j] = lb + (j < JQ ? L_Q + (8 * g + q4) * (NQ * 16) + ((pos ^ ((g & 1) << 1)) << 4) + 8 * (p4 & 1)
                              : L_H + (8 * g + q4) * (NH * 16) + (((wn * (7 - JQ) + j - JQ) ^ f) << 5) + 8 * p4);
      }
    }
#pragma unroll
    for (int i = 0; i < 4; ++i)
#pragma unroll
      for (int j = 0; j < 7; ++j) acc[i][j] = zero4;
    bf8 fa[2][4], fb[2][7];
    s4 hb[2][7][2];
    auto rd1 = [&](int s, int stg, int n) {
      if (n < 4) fa[s][n] = *(const AS3 bf8*)(size_t)(ra[s] + stg * A_BYTES + n * 2048);
      else {
        const int j = (n - 4) >> 1, h = (n - 4) & 1;
        const AS3 char* p = (const AS3 char*)(size_t)rb_[j] + (j < JQ ? stg * QB + (s * 32 + 4 * h) * NQ * 16 : stg * HB + (s * 32 + 4 * h) * NH * 16);
        hb[s][j][h] = __builtin_amdgcn_ds_read_tr16_b64_v4i16((AS3 s4*)p);
        if (h) fb[s][j] = __builtin_bit_cast(bf8, __builtin_shufflevector(hb[s][j][0], hb[s][j][1], 0, 1, 2, 3, 4, 5, 6, 7));
      }
    };
    auto mm1 = [&](int s, int n) {
      const int j = n >> 2, i = n & 3;
      if (KV && j < 5) asm volatile("v_mfma_f32_16x16x32_bf16 %0, %1, %2, %0" : "+v"(acc[i][j]) : "v"(fa[s][i]), "v"(fb[s][j]));
      else asm volatile("v_mfma_f32_16x16x32_bf16 %0, %1, %2, %0" : "+v"(acc[i][j]) : "v"(fb[s][j]), "v"(fa[s][i]));
    };
    auto phase = [&](int sm, int sr, int stg, bool domm, bool dord) {
#pragma unroll
      for (int n = 0; n < 28; ++n) {
        if (domm) mm1(sm, n);
        if (dord && n < 18) rd1(sr, stg, n);
        __builtin_amdgcn_sched_barrier(0);
      }
      if (domm)
        asm volatile("" :: "v"(fa[sm][0]), "v"(fa[sm][1]), "v"(fa[sm][2]), "v"(fa[sm][3]), "v"(fb[sm][0]), "v"(fb[sm][1]), "v"(fb[sm][2]),
                     "v"(fb[sm][3]), "v"(fb[sm][4]), "v"(fb[sm][5]), "v"(fb[sm][6]));
    };
    auto body = [&](int stg, bool first) {
      __builtin_amdgcn_s_barrier();
      __builtin_amdgcn_sched_barrier(0);
      phase(1, 0, stg, !first, true);
      phase(0, 1, stg, true, true);
      WAITL0();
    };
#pragma unroll
    for (int j = 0; j < 7; ++j) asm volatile("" : "+v"(acc[0][j]), "+v"(acc[1][j]), "+v"(acc[2][j]), "+v"(acc[3][j]));
    STAMP(0, 1);
    body(0, true);
    STAMP(0, 2);
#pragma unroll 1
    for (int t = 0; t < 3; ++t) {
      body(1, false);
      body(2, false);
      body(0, false);
    }
    phase(1, 0, 0, true, false);
    asm volatile("s_nop 15" ::: "memory");
    __builtin_amdgcn_sched_barrier(0);
    STAMP(0, 4);
    __builtin_amdgcn_s_barrier();
    const char* sg = smem + L_ROW + 7168 + wm * 512;
    unsigned ms[4];
    bf8 am[4];
#pragma unroll
    for (int i = 0; i < 4; ++i) {
      ms[i] = *(const unsigned*)(sg + (i * 16 + l15) * 8);
      const unsigned nm = (ms[i] & 0xffffu) ^ 0x8000u;
      am[i] = __builtin_bit_cast(bf8, u4{nm | (nm << 16), nm, 0u, 0u});
    }
#pragma unroll
    for (int j = 0; j < 7; ++j) {
      const float c = *(const float*)(smem + L_ROW + (g * 224 + (wn * 7 + j) * 16 + l15) * 8);
      const float r1 = c - bfr(c), r2 = r1 - bfr(r1);
      const bf8 bm = __builtin_bit_cast(bf8, u4{pk2(c, r1), pk2(r2, 0.f), 0u, 0u});
#pragma unroll
      for (int i = 0; i < 4; ++i) acc[i][j] = (KV && j < 5) ? mfma(am[i], bm, acc[i][j]) : mfma(bm, am[i], acc[i][j]);
    }
    STAMP(0, 5);
    const int rt0 = rb * 8 + wm * 4;
    typedef __attribute__((ext_vector_type(2))) short s2_t;
    auto relu2 = [&](float a, float b) {
      s2_t v = __builtin_bit_cast(s2_t, pk2(a, b));
      return __builtin_bit_cast(unsigned, __builtin_elementwise_max(v, s2_t{0, 0}));
    };
#pragma unroll
    for (int i = 0; i < 4; ++i) {
      const float rstd = __builtin_amdgcn_rcpf(hi16(ms[i]));
#pragma unroll
      for (int j = JQ; j < 7; j += 2) {
        const int ks = (KV ? 1024 + 64 * ci + 32 * wn : 128 * ci + 64 * wn + 16 * (j - JQ)) >> 5;
        const f4 y0 = acc[i][j] * rstd, y1 = acc[i][j + 1] * rstd;
        pst16(u4{relu2(y0[0], y0[1]), relu2(y0[2], y0[3]), relu2(y1[0], y1[1]), relu2(y1[2], y1[3])}, rs, lane * 16,
              WS_HF + ((rt0 + i) * 48 + ks) * 1024);
      }
      if (!KV) {
#pragma unroll
        for (int j = 0; j < 3; ++j) {
          if (j == 2 && wn) break;
          const int col = 80 * ci + 48 * wn + 16 * j;
          const f4 y = acc[i][j] * rstd;
          pst8(u2{pk2(phi(y[0]), phi(y[1])), pk2(phi(y[2]), phi(y[3]))}, rs, lane * 16,
               WS_PQ + ((rt0 + i) * 20 + (col >> 5)) * 1024 + ((col >> 4) & 1) * 8);
        }
      }
    }
    STAMP(0, 6);
    if (KV) {
      f4 kv[8];
#pragma unroll
      for (int hp = 0; hp < 2; ++hp) {
        u4 fr[6];
#pragma unroll
        for (int ii = 0; ii < 2; ++ii) {
          const int i = hp * 2 + ii;
          const u4 s0 = *(const u4*)(sg + (i * 16 + 4 * g) * 8), s1 = *(const u4*)(sg + (i * 16 + 4 * g) * 8 + 16);
          const f4 rr = {__builtin_amdgcn_rcpf(hi16(s0[0])), __builtin_amdgcn_rcpf(hi16(s0[2])), __builtin_amdgcn_rcpf(hi16(s1[0])),
                         __builtin_amdgcn_rcpf(hi16(s1[2]))};
#pragma unroll
          for (int j = 0; j < 5; ++j) {
            f4 y = acc[i][j] * rr;
            if (j < 3) {
#pragma unroll
              for (int e = 0; e < 4; ++e) y[e] = (j < 2 || l15 < 8) ? phi(y[e]) : y[e];
            }
            fr[j][2 * ii] = pk2(y[0], y[1]);
            fr[j][2 * ii + 1] = pk2(y[2], y[3]);
          }
        }
#pragma unroll
        for (int e = 0; e < 4; ++e) fr[5][e] = l15 < 8 ? 0x3f803f80u : fr[2][e];
        const int ta[8] = {0, 0, 1, 1, 1, 2, 2, 2}, tb[8] = {5, 3, 5, 3, 4, 5, 3, 4};
#pragma unroll
        for (int m = 0; m < 8; ++m) kv[m] = mfma(__builtin_bit_cast(bf8, fr[tb[m]]), __builtin_bit_cast(bf8, fr[ta[m]]), hp ? kv[m] : zero4);
      }
#pragma unroll
      for (int m = 0; m < 8; ++m) *(f4*)(smem + (w * 8 + m) * 1024 + lane * 16) = kv[m];
      WAITL0();
    }
  }
  if (KV) {
    __builtin_amdgcn_s_barrier();
    if (tid < 480) {
      const bool isz = tid >= 400;
      int hl = isz ? (tid - 400) / 20 : tid / 100, rem = isz ? (tid - 400 - hl * 20) * 5 : tid - hl * 100, d = rem / 5, grp = rem - d * 5;
      int hk = hl & 1, kc = 20 * hk + d, a = kc >> 4;
      int pos = isz ? 32 : 40 + 20 * hk + 4 * grp, b = pos >> 4;
      int m = a == 0 ? b - 2 : (a == 1 ? b : b + 3);
      const char* p = smem + ((hl >> 1) * 8 + m) * 1024 + (((pos >> 2) & 3) * 16 + (kc & 15)) * 16;
      f4 v = *(const f4*)p + *(const f4*)(p + 16384);
      if (isz) __builtin_amdgcn_raw_buffer_store_b32(__builtin_bit_cast(unsigned, v[0]), rs, (tid - 400) * 4, WS_Z + (rb * 640 + ci * 80) * 4, 0);
      else pst16(__builtin_bit_cast(u4, v), rs, tid * 16, WS_KV + (rb * 32 + ci * 4) * 1600);
    }
  }
  STAMP(0, 7);
}

__global__ __launch_bounds__(512, 2) void k1_kernel(const __bf16* __restrict__ x, const __bf16* __restrict__ wqkv,
                                                    const __bf16* __restrict__ w1, const __bf16* __restrict__ w2, char* ws) {
  extern __shared__ __attribute__((aligned(16))) char smem[];
  const int bid = blockIdx.x, xcd = bid & 7, idx = bid >> 3;
  STAMP(0, 0);
  const int rb = (xcd >> 1) * 4 + (idx & 3), cgi = idx >> 2;
  const int ci = (xcd & 1) * 4 + (cgi >> 1);
  if (cgi & 1) k1_body<false>(x, wqkv, w1, w2, ws, smem, rb, ci, bid);
  else k1_body<true>(x, wqkv, w1, w2, ws, smem, rb, ci, bid);
}

template <int AUX>
DI bf8 ldfrag(rsrc_t r, int voff, int soff) {
  return __builtin_bit_cast(bf8, __builtin_amdgcn_raw_buffer_load_b128(r, voff, soff, AUX));
}
DI void st8_sc1(char* p, unsigned a, unsigned b) {
  __hip_atomic_store((AS1 unsigned long long*)p, (unsigned long long)a | ((unsigned long long)b << 32), __ATOMIC_RELAXED,
                     __HIP_MEMORY_SCOPE_AGENT);
}
constexpr int L2_ACC = 0;
constexpr int L2_WO = 61440;
constexpr int L2_KV = L2_WO + 5120;
constexpr int L2_Z = L2_KV + 2048;
constexpr int L2_CNT = L2_Z + 2048;
constexpr int K2_LDS = 98304;

#define RING 3
DI void k2_publish(char* ws, char* smem, int flag, int lane) {
  WAITV(0);
  if (lane == 0) {
    unsigned old = __hip_atomic_fetch_add((AS3 unsigned*)(smem + L2_CNT), 1u, __ATOMIC_RELAXED, __HIP_MEMORY_SCOPE_WORKGROUP);
    if (old == 2u) __hip_atomic_store((AS1 unsigned*)(ws + WS_F2 + flag * 128), 1u, __ATOMIC_RELAXED, __HIP_MEMORY_SCOPE_AGENT);
  }
}
__global__ __launch_bounds__(512, 2) void k2_kernel(const __bf16* __restrict__ x, const __bf16* __restrict__ wo,
                                                    char* ws, __bf16* __restrict__ out) {
  extern __shared__ __attribute__((aligned(16))) char smem[];
  const int tid = threadIdx.x, lane = tid & 63, w = __builtin_amdgcn_readfirstlane(tid >> 6);
  const int g = lane >> 4, l15 = lane & 15;
  const int xcd = blockIdx.x & 7, idx = blockIdx.x >> 3;
  const int r = (xcd >> 1) * 8 + (idx & 7), c = (xcd & 1) * 4 + (idx >> 3);
  STAMP(1, 0);
  if (tid == 0) *(unsigned*)(smem + L2_CNT) = 0u;
  WAITL0();
  __builtin_amdgcn_s_barrier();
  const rsrc_t rs = mkrsrc(ws, (unsigned)WS_END);
  const int vo = lane * 16;
  const f4 zero4 = {0.f, 0.f, 0.f, 0.f};
  if (w >= 4) __builtin_amdgcn_s_setprio(3);
  if (w == 4) {
    const f4* p = (const f4*)(ws + WS_KV) + r * 100;
    const int i1 = lane < 36 ? lane + 64 : lane;
    f4 v0[16], v1[16];
#pragma unroll
    for (int i = 0; i < 16; ++i) { v0[i] = p[i * 3200 + lane]; v1[i] = p[i * 3200 + i1]; }
    u4 wv[4];
#pragma unroll
    for (int i = 0; i < 4; ++i) {
      int ch = i * 64 + lane, row = ch / 10;
      wv[i] = u4{0u, 0u, 0u, 0u};
      if (ch < 200) wv[i] = *(const u4*)(wo + (20 * r + row) * D_ + 80 * c + (ch - row * 10) * 8);
    }
#pragma unroll
    for (int st = 1; st < 16; st *= 2)
#pragma unroll
      for (int i = 0; i < 16; i += 2 * st) { v0[i] += v0[i + st]; v1[i] += v1[i + st]; }
    *(f4*)(smem + L2_KV + vo) = v0[0];
    *(f4*)(smem + L2_KV + 1024 + vo) = v1[0];
#pragma unroll
    for (int i = 0; i < 4; ++i) *(u4*)(smem + L2_WO + i * 1024 + vo) = wv[i];
    *(u4*)(smem + L2_WO + 4096 + vo) = u4{0u, 0u, 0u, 0u};
    u4 au[2];
#pragma unroll
    for (int dt = 0; dt < 2; ++dt)
#pragma unroll
      for (int hh = 0; hh < 2; ++hh) {
        int d = dt * 16 + l15, grp = 2 * g + hh;
        bool ok = d < 20 && grp < 5;
        f4 v = *(const f4*)(smem + L2_KV + (ok ? d * 5 + grp : 0) * 16);
        au[dt][2 * hh] = ok ? pk2(v[0], v[1]) : 0u;
        au[dt][2 * hh + 1] = ok ? pk2(v[2], v[3]) : 0u;
      }
    const int k0 = 20 * r + 4 * g, k1 = 20 * r + 16;
    const char* p0 = smem + L2_WO + (8 * g + (l15 >> 2)) * 160 + (lane & 3) * 8;
    char* mf = ws + WS_MF + c * (20 * 5120) + l15 * 16;
    const int o0 = (k0 >> 5) * 5120 + ((k0 >> 2) & 3) * 256 + ((k0 >> 4) & 1) * 8;
    const int o1 = (k1 >> 5) * 5120 + ((k1 >> 2) & 3) * 256 + ((k1 >> 4) & 1) * 8;
#pragma unroll
    for (int t = 0; t < 5; ++t) {
      bf8 b = trfrag(p0 + t * 32, p0 + t * 32 + 640);
      f4 m0 = mfma(__builtin_bit_cast(bf8, au[0]), b, zero4), m1 = mfma(__builtin_bit_cast(bf8, au[1]), b, zero4);
      st8_sc1(mf + t * 1024 + o0, pk2(m0[0], m0[1]), pk2(m0[2], m0[3]));
      if (g == 0) st8_sc1(mf + t * 1024 + o1, pk2(m1[0], m1[1]), pk2(m1[2], m1[3]));
    }
    k2_publish(ws, smem, r * 8 + c, lane);
    STAMPW(1, 8);
  } else if (w == 5 || w == 6) {
    const int fo = ((r * 4 + (c & 1) * 2 + w - 5) * 20 + (c >> 1) * 5) * 1024;
    const f4* zp = (const f4*)(ws + WS_Z) + (c >> 1) * 40 + (lane < 40 ? lane : 0);
    f4 zv[16];
#pragma unroll
    for (int i = 0; i < 16; ++i) zv[i] = zp[i * 160];
    u4 pq[5];
#pragma unroll
    for (int k = 0; k < 5; ++k) pq[k] = __builtin_bit_cast(u4, ldfrag<0>(rs, vo, (int)WS_PQ + fo + k * 1024));
#pragma unroll
    for (int st = 1; st < 16; st *= 2)
#pragma unroll
      for (int i = 0; i < 16; i += 2 * st) zv[i] += zv[i + st];
    char* zl = smem + L2_Z + (w - 5) * 1024;
    if (lane < 40) *(u2*)(zl + lane * 8) = u2{pk2(zv[0][0], zv[0][1]), pk2(zv[0][2], zv[0][3])};
    f4 den = zero4;
#pragma unroll
    for (int k = 0; k < 5; ++k) {
      u4 zf;
#pragma unroll
      for (int p = 0; p < 2; ++p) {
        int gi = 8 * k + 4 * p + g;
        u2 z2 = *(const u2*)(zl + gi * 8);
        bool mine = gi / 5 == l15;
        zf[2 * p] = mine ? z2[0] : 0u;
        zf[2 * p + 1] = mine ? z2[1] : 0u;
      }
      den = mfma(__builtin_bit_cast(bf8, zf), __builtin_bit_cast(bf8, pq[k]), den);
    }
    f4 rd;
#pragma unroll
    for (int e = 0; e < 4; ++e) rd[e] = __builtin_amdgcn_rcpf(bfr(bfr(den[e]) + 1e-6f));
    if (g < 2) *(f4*)(zl + 512 + l15 * 32 + g * 16) = rd;
#pragma unroll
    for (int k = 0; k < 5; ++k) {
      u4 o;
#pragma unroll
      for (int p = 0; p < 2; ++p) {
        float s = *(const float*)(zl + 512 + l15 * 32 + ((8 * k + 4 * p + g) / 5) * 4);
        o[2 * p] = pk2(lo16(pq[k][2 * p]) * s, hi16(pq[k][2 * p]) * s);
        o[2 * p + 1] = pk2(lo16(pq[k][2 * p + 1]) * s, hi16(pq[k][2 * p + 1]) * s);
      }
      st16(o, rs, vo, WS_AF + fo + k * 1024);
    }
    k2_publish(ws, smem, r * 8 + c, lane);
    STAMPW(1, 9);
  }
  if (w >= 4) {
    __builtin_amdgcn_s_barrier();
    return;
  }
  const int hA = (int)WS_HF + (r * 4 * 48 + w) * 1024, hB = (int)WS_W2 + (c * 48 + w) * 5120;
  const int aA = (int)WS_AF + (r * 4 * 20 + w) * 1024, aB = (int)WS_MF + (c * 20 + w) * 5120;
#define ROT 0
  const int rot = ROT ? ((idx >> 3) * 3 + (idx & 7) * 5) % 12 : 0, rot2 = ROT ? idx % 5 : 0;
  bf8 fa[RING][4], fb[RING][5];
  auto ld = [&](int i) {
    const int sl = i % RING;
    if (i < 12) {
      int ii = i + rot;
      ii = ii >= 12 ? ii - 12 : ii;
#pragma unroll
      for (int j = 0; j < 5; ++j) fb[sl][j] = ldfrag<0>(rs, vo + j * 1024, hB + ii * 20480);
#pragma unroll
      for (int j = 0; j < 4; ++j) fa[sl][j] = ldfrag<0>(rs, vo, hA + ii * 4096 + j * 49152);
    } else {
      int ii = i - 12 + rot2;
      ii = ii >= 5 ? ii - 5 : ii;
#pragma unroll
      for (int j = 0; j < 5; ++j) fb[sl][j] = ldfrag<16>(rs, vo + j * 1024, aB + ii * 20480);
#pragma unroll
      for (int j = 0; j < 4; ++j) fa[sl][j] = ldfrag<16>(rs, vo, aA + ii * 4096 + j * 20480);
    }
  };
#pragma unroll
  for (int i = 0; i < RING; ++i) ld(i);
  const int orow = (r * 64 + w * 16 + l15) * D_ + c * 80 + 4 * g;
  u2 xr[5];
#pragma unroll
  for (int j = 0; j < 5; ++j) xr[j] = *(const u2*)(x + orow + 16 * j);
  const AS1 unsigned* fl = (const AS1 unsigned*)(ws + WS_F2 + (lane < 8 ? r * 8 + lane : (lane < 40 ? (lane - 8) * 8 + c : r * 8 + c)) * 128);
  STAMP(1, 1);
  __builtin_amdgcn_sched_barrier(0);
  f4 acc[4][5];
#pragma unroll
  for (int i = 0; i < 4; ++i)
#pragma unroll
    for (int j = 0; j < 5; ++j) acc[i][j] = zero4;
  unsigned flv = 0;
#pragma unroll
  for (int i = 0; i < 17; ++i) {
    const int sl = i % RING;
#pragma unroll
    for (int j = 0; j < 5; ++j)
#pragma unroll
      for (int m = 0; m < 4; ++m) acc[m][j] = mfma(fb[sl][j], fa[sl][m], acc[m][j]);
    if (i + RING == 11) flv = __hip_atomic_load(fl, __ATOMIC_RELAXED, __HIP_MEMORY_SCOPE_AGENT);
    if (i + RING == 12) {
      for (unsigned spin = 0; !__all(flv == 1u) && spin < (1u << 22); ++spin) {
        __builtin_amdgcn_s_sleep(1);
        flv = __hip_atomic_load(fl, __ATOMIC_RELAXED, __HIP_MEMORY_SCOPE_AGENT);
      }
      __builtin_amdgcn_fence(__ATOMIC_ACQUIRE, "wavefront");
    }
    if (i + RING < 17) ld(i + RING);
    __builtin_amdgcn_sched_barrier(0);
  }
  STAMP(1, 6);
#pragma unroll
  for (int m = 0; m < 4; ++m) {
    if (m != w) {
      int slot = m < w ? m : m - 1;
#pragma unroll
      for (int j = 0; j < 5; ++j) *(f4*)(smem + L2_ACC + ((w * 3 + slot) * 5 + j) * 1024 + vo) = acc[m][j];
    }
  }
  WAITL0();
  __builtin_amdgcn_s_barrier();
  f4 own[5];
#pragma unroll
  for (int j = 0; j < 5; ++j) own[j] = w == 0 ? acc[0][j] : (w == 1 ? acc[1][j] : (w == 2 ? acc[2][j] : acc[3][j]));
  f4 tot[5];
#pragma unroll
  for (int j = 0; j < 5; ++j) tot[j] = f4{lo16(xr[j][0]), hi16(xr[j][0]), lo16(xr[j][1]), hi16(xr[j][1])};
#pragma unroll
  for (int s = 0; s < 4; ++s) {
    int slot = w < s ? w : w - 1;
#pragma unroll
    for (int j = 0; j < 5; ++j) {
      f4 v = own[j];
      if (s != w) v = *(const f4*)(smem + L2_ACC + ((s * 3 + slot) * 5 + j) * 1024 + vo);
      tot[j] += v;
    }
  }
#pragma unroll
  for (int j = 0; j < 5; ++j) *(u2*)(out + orow + 16 * j) = u2{pk2(tot[j][0], tot[j][1]), pk2(tot[j][2], tot[j][3])};
  STAMP(1, 7);
}

extern "C" void kernel_launch(void* const* d_in, const int* in_sizes, int n_in, void* d_out, int out_size, void* d_ws,
                              size_t ws_size, hipStream_t stream) {
  const __bf16* x = (const __bf16*)d_in[0];
  const __bf16* wqkv = (const __bf16*)d_in[1];
  const __bf16* wo = (const __bf16*)d_in[2];
  const __bf16* w1 = (const __bf16*)d_in[4];
  const __bf16* w2 = (const __bf16*)d_in[5];
  char* ws = (char*)d_ws;
  (void)hipFuncSetAttribute((const void*)k1_kernel, hipFuncAttributeMaxDynamicSharedMemorySize, K1_LDS);
  (void)hipFuncSetAttribute((const void*)k2_kernel, hipFuncAttributeMaxDynamicSharedMemorySize, K2_LDS);
  k1_kernel<<<256, 512, K1_LDS, stream>>>(x, wqkv, w1, w2, ws);
  k2_kernel<<<256, 512, K2_LDS, stream>>>(x, wo, ws, (__bf16*)d_out);
}
```

```cpp
#include <hip/hip_runtime.h>
#include <hip/hip_bf16.h>
#include <stdint.h>

typedef __attribute__((ext_vector_type(8))) __bf16 bf8;
typedef __attribute__((ext_vector_type(4))) __bf16 bf4;
typedef __attribute__((ext_vector_type(2))) __bf16 bf2;
typedef __attribute__((ext_vector_type(4))) short s4;
typedef __attribute__((ext_vector_type(4))) float f4;
typedef __attribute__((ext_vector_type(2))) float f2;
typedef __attribute__((ext_vector_type(4))) unsigned u4;
typedef __attribute__((ext_vector_type(2))) unsigned u2;
#define AS1 __attribute__((address_space(1)))
#define AS3 __attribute__((address_space(3)))
#define DI __device__ __forceinline__

constexpr int S_ = 2048, D_ = 640, DFF = 1536, N3 = 1920;
constexpr int WS_HF = 0;
constexpr int WS_PQ = WS_HF + 128 * 48 * 1024;
constexpr int WS_AF = WS_PQ + 128 * 20 * 1024;
constexpr int WS_W2 = WS_AF + 128 * 20 * 1024;
constexpr int WS_MF = WS_W2 + 8 * 48 * 5 * 1024;
constexpr int WS_KV = WS_MF + 8 * 20 * 5 * 1024;
constexpr int WS_Z = WS_KV + 16 * 32 * 400 * 4;
constexpr int WS_CG = WS_Z + 16 * 640 * 4;
constexpr int WS_SG = WS_CG + 4 * 3456 * 8;
constexpr int WS_FW = WS_SG + 2048 * 8;
constexpr int WS_F1 = WS_FW + 1024;
constexpr int WS_F2 = WS_F1 + 1024;
constexpr int WS_EP = WS_F2 + 256 * 128;
constexpr int WS_END = WS_EP + 128;
#define STAMPW(k, i) do {} while (0)
#define STAMP(k, i) do {} while (0)

DI float bfr(float v) { return (float)(__bf16)v; }
DI unsigned pk2(float a, float b) { f2 v = {a, b}; return __builtin_bit_cast(unsigned, __builtin_convertvector(v, bf2)); }
DI float lo16(unsigned u) { return __builtin_bit_cast(float, u << 16); }
DI float hi16(unsigned u) { return __builtin_bit_cast(float, u & 0xffff0000u); }
DI f4 mfma(bf8 a, bf8 b, f4 c) { return __builtin_amdgcn_mfma_f32_16x16x32_bf16(a, b, c, 0, 0, 0); }
DI void glds16(const void* g, char* l) {
  __builtin_amdgcn_global_load_lds((const AS1 void*)g, (AS3 void*)l, 16, 0, 0);
}
DI bf8 trfrag(const char* p0, const char* p1) {
  s4 a = __builtin_amdgcn_ds_read_tr16_b64_v4i16((AS3 s4*)p0);
  s4 b = __builtin_amdgcn_ds_read_tr16_b64_v4i16((AS3 s4*)p1);
  return __builtin_bit_cast(bf8, __builtin_shufflevector(a, b, 0, 1, 2, 3, 4, 5, 6, 7));
}
DI bf8 trfrag3(const AS3 char* p0, const AS3 char* p1) {
  s4 a = __builtin_amdgcn_ds_read_tr16_b64_v4i16((AS3 s4*)p0);
  s4 b = __builtin_amdgcn_ds_read_tr16_b64_v4i16((AS3 s4*)p1);
  return __builtin_bit_cast(bf8, __builtin_shufflevector(a, b, 0, 1, 2, 3, 4, 5, 6, 7));
}
DI float phi(float t) {
  return fmaxf(t, 0.f) + 1e-6f + __builtin_amdgcn_exp2f(fminf(t, 0.f) * 1.44269504088896341f);
}
#define MT(i) do {} while (0)
#define WAITV(n) asm volatile("s_waitcnt vmcnt(" #n ")" ::: "memory")
#define WAITL0() asm volatile("s_waitcnt lgkmcnt(0)" ::: "memory")

typedef __amdgpu_buffer_rsrc_t rsrc_t;
DI rsrc_t mkrsrc(const void* p, unsigned bytes) { return __builtin_amdgcn_make_buffer_rsrc((void*)p, 0, bytes, 0x00020000); }
template <int AUX>
DI void blds16(rsrc_t r, char* l, int voff, int soff) {
  __builtin_amdgcn_raw_ptr_buffer_load_lds(r, (AS3 void*)l, 16, voff, soff, 0, AUX);
}
DI u4 ld16(rsrc_t r, int voff, int soff) { return __builtin_amdgcn_raw_buffer_load_b128(r, voff, soff, 16); }
DI void st16(u4 v, rsrc_t r, int voff, int soff) { __builtin_amdgcn_raw_buffer_store_b128(v, r, voff + soff, 0, 16); }
DI void pst16(u4 v, rsrc_t r, int voff, int soff) { __builtin_amdgcn_raw_buffer_store_b128(v, r, voff + soff, 0, 0); }
DI void pst8(u2 v, rsrc_t r, int voff, int soff) { __builtin_amdgcn_raw_buffer_store_b64(v, r, voff, soff, 0); }
DI void st8(u2 v, rsrc_t r, int voff, int soff) { __builtin_amdgcn_raw_buffer_store_b64(v, r, voff, soff, 16); }
DI void st4(unsigned v, rsrc_t r, int voff, int soff) { __builtin_amdgcn_raw_buffer_store_b32(v, r, voff, soff, 16); }
DI unsigned ld4(rsrc_t r, int voff, int soff) { return __builtin_amdgcn_raw_buffer_load_b32(r, voff, soff, 16); }
DI float wave_sum(float v) {
#define DPP_ADD(c) v += __builtin_bit_cast(float, __builtin_amdgcn_update_dpp(0, __builtin_bit_cast(int, v), c, 0xf, 0xf, true))
  DPP_ADD(0x111); DPP_ADD(0x112); DPP_ADD(0x114); DPP_ADD(0x118);
#undef DPP_ADD
  const int u = __builtin_bit_cast(int, v);
  return (__builtin_bit_cast(float, __builtin_amdgcn_readlane(u, 15)) + __builtin_bit_cast(float, __builtin_amdgcn_readlane(u, 31))) +
         (__builtin_bit_cast(float, __builtin_amdgcn_readlane(u, 47)) + __builtin_bit_cast(float, __builtin_amdgcn_readlane(u, 63)));
}
constexpr unsigned SPIN = 1u << 20;
#define PSLEEP 8
DI void wait16(rsrc_t rs, int voff, int soff, unsigned tag) {
#pragma unroll 1
  for (unsigned spin = 0; spin < SPIN; ++spin) {
    asm volatile("" : "+v"(voff) :: "memory");
    u4 v = ld16(rs, voff, soff);
    if (__all(v[0] == tag && v[1] == tag && v[2] == tag && v[3] == tag)) break;
    __builtin_amdgcn_s_sleep(PSLEEP);
  }
  asm volatile("" ::: "memory");
  __builtin_amdgcn_fence(__ATOMIC_ACQUIRE, "wavefront");
}
DI void wait4(rsrc_t rs, int voff, int soff, unsigned tag) {
#pragma unroll 1
  for (unsigned spin = 0; spin < SPIN; ++spin) {
    asm volatile("" : "+v"(voff) :: "memory");
    if (__all(ld4(rs, voff, soff) == tag)) break;
    __builtin_amdgcn_s_sleep(PSLEEP);
  }
  asm volatile("" ::: "memory");
  __builtin_amdgcn_fence(__ATOMIC_ACQUIRE, "wavefront");
}

DI void wait_lds(char* p, unsigned lvl) {
#pragma unroll 1
  for (unsigned spin = 0; spin < SPIN; ++spin) {
    if (__hip_atomic_load((AS3 unsigned*)p, __ATOMIC_RELAXED, __HIP_MEMORY_SCOPE_WORKGROUP) >= lvl) break;
    __builtin_amdgcn_s_sleep(1);
  }
  asm volatile("" ::: "memory");
  __builtin_amdgcn_fence(__ATOMIC_ACQUIRE, "wavefront");
}
DI void set_lds(char* p, unsigned lvl) { __hip_atomic_store((AS3 unsigned*)p, lvl, __ATOMIC_RELAXED, __HIP_MEMORY_SCOPE_WORKGROUP); }

constexpr int A_BYTES = 128 * 128;
constexpr int L_Q = 3 * A_BYTES;
constexpr int L_CB = L_Q + 3 * 64 * 448;
constexpr int L_ROW = L_CB + 20480;
constexpr int K1_LDS = L_ROW + 8192;

constexpr int PF2 = 64;
#define TSIDE 2
template <bool KV>
DI void k1_body(const __bf16* __restrict__ x, const __bf16* __restrict__ wqkv, const __bf16* __restrict__ w1,
                const __bf16* __restrict__ w2, char* ws, char* smem, int rb, int ci, int bid, const char* pc) {
  constexpr int NQ = KV ? 20 : 12, NH = KV ? 8 : 16;
  constexpr int QB = 64 * NQ * 16, HB = 64 * NH * 16;
  constexpr int L_H = L_Q + 3 * QB;
  constexpr int JQ = KV ? 5 : 3;
  const int tid = threadIdx.x, lane = tid & 63;
  const int w = __builtin_amdgcn_readfirstlane(tid >> 6), wm = (w >> 1) & 1, wn = w & 1;
  const int g = lane >> 4, l15 = lane & 15, q4 = l15 >> 2, p4 = lane & 3;
  const rsrc_t rs = mkrsrc(ws, WS_END);
  const f4 zero4 = {0.f, 0.f, 0.f, 0.f};
  f4 acc[4][7];
  if (w >= 4) {
    const int lw = w - 4;
    __builtin_amdgcn_s_setprio(3);
    const rsrc_t rx = mkrsrc(x, S_ * D_ * 2), rq = mkrsrc(wqkv, D_ * N3 * 2), r1 = mkrsrc(w1, D_ * DFF * 2);
    int vo[11];
#pragma unroll
    for (int n = 0; n < 4; ++n) {
      const int row = (n * 4 + lw) * 8 + (lane >> 3);
      vo[n] = (rb * 128 + row) * (D_ * 2) + (((lane & 7) ^ ((row >> 1) & 7)) << 4);
      if (n == 0) { for (int m = 1; m < 4; ++m) vo[m] = vo[0] + m * 32 * D_ * 2; break; }
    }
    auto ldA = [&](int stg, int t) {
#pragma unroll
      for (int n = 0; n < 4; ++n) blds16<0>(rx, smem + stg * A_BYTES + n * 4096 + lw * 1024, vo[n], t * 128);
    };
    ldA(0, 0);
#pragma unroll
    for (int n = 0; n < 7; ++n) {
      if (n < NQ / 4) {
        const int c = (n * 4 + lw) * 64 + lane, k = c / NQ, ch = (c - k * NQ) ^ ((k >> 2) & 2);
        vo[4 + n] = (k * N3 + 80 * ci + (KV ? (ch >= 10 ? 1200 : 640) + 8 * ch : 8 * (ch >= 10 ? ch - 2 : ch))) * 2;
      } else {
        const int c = ((n - NQ / 4) * 4 + lw) * 64 + lane, k = c / NH;
        const int f = KV ? (((k >> 1) & 1) | ((k >> 2) & 2)) : ((k & 3) | ((k >> 1) & 4));
        vo[4 + n] = (k * DFF + (KV ? 1024 + 64 * ci : 128 * ci) + 8 * ((c - k * NH) ^ (f << 1))) * 2;
      }
    }
    auto ldB = [&](int stg, int t) {
#pragma unroll
      for (int n = 0; n < 7; ++n) {
        if (n < NQ / 4) blds16<0>(rq, smem + L_Q + stg * QB + n * 4096 + lw * 1024, vo[4 + n], t * (64 * N3 * 2));
        else blds16<0>(r1, smem + L_H + stg * HB + (n - NQ / 4) * 4096 + lw * 1024, vo[4 + n], t * (64 * DFF * 2));
      }
    };
    ldB(0, 0);
    ldA(1, 1);
    ldB(1, 1);
    const unsigned tag = *(const unsigned*)(ws + WS_EP) + 1u;
    const int cl = bid % 54, cq = bid / 54, wks = bid / 5, wcb = bid - wks * 5;
    u4 xa[2] = {}, xb[2] = {};
    auto side_loads = [&]() {
      const bool q = cl < 30;
      const rsrc_t rc = mkrsrc(q ? wqkv : w1, bid < 216 ? (q ? D_ * N3 * 2 : D_ * DFF * 2) : 0);
      const int st = q ? N3 * 2 : DFF * 2;
      const int vb = (cq * 160 + lw * 8 + (lane >> 3)) * st + (q ? cl : cl - 30) * 128 + (lane & 7) * 16;
#pragma unroll
      for (int n = 0; n < 5; ++n) blds16<0>(rc, smem + L_CB + n * 4096 + lw * 1024, vb, n * 32 * st);
      const rsrc_t r2 = mkrsrc(w2, bid < 240 ? DFF * D_ * 2 : 0);
#pragma unroll
      for (int n = 0; n < 2; ++n) blds16<0>(r2, smem + L_ROW + n * 4096 + lw * 1024, ((wks * 32 + lw * 4 + g) * D_ + wcb * 128 + l15 * 8) * 2, n * 16 * D_ * 2);
      const char* xr = (const char*)x + (bid * 8 + lw * 2) * (D_ * 2);
      const char *p0 = xr + lane * 16, *p1 = xr + 1024 + l15 * 16;
      asm volatile("global_load_dwordx4 %0, %4, off\n\tglobal_load_dwordx4 %1, %5, off\n\tglobal_load_dwordx4 %2, %4, off offset:1280\n\t"
                   "global_load_dwordx4 %3, %5, off offset:1280" : "=&v"(xa[0]), "=&v"(xb[0]), "=&v"(xa[1]), "=&v"(xb[1]) : "v"(p0), "v"(p1) : "memory");
    };
    int voG[2];
#pragma unroll
    for (int n = 0; n < 2; ++n) {
      const int c = (n * 4 + lw) * 64 + lane, qq = c / 112, rem = c - qq * 112, tl = rem >> 3, cw = tl >= 7, j = tl - 7 * cw, pr = (rem & 7) * 2;
      int col;
      if (KV) { int cc = j * 16 + pr; col = j < 5 ? 640 + 80 * ci + 40 * cw + (cc >= 40 ? 600 + cc : cc) : N3 + 1024 + 64 * ci + 32 * cw + cc - 80; }
      else col = j < 3 ? 80 * ci + 48 * cw + 16 * (cw && j == 2 ? 1 : j) + pr : N3 + 128 * ci + 64 * cw + 16 * (j - 3) + pr;
      voG[n] = c < 448 ? WS_CG + (qq * 3456 + col) * 8 : WS_SG + rb * 1024 + lane * 16;
    }
    int stg = 2;
#pragma unroll 1
    for (int t = 0; t < 10; ++t) {
      {
        if (t < 9) WAITV(11); else WAITV(0);
        asm volatile("" : "+v"(xa[0]), "+v"(xb[0]), "+v"(xa[1]), "+v"(xb[1]));
        __builtin_amdgcn_s_barrier();
        __builtin_amdgcn_s_setprio(3);
        if (t == TSIDE) side_loads();
        if (t == 8) {
#pragma unroll
          for (int n = 0; n < 2; ++n) blds16<16>(rs, smem + L_ROW + n * 4096 + lw * 1024, voG[n], 0);
        }
        if (t < 8) { ldA(stg, t + 2); ldB(stg, t + 2); }
        stg = stg == 2 ? 0 : stg + 1;
      }
      __builtin_amdgcn_s_setprio(0);
      if (t == TSIDE + 1) {
        float su[2] = {0.f, 0.f}, sq[2] = {0.f, 0.f}, sh[2];
#pragma unroll
        for (int n = 0; n < 2; ++n) {
          sh[n] = lo16(__builtin_amdgcn_readfirstlane(xa[n][0]));
          float s2 = 0.f, q2 = 0.f;
#pragma unroll
          for (int e = 0; e < 4; ++e) {
            const float a0 = lo16(xa[n][e]) - sh[n], a1 = hi16(xa[n][e]) - sh[n], b0 = lo16(xb[n][e]) - sh[n], b1 = hi16(xb[n][e]) - sh[n];
            su[n] += a0 + a1; sq[n] += a0 * a0; sq[n] += a1 * a1; s2 += b0 + b1; q2 += b0 * b0; q2 += b1 * b1;
          }
          if (lane < 16) { su[n] += s2; sq[n] += q2; }
        }
        const float m0 = wave_sum(su[0]) * (1.f / 640.f), m1 = wave_sum(su[1]) * (1.f / 640.f);
        const float e0 = wave_sum(sq[0]) * (1.f / 640.f), e1 = wave_sum(sq[1]) * (1.f / 640.f);
        const float m = lane ? m1 : m0, var = fmaxf((lane ? e1 : e0) - m * m, 0.f);
        const float sd = __builtin_amdgcn_sqrtf(bfr(bfr(var) + 1e-5f));
        if (lane < 2) st8(u2{pk2(m + (lane ? sh[1] : sh[0]), sd), tag}, rs, lane * 8, WS_SG + (bid * 8 + lw * 2) * 8);
        if (tid == 256) { st4(tag, rs, 0, WS_FW + bid * 4); __builtin_amdgcn_raw_buffer_store_b32(0u, rs, 0, WS_F2 + bid * 128, 0); }
      } else if (t == TSIDE + 2) {
        const bf8 ones = __builtin_bit_cast(bf8, u4{0x3f803f80u, 0x3f803f80u, 0x3f803f80u, 0x3f803f80u});
        const char* c0 = smem + L_CB + (8 * g + q4) * 128 + lw * 32 + 8 * p4;
        f4 c = zero4;
#pragma unroll
        for (int k = 0; k < 5; ++k) c = mfma(ones, trfrag(c0 + k * 4096, c0 + k * 4096 + 512), c);
        if (g == 0 && bid < 216) st8(u2{__builtin_bit_cast(unsigned, c[0]), tag}, rs, l15 * 8, WS_CG + (cq * 3456 + cl * 64 + lw * 16) * 8);
      } else if (t == TSIDE + 3) {
#pragma unroll
        for (int n = 0; n < 2; ++n) {
          const char* p0 = smem + L_ROW + (4 * g + q4) * 256 + (lw * 2 + n) * 32 + 8 * p4;
          const bf8 f = trfrag(p0, p0 + 4096);
          const int col = wcb * 128 + (lw * 2 + n) * 16, cg = col / 80;
          if (bid < 240) pst16(__builtin_bit_cast(u4, f), rs, lane * 16, WS_W2 + ((cg * 48 + wks) * 5 + ((col - cg * 80) >> 4)) * 1024);
        }
      }
      WAITL0();
    }
#pragma unroll
    for (int n = 0; n < 2; ++n) {
#pragma unroll 1
      for (unsigned spin = 0; spin < SPIN; ++spin) {
        const u4 v = *(const u4*)(smem + L_ROW + n * 4096 + lw * 1024 + lane * 16);
        if (__all(v[1] == tag && v[3] == tag)) break;
        __builtin_amdgcn_s_sleep(1);
        asm volatile("" : "+v"(voG[n]) :: "memory");
        *(u4*)(smem + L_ROW + n * 4096 + lw * 1024 + lane * 16) = ld16(rs, voG[n], 0);
      }
    }
    WAITL0();
    __builtin_amdgcn_s_barrier();
    if (bid == 0 && w == 7) {
      wait16(rs, lane * 16, WS_FW, tag);
      if (lane == 0) st4(tag, rs, 0, WS_EP);
    }
  } else {
    unsigned pf0, pf1;
    asm volatile("global_load_dword %0, %2, off\n\tglobal_load_dword %1, %3, off" : "=&v"(pf0), "=&v"(pf1)
                 : "v"(pc + tid * 128), "v"(pc + 32768 + (tid < PF2 ? tid : 0) * 128) : "memory");
    unsigned ra[2], rb_[7];
    {
      const unsigned lb = (unsigned)(size_t)(AS3 char*)smem;
      const int f = KV ? (((q4 >> 1) & 1) | ((g & 1) << 1)) : (q4 | ((g & 1) << 2));
#pragma unroll
      for (int s = 0; s < 2; ++s) ra[s] = lb + (wm * 64 + l15) * 128 + (((s * 4 + g) ^ (l15 >> 1)) << 4);
#pragma unroll
      for (int j = 0; j < 7; ++j) {
        const int cc = 2 * j + (p4 >> 1);
        const int pos = KV ? wn * 5 + cc + (cc >= 5 ? 5 : 0) : wn * 6 + cc;
        rb_[j] = lb + (j < JQ ? L_Q + (8 * g + q4) * (NQ * 16) + ((pos ^ ((g & 1) << 1)) << 4) + 8 * (p4 & 1)
                              : L_H + (8 * g + q4) * (NH * 16) + (((wn * (7 - JQ) + j - JQ) ^ f) << 5) + 8 * p4);
      }
    }
#pragma unroll
    for (int i = 0; i < 4; ++i)
#pragma unroll
      for (int j = 0; j < 7; ++j) acc[i][j] = zero4;
    bf8 fa[2][4], fb[2][7];
    s4 hb[2][7][2];
    auto rd1 = [&](int s, int stg, int n) {
      if (n < 4) fa[s][n] = *(const AS3 bf8*)(size_t)(ra[s] + stg * A_BYTES + n * 2048);
      else {
        const int j = (n - 4) >> 1, h = (n - 4) & 1;
        const AS3 char* p = (const AS3 char*)(size_t)rb_[j] + (j < JQ ? stg * QB + (s * 32 + 4 * h) * NQ * 16 : stg * HB + (s * 32 + 4 * h) * NH * 16);
        hb[s][j][h] = __builtin_amdgcn_ds_read_tr16_b64_v4i16((AS3 s4*)p);
        if (h) fb[s][j] = __builtin_bit_cast(bf8, __builtin_shufflevector(hb[s][j][0], hb[s][j][1], 0, 1, 2, 3, 4, 5, 6, 7));
      }
    };
    auto mm1 = [&](int s, int n) {
      const int j = n >> 2, i = n & 3;
      if (KV && j < 5) asm volatile("v_mfma_f32_16x16x32_bf16 %0, %1, %2, %0" : "+v"(acc[i][j]) : "v"(fa[s][i]), "v"(fb[s][j]));
      else asm volatile("v_mfma_f32_16x16x32_bf16 %0, %1, %2, %0" : "+v"(acc[i][j]) : "v"(fb[s][j]), "v"(fa[s][i]));
    };
    auto phase = [&](int sm, int sr, int stg, bool domm, bool dord) {
#pragma unroll
      for (int n = 0; n < 28; ++n) {
        if (domm) mm1(sm, n);
        if (dord && n < 18) rd1(sr, stg, n);
        __builtin_amdgcn_sched_barrier(0);
      }
      if (domm)
        asm volatile("" :: "v"(fa[sm][0]), "v"(fa[sm][1]), "v"(fa[sm][2]), "v"(fa[sm][3]), "v"(fb[sm][0]), "v"(fb[sm][1]), "v"(fb[sm][2]),
                     "v"(fb[sm][3]), "v"(fb[sm][4]), "v"(fb[sm][5]), "v"(fb[sm][6]));
    };
    auto body = [&](int stg, bool first) {
      __builtin_amdgcn_s_barrier();
      __builtin_amdgcn_sched_barrier(0);
      phase(1, 0, stg, !first, true);
      phase(0, 1, stg, true, true);
      WAITL0();
    };
#pragma unroll
    for (int j = 0; j < 7; ++j) asm volatile("" : "+v"(acc[0][j]), "+v"(acc[1][j]), "+v"(acc[2][j]), "+v"(acc[3][j]));
    STAMP(0, 1);
    body(0, true);
    STAMP(0, 2);
#pragma unroll 1
    for (int t = 0; t < 3; ++t) {
      body(1, false);
      body(2, false);
      body(0, false);
    }
    phase(1, 0, 0, true, false);
    asm volatile("s_waitcnt vmcnt(0)" : "+v"(pf0), "+v"(pf1) :: "memory");
    asm volatile("s_nop 15" ::: "memory");
    __builtin_amdgcn_sched_barrier(0);
    STAMP(0, 4);
    __builtin_amdgcn_s_barrier();
    const char* sg = smem + L_ROW + 7168 + wm * 512;
    unsigned ms[4];
    bf8 am[4];
#pragma unroll
    for (int i = 0; i < 4; ++i) {
      ms[i] = *(const unsigned*)(sg + (i * 16 + l15) * 8);
      const unsigned nm = (ms[i] & 0xffffu) ^ 0x8000u;
      am[i] = __builtin_bit_cast(bf8, u4{nm | (nm << 16), nm, 0u, 0u});
    }
#pragma unroll
    for (int j = 0; j < 7; ++j) {
      const float c = *(const float*)(smem + L_ROW + (g * 224 + (wn * 7 + j) * 16 + l15) * 8);
      const float r1 = c - bfr(c), r2 = r1 - bfr(r1);
      const bf8 bm = __builtin_bit_cast(bf8, u4{pk2(c, r1), pk2(r2, 0.f), 0u, 0u});
#pragma unroll
      for (int i = 0; i < 4; ++i) acc[i][j] = (KV && j < 5) ? mfma(am[i], bm, acc[i][j]) : mfma(bm, am[i], acc[i][j]);
    }
    STAMP(0, 5);
    const int rt0 = rb * 8 + wm * 4;
    typedef __attribute__((ext_vector_type(2))) short s2_t;
    auto relu2 = [&](float a, float b) {
      s2_t v = __builtin_bit_cast(s2_t, pk2(a, b));
      return __builtin_bit_cast(unsigned, __builtin_elementwise_max(v, s2_t{0, 0}));
    };
#pragma unroll
    for (int i = 0; i < 4; ++i) {
      const float rstd = __builtin_amdgcn_rcpf(hi16(ms[i]));
#pragma unroll
      for (int j = JQ; j < 7; j += 2) {
        const int ks = (KV ? 1024 + 64 * ci + 32 * wn : 128 * ci + 64 * wn + 16 * (j - JQ)) >> 5;
        const f4 y0 = acc[i][j] * rstd, y1 = acc[i][j + 1] * rstd;
        pst16(u4{relu2(y0[0], y0[1]), relu2(y0[2], y0[3]), relu2(y1[0], y1[1]), relu2(y1[2], y1[3])}, rs, lane * 16,
              WS_HF + ((rt0 + i) * 48 + ks) * 1024);
      }
      if (!KV) {
#pragma unroll
        for (int j = 0; j < 3; ++j) {
          if (j == 2 && wn) break;
          const int col = 80 * ci + 48 * wn + 16 * j;
          const f4 y = acc[i][j] * rstd;
          pst8(u2{pk2(phi(y[0]), phi(y[1])), pk2(phi(y[2]), phi(y[3]))}, rs, lane * 16,
               WS_PQ + ((rt0 + i) * 20 + (col >> 5)) * 1024 + ((col >> 4) & 1) * 8);
        }
      }
    }
    STAMP(0, 6);
    if (KV) {
      f4 kv[8];
#pragma unroll
      for (int hp = 0; hp < 2; ++hp) {
        u4 fr[6];
#pragma unroll
        for (int ii = 0; ii < 2; ++ii) {
          const int i = hp * 2 + ii;
          const u4 s0 = *(const u4*)(sg + (i * 16 + 4 * g) * 8), s1 = *(const u4*)(sg + (i * 16 + 4 * g) * 8 + 16);
          const f4 rr = {__builtin_amdgcn_rcpf(hi16(s0[0])), __builtin_amdgcn_rcpf(hi16(s0[2])), __builtin_amdgcn_rcpf(hi16(s1[0])),
                         __builtin_amdgcn_rcpf(hi16(s1[2]))};
#pragma unroll
          for (int j = 0; j < 5; ++j) {
            f4 y = acc[i][j] * rr;
            if (j < 3) {
#pragma unroll
              for (int e = 0; e < 4; ++e) y[e] = (j < 2 || l15 < 8) ? phi(y[e]) : y[e];
            }
            fr[j][2 * ii] = pk2(y[0], y[1]);
            fr[j][2 * ii + 1] = pk2(y[2], y[3]);
          }
        }
#pragma unroll
        for (int e = 0; e < 4; ++e) fr[5][e] = l15 < 8 ? 0x3f803f80u : fr[2][e];
        const int ta[8] = {0, 0, 1, 1, 1, 2, 2, 2}, tb[8] = {5, 3, 5, 3, 4, 5, 3, 4};
#pragma unroll
        for (int m = 0; m < 8; ++m) kv[m] = mfma(__builtin_bit_cast(bf8, fr[tb[m]]), __builtin_bit_cast(bf8, fr[ta[m]]), hp ? kv[m] : zero4);
      }
#pragma unroll
      for (int m = 0; m < 8; ++m) *(f4*)(smem + (w * 8 + m) * 1024 + lane * 16) = kv[m];
      WAITL0();
    }
  }
  if (KV) {
    __builtin_amdgcn_s_barrier();
    if (tid < 480) {
      const bool isz = tid >= 400;
      int hl = isz ? (tid - 400) / 20 : tid / 100, rem = isz ? (tid - 400 - hl * 20) * 5 : tid - hl * 100, d = rem / 5, grp = rem - d * 5;
      int hk = hl & 1, kc = 20 * hk + d, a = kc >> 4;
      int pos = isz ? 32 : 40 + 20 * hk + 4 * grp, b = pos >> 4;
      int m = a == 0 ? b - 2 : (a == 1 ? b : b + 3);
      const char* p = smem + ((hl >> 1) * 8 + m) * 1024 + (((pos >> 2) & 3) * 16 + (kc & 15)) * 16;
      f4 v = *(const f4*)p + *(const f4*)(p + 16384);
      if (isz) __builtin_amdgcn_raw_buffer_store_b32(__builtin_bit_cast(unsigned, v[0]), rs, (tid - 400) * 4, WS_Z + (rb * 640 + ci * 80) * 4, 0);
      else pst16(__builtin_bit_cast(u4, v), rs, tid * 16, WS_KV + (rb * 32 + ci * 4) * 1600);
    }
  }
  STAMP(0, 7);
}

__global__ __launch_bounds__(512, 2) void k1_kernel(const __bf16* __restrict__ x, const __bf16* __restrict__ wqkv,
                                                    const __bf16* __restrict__ w1, const __bf16* __restrict__ w2, char* ws) {
  extern __shared__ __attribute__((aligned(16))) char smem[];
  const int bid = blockIdx.x, xcd = bid & 7, idx = bid >> 3;
  STAMP(0, 0);
  const char* pc;
  asm volatile("s_getpc_b64 %0" : "=s"(pc));
  const int rb = (xcd >> 1) * 4 + (idx & 3), cgi = idx >> 2;
  const int ci = (xcd & 1) * 4 + (cgi >> 1);
  if (cgi & 1) k1_body<false>(x, wqkv, w1, w2, ws, smem, rb, ci, bid, pc);
  else k1_body<true>(x, wqkv, w1, w2, ws, smem, rb, ci, bid, pc);
}

template <int AUX>
DI bf8 ldfrag(rsrc_t r, int voff, int soff) {
  return __builtin_bit_cast(bf8, __builtin_amdgcn_raw_buffer_load_b128(r, voff, soff, AUX));
}
DI void st8_sc1(char* p, unsigned a, unsigned b) {
  __hip_atomic_store((AS1 unsigned long long*)p, (unsigned long long)a | ((unsigned long long)b << 32), __ATOMIC_RELAXED,
                     __HIP_MEMORY_SCOPE_AGENT);
}
constexpr int L2_ACC = 0;
constexpr int L2_WO = 61440;
constexpr int L2_KV = L2_WO + 5120;
constexpr int L2_Z = L2_KV + 2048;
constexpr int L2_CNT = L2_Z + 2048;
constexpr int K2_LDS = 98304;

#define RING 3
DI void k2_publish(char* ws, char* smem, int flag, int lane) {
  WAITV(0);
  if (lane == 0) {
    unsigned old = __hip_atomic_fetch_add((AS3 unsigned*)(smem + L2_CNT), 1u, __ATOMIC_RELAXED, __HIP_MEMORY_SCOPE_WORKGROUP);
    if (old == 2u) __hip_atomic_store((AS1 unsigned*)(ws + WS_F2 + flag * 128), 1u, __ATOMIC_RELAXED, __HIP_MEMORY_SCOPE_AGENT);
  }
}
__global__ __launch_bounds__(512, 2) void k2_kernel(const __bf16* __restrict__ x, const __bf16* __restrict__ wo,
                                                    char* ws, __bf16* __restrict__ out) {
  extern __shared__ __attribute__((aligned(16))) char smem[];
  const int tid = threadIdx.x, lane = tid & 63, w = __builtin_amdgcn_readfirstlane(tid >> 6);
  const int g = lane >> 4, l15 = lane & 15;
  const int xcd = blockIdx.x & 7, idx = blockIdx.x >> 3;
  const int r = (xcd >> 1) * 8 + (idx & 7), c = (xcd & 1) * 4 + (idx >> 3);
  STAMP(1, 0);
  if (tid == 0) *(unsigned*)(smem + L2_CNT) = 0u;
  WAITL0();
  __builtin_amdgcn_s_barrier();
  const rsrc_t rs = mkrsrc(ws, (unsigned)WS_END);
  const int vo = lane * 16;
  const f4 zero4 = {0.f, 0.f, 0.f, 0.f};
  if (w >= 4) __builtin_amdgcn_s_setprio(3);
  if (w == 4) {
    const f4* p = (const f4*)(ws + WS_KV) + r * 100;
    const int i1 = lane < 36 ? lane + 64 : lane;
    f4 v0[16], v1[16];
#pragma unroll
    for (int i = 0; i < 16; ++i) { v0[i] = p[i * 3200 + lane]; v1[i] = p[i * 3200 + i1]; }
    u4 wv[4];
#pragma unroll
    for (int i = 0; i < 4; ++i) {
      int ch = i * 64 + lane, row = ch / 10;
      wv[i] = u4{0u, 0u, 0u, 0u};
      if (ch < 200) wv[i] = *(const u4*)(wo + (20 * r + row) * D_ + 80 * c + (ch - row * 10) * 8);
    }
#pragma unroll
    for (int st = 1; st < 16; st *= 2)
#pragma unroll
      for (int i = 0; i < 16; i += 2 * st) { v0[i] += v0[i + st]; v1[i] += v1[i + st]; }
    *(f4*)(smem + L2_KV + vo) = v0[0];
    *(f4*)(smem + L2_KV + 1024 + vo) = v1[0];
#pragma unroll
    for (int i = 0; i < 4; ++i) *(u4*)(smem + L2_WO + i * 1024 + vo) = wv[i];
    *(u4*)(smem + L2_WO + 4096 + vo) = u4{0u, 0u, 0u, 0u};
    u4 au[2];
#pragma unroll
    for (int dt = 0; dt < 2; ++dt)
#pragma unroll
      for (int hh = 0; hh < 2; ++hh) {
        int d = dt * 16 + l15, grp = 2 * g + hh;
        bool ok = d < 20 && grp < 5;
        f4 v = *(const f4*)(smem + L2_KV + (ok ? d * 5 + grp : 0) * 16);
        au[dt][2 * hh] = ok ? pk2(v[0], v[1]) : 0u;
        au[dt][2 * hh + 1] = ok ? pk2(v[2], v[3]) : 0u;
      }
    const int k0 = 20 * r + 4 * g, k1 = 20 * r + 16;
    const char* p0 = smem + L2_WO + (8 * g + (l15 >> 2)) * 160 + (lane & 3) * 8;
    char* mf = ws + WS_MF + c * (20 * 5120) + l15 * 16;
    const int o0 = (k0 >> 5) * 5120 + ((k0 >> 2) & 3) * 256 + ((k0 >> 4) & 1) * 8;
    const int o1 = (k1 >> 5) * 5120 + ((k1 >> 2) & 3) * 256 + ((k1 >> 4) & 1) * 8;
#pragma unroll
    for (int t = 0; t < 5; ++t) {
      bf8 b = trfrag(p0 + t * 32, p0 + t * 32 + 640);
      f4 m0 = mfma(__builtin_bit_cast(bf8, au[0]), b, zero4), m1 = mfma(__builtin_bit_cast(bf8, au[1]), b, zero4);
      st8_sc1(mf + t * 1024 + o0, pk2(m0[0], m0[1]), pk2(m0[2], m0[3]));
      if (g == 0) st8_sc1(mf + t * 1024 + o1, pk2(m1[0], m1[1]), pk2(m1[2], m1[3]));
    }
    k2_publish(ws, smem, r * 8 + c, lane);
    STAMPW(1, 8);
  } else if (w == 5 || w == 6) {
    const int fo = ((r * 4 + (c & 1) * 2 + w - 5) * 20 + (c >> 1) * 5) * 1024;
    const f4* zp = (const f4*)(ws + WS_Z) + (c >> 1) * 40 + (lane < 40 ? lane : 0);
    f4 zv[16];
#pragma unroll
    for (int i = 0; i < 16; ++i) zv[i] = zp[i * 160];
    u4 pq[5];
#pragma unroll
    for (int k = 0; k < 5; ++k) pq[k] = __builtin_bit_cast(u4, ldfrag<0>(rs, vo, (int)WS_PQ + fo + k * 1024));
#pragma unroll
    for (int st = 1; st < 16; st *= 2)
#pragma unroll
      for (int i = 0; i < 16; i += 2 * st) zv[i] += zv[i + st];
    char* zl = smem + L2_Z + (w - 5) * 1024;
    if (lane < 40) *(u2*)(zl + lane * 8) = u2{pk2(zv[0][0], zv[0][1]), pk2(zv[0][2], zv[0][3])};
    f4 den = zero4;
#pragma unroll
    for (int k = 0; k < 5; ++k) {
      u4 zf;
#pragma unroll
      for (int p = 0; p < 2; ++p) {
        int gi = 8 * k + 4 * p + g;
        u2 z2 = *(const u2*)(zl + gi * 8);
        bool mine = gi / 5 == l15;
        zf[2 * p] = mine ? z2[0] : 0u;
        zf[2 * p + 1] = mine ? z2[1] : 0u;
      }
      den = mfma(__builtin_bit_cast(bf8, zf), __builtin_bit_cast(bf8, pq[k]), den);
    }
    f4 rd;
#pragma unroll
    for (int e = 0; e < 4; ++e) rd[e] = __builtin_amdgcn_rcpf(bfr(bfr(den[e]) + 1e-6f));
    if (g < 2) *(f4*)(zl + 512 + l15 * 32 + g * 16) = rd;
#pragma unroll
    for (int k = 0; k < 5; ++k) {
      u4 o;
#pragma unroll
      for (int p = 0; p < 2; ++p) {
        float s = *(const float*)(zl + 512 + l15 * 32 + ((8 * k + 4 * p + g) / 5) * 4);
        o[2 * p] = pk2(lo16(pq[k][2 * p]) * s, hi16(pq[k][2 * p]) * s);
        o[2 * p + 1] = pk2(lo16(pq[k][2 * p + 1]) * s, hi16(pq[k][2 * p + 1]) * s);
      }
      st16(o, rs, vo, WS_AF + fo + k * 1024);
    }
    k2_publish(ws, smem, r * 8 + c, lane);
    STAMPW(1, 9);
  }
  if (w >= 4) {
    __builtin_amdgcn_s_barrier();
    return;
  }
  const int hA = (int)WS_HF + (r * 4 * 48 + w) * 1024, hB = (int)WS_W2 + (c * 48 + w) * 5120;
  const int aA = (int)WS_AF + (r * 4 * 20 + w) * 1024, aB = (int)WS_MF + (c * 20 + w) * 5120;
#define ROT 0
  const int rot = ROT ? ((idx >> 3) * 3 + (idx & 7) * 5) % 12 : 0, rot2 = ROT ? idx % 5 : 0;
  bf8 fa[RING][4], fb[RING][5];
  auto ld = [&](int i) {
    const int sl = i % RING;
    if (i < 12) {
      int ii = i + rot;
      ii = ii >= 12 ? ii - 12 : ii;
#pragma unroll
      for (int j = 0; j < 5; ++j) fb[sl][j] = ldfrag<0>(rs, vo + j * 1024, hB + ii * 20480);
#pragma unroll
      for (int j = 0; j < 4; ++j) fa[sl][j] = ldfrag<0>(rs, vo, hA + ii * 4096 + j * 49152);
    } else {
      int ii = i - 12 + rot2;
      ii = ii >= 5 ? ii - 5 : ii;
#pragma unroll
      for (int j = 0; j < 5; ++j) fb[sl][j] = ldfrag<16>(rs, vo + j * 1024, aB + ii * 20480);
#pragma unroll
      for (int j = 0; j < 4; ++j) fa[sl][j] = ldfrag<16>(rs, vo, aA + ii * 4096 + j * 20480);
    }
  };
#pragma unroll
  for (int i = 0; i < RING; ++i) ld(i);
  const int orow = (r * 64 + w * 16 + l15) * D_ + c * 80 + 4 * g;
  u2 xr[5];
#pragma unroll
  for (int j = 0; j < 5; ++j) xr[j] = *(const u2*)(x + orow + 16 * j);
  const AS1 unsigned* fl = (const AS1 unsigned*)(ws + WS_F2 + (lane < 8 ? r * 8 + lane : (lane < 40 ? (lane - 8) * 8 + c : r * 8 + c)) * 128);
  STAMP(1, 1);
  __builtin_amdgcn_sched_barrier(0);
  f4 acc[4][5];
#pragma unroll
  for (int i = 0; i < 4; ++i)
#pragma unroll
    for (int j = 0; j < 5; ++j) acc[i][j] = zero4;
  unsigned flv = 0;
#pragma unroll
  for (int i = 0; i < 17; ++i) {
    const int sl = i % RING;
#pragma unroll
    for (int j = 0; j < 5; ++j)
#pragma unroll
      for (int m = 0; m < 4; ++m) acc[m][j] = mfma(fb[sl][j], fa[sl][m], acc[m][j]);
    if (i + RING == 11) flv = __hip_atomic_load(fl, __ATOMIC_RELAXED, __HIP_MEMORY_SCOPE_AGENT);
    if (i + RING == 12) {
      for (unsigned spin = 0; !__all(flv == 1u) && spin < (1u << 22); ++spin) {
        __builtin_amdgcn_s_sleep(1);
        flv = __hip_atomic_load(fl, __ATOMIC_RELAXED, __HIP_MEMORY_SCOPE_AGENT);
      }
      __builtin_amdgcn_fence(__ATOMIC_ACQUIRE, "wavefront");
    }
    if (i + RING < 17) ld(i + RING);
    __builtin_amdgcn_sched_barrier(0);
  }
  STAMP(1, 6);
#pragma unroll
  for (int m = 0; m < 4; ++m) {
    if (m != w) {
      int slot = m < w ? m : m - 1;
#pragma unroll
      for (int j = 0; j < 5; ++j) *(f4*)(smem + L2_ACC + ((w * 3 + slot) * 5 + j) * 1024 + vo) = acc[m][j];
    }
  }
  WAITL0();
  __builtin_amdgcn_s_barrier();
  f4 own[5];
#pragma unroll
  for (int j = 0; j < 5; ++j) own[j] = w == 0 ? acc[0][j] : (w == 1 ? acc[1][j] : (w == 2 ? acc[2][j] : acc[3][j]));
  f4 tot[5];
#pragma unroll
  for (int j = 0; j < 5; ++j) tot[j] = f4{lo16(xr[j][0]), hi16(xr[j][0]), lo16(xr[j][1]), hi16(xr[j][1])};
#pragma unroll
  for (int s = 0; s < 4; ++s) {
    int slot = w < s ? w : w - 1;
#pragma unroll
    for (int j = 0; j < 5; ++j) {
      f4 v = own[j];
      if (s != w) v = *(const f4*)(smem + L2_ACC + ((s * 3 + slot) * 5 + j) * 1024 + vo);
      tot[j] += v;
    }
  }
#pragma unroll
  for (int j = 0; j < 5; ++j) *(u2*)(out + orow + 16 * j) = u2{pk2(tot[j][0], tot[j][1]), pk2(tot[j][2], tot[j][3])};
  STAMP(1, 7);
}

extern "C" void kernel_launch(void* const* d_in, const int* in_sizes, int n_in, void* d_out, int out_size, void* d_ws,
                              size_t ws_size, hipStream_t stream) {
  const __bf16* x = (const __bf16*)d_in[0];
  const __bf16* wqkv = (const __bf16*)d_in[1];
  const __bf16* wo = (const __bf16*)d_in[2];
  const __bf16* w1 = (const __bf16*)d_in[4];
  const __bf16* w2 = (const __bf16*)d_in[5];
  char* ws = (char*)d_ws;
  (void)hipFuncSetAttribute((const void*)k1_kernel, hipFuncAttributeMaxDynamicSharedMemorySize, K1_LDS);
  (void)hipFuncSetAttribute((const void*)k2_kernel, hipFuncAttributeMaxDynamicSharedMemorySize, K2_LDS);
  k1_kernel<<<256, 512, K1_LDS, stream>>>(x, wqkv, w1, w2, ws);
  k2_kernel<<<256, 512, K2_LDS, stream>>>(x, wo, ws, (__bf16*)d_out);
}
```

```cpp
#include <hip/hip_runtime.h>
#include <hip/hip_bf16.h>
#include <stdint.h>

typedef __attribute__((ext_vector_type(8))) __bf16 bf8;
typedef __attribute__((ext_vector_type(4))) __bf16 bf4;
typedef __attribute__((ext_vector_type(2))) __bf16 bf2;
typedef __attribute__((ext_vector_type(4))) short s4;
typedef __attribute__((ext_vector_type(4))) float f4;
typedef __attribute__((ext_vector_type(2))) float f2;
typedef __attribute__((ext_vector_type(4))) unsigned u4;
typedef __attribute__((ext_vector_type(2))) unsigned u2;
#define AS1 __attribute__((address_space(1)))
#define AS3 __attribute__((address_space(3)))
#define DI __device__ __forceinline__

constexpr int S_ = 2048, D_ = 640, DFF = 1536, N3 = 1920;
constexpr int WS_HF = 0;
constexpr int WS_PQ = WS_HF + 128 * 48 * 1024;
constexpr int WS_AF = WS_PQ + 128 * 20 * 1024;
constexpr int WS_W2 = WS_AF + 128 * 20 * 1024;
constexpr int WS_MF = WS_W2 + 8 * 48 * 5 * 1024;
constexpr int WS_KV = WS_MF + 8 * 20 * 5 * 1024;
constexpr int WS_Z = WS_KV + 16 * 32 * 400 * 4;
constexpr int WS_CG = WS_Z + 16 * 640 * 4;
constexpr int WS_SG = WS_CG + 4 * 3456 * 8;
constexpr int WS_FW = WS_SG + 2048 * 8;
constexpr int WS_F1 = WS_FW + 1024;
constexpr int WS_F2 = WS_F1 + 1024;
constexpr int WS_EP = WS_F2 + 256 * 128;
constexpr int WS_END = WS_EP + 128;
#define STAMPW(k, i) do {} while (0)
#define STAMP(k, i) do {} while (0)

DI float bfr(float v) { return (float)(__bf16)v; }
DI unsigned pk2(float a, float b) { f2 v = {a, b}; return __builtin_bit_cast(unsigned, __builtin_convertvector(v, bf2)); }
DI float lo16(unsigned u) { return __builtin_bit_cast(float, u << 16); }
DI float hi16(unsigned u) { return __builtin_bit_cast(float, u & 0xffff0000u); }
DI f4 mfma(bf8 a, bf8 b, f4 c) { return __builtin_amdgcn_mfma_f32_16x16x32_bf16(a, b, c, 0, 0, 0); }
DI void glds16(const void* g, char* l) {
  __builtin_amdgcn_global_load_lds((const AS1 void*)g, (AS3 void*)l, 16, 0, 0);
}
DI bf8 trfrag(const char* p0, const char* p1) {
  s4 a = __builtin_amdgcn_ds_read_tr16_b64_v4i16((AS3 s4*)p0);
  s4 b = __builtin_amdgcn_ds_read_tr16_b64_v4i16((AS3 s4*)p1);
  return __builtin_bit_cast(bf8, __builtin_shufflevector(a, b, 0, 1, 2, 3, 4, 5, 6, 7));
}
DI bf8 trfrag3(const AS3 char* p0, const AS3 char* p1) {
  s4 a = __builtin_amdgcn_ds_read_tr16_b64_v4i16((AS3 s4*)p0);
  s4 b = __builtin_amdgcn_ds_read_tr16_b64_v4i16((AS3 s4*)p1);
  return __builtin_bit_cast(bf8, __builtin_shufflevector(a, b, 0, 1, 2, 3, 4, 5, 6, 7));
}
DI float phi(float t) {
  return fmaxf(t, 0.f) + 1e-6f + __builtin_amdgcn_exp2f(fminf(t, 0.f) * 1.44269504088896341f);
}
#define MT(i) do {} while (0)
#define WAITV(n) asm volatile("s_waitcnt vmcnt(" #n ")" ::: "memory")
#define WAITL0() asm volatile("s_waitcnt lgkmcnt(0)" ::: "memory")

#define PAUX 16
#define LAUX 0
typedef __amdgpu_buffer_rsrc_t rsrc_t;
DI rsrc_t mkrsrc(const void* p, unsigned bytes) { return __builtin_amdgcn_make_buffer_rsrc((void*)p, 0, bytes, 0x00020000); }
template <int AUX>
DI void blds16(rsrc_t r, char* l, int voff, int soff) {
  __builtin_amdgcn_raw_ptr_buffer_load_lds(r, (AS3 void*)l, 16, voff, soff, 0, AUX);
}
DI u4 ld16(rsrc_t r, int voff, int soff) { return __builtin_amdgcn_raw_buffer_load_b128(r, voff, soff, 16); }
DI void st16(u4 v, rsrc_t r, int voff, int soff) { __builtin_amdgcn_raw_buffer_store_b128(v, r, voff + soff, 0, 16); }
DI void pst16(u4 v, rsrc_t r, int voff, int soff) { __builtin_amdgcn_raw_buffer_store_b128(v, r, voff + soff, 0, PAUX); }
DI void pst8(u2 v, rsrc_t r, int voff, int soff) { __builtin_amdgcn_raw_buffer_store_b64(v, r, voff, soff, PAUX); }
DI void st8(u2 v, rsrc_t r, int voff, int soff) { __builtin_amdgcn_raw_buffer_store_b64(v, r, voff, soff, 16); }
DI void st4(unsigned v, rsrc_t r, int voff, int soff) { __builtin_amdgcn_raw_buffer_store_b32(v, r, voff, soff, 16); }
DI unsigned ld4(rsrc_t r, int voff, int soff) { return __builtin_amdgcn_raw_buffer_load_b32(r, voff, soff, 16); }
DI float wave_sum(float v) {
#define DPP_ADD(c) v += __builtin_bit_cast(float, __builtin_amdgcn_update_dpp(0, __builtin_bit_cast(int, v), c, 0xf, 0xf, true))
  DPP_ADD(0x111); DPP_ADD(0x112); DPP_ADD(0x114); DPP_ADD(0x118);
#undef DPP_ADD
  const int u = __builtin_bit_cast(int, v);
  return (__builtin_bit_cast(float, __builtin_amdgcn_readlane(u, 15)) + __builtin_bit_cast(float, __builtin_amdgcn_readlane(u, 31))) +
         (__builtin_bit_cast(float, __builtin_amdgcn_readlane(u, 47)) + __builtin_bit_cast(float, __builtin_amdgcn_readlane(u, 63)));
}
constexpr unsigned SPIN = 1u << 20;
#define PSLEEP 8
DI void wait16(rsrc_t rs, int voff, int soff, unsigned tag) {
#pragma unroll 1
  for (unsigned spin = 0; spin < SPIN; ++spin) {
    asm volatile("" : "+v"(voff) :: "memory");
    u4 v = ld16(rs, voff, soff);
    if (__all(v[0] == tag && v[1] == tag && v[2] == tag && v[3] == tag)) break;
    __builtin_amdgcn_s_sleep(PSLEEP);
  }
  asm volatile("" ::: "memory");
  __builtin_amdgcn_fence(__ATOMIC_ACQUIRE, "wavefront");
}
DI void wait4(rsrc_t rs, int voff, int soff, unsigned tag) {
#pragma unroll 1
  for (unsigned spin = 0; spin < SPIN; ++spin) {
    asm volatile("" : "+v"(voff) :: "memory");
    if (__all(ld4(rs, voff, soff) == tag)) break;
    __builtin_amdgcn_s_sleep(PSLEEP);
  }
  asm volatile("" ::: "memory");
  __builtin_amdgcn_fence(__ATOMIC_ACQUIRE, "wavefront");
}

DI void wait_lds(char* p, unsigned lvl) {
#pragma unroll 1
  for (unsigned spin = 0; spin < SPIN; ++spin) {
    if (__hip_atomic_load((AS3 unsigned*)p, __ATOMIC_RELAXED, __HIP_MEMORY_SCOPE_WORKGROUP) >= lvl) break;
    __builtin_amdgcn_s_sleep(1);
  }
  asm volatile("" ::: "memory");
  __builtin_amdgcn_fence(__ATOMIC_ACQUIRE, "wavefront");
}
DI void set_lds(char* p, unsigned lvl) { __hip_atomic_store((AS3 unsigned*)p, lvl, __ATOMIC_RELAXED, __HIP_MEMORY_SCOPE_WORKGROUP); }

constexpr int A_BYTES = 128 * 128;
constexpr int L_Q = 3 * A_BYTES;
constexpr int L_CB = L_Q + 3 * 64 * 448;
constexpr int L_ROW = L_CB + 20480;
constexpr int K1_LDS = L_ROW + 8192;

constexpr int PF2 = 64;
#define TSIDE 2
template <bool KV>
DI void k1_body(const __bf16* __restrict__ x, const __bf16* __restrict__ wqkv, const __bf16* __restrict__ w1,
                const __bf16* __restrict__ w2, char* ws, char* smem, int rb, int ci, int bid, const char* pc) {
  constexpr int NQ = KV ? 20 : 12, NH = KV ? 8 : 16;
  constexpr int QB = 64 * NQ * 16, HB = 64 * NH * 16;
  constexpr int L_H = L_Q + 3 * QB;
  constexpr int JQ = KV ? 5 : 3;
  const int tid = threadIdx.x, lane = tid & 63;
  const int w = __builtin_amdgcn_readfirstlane(tid >> 6), wm = (w >> 1) & 1, wn = w & 1;
  const int g = lane >> 4, l15 = lane & 15, q4 = l15 >> 2, p4 = lane & 3;
  const rsrc_t rs = mkrsrc(ws, WS_END);
  const f4 zero4 = {0.f, 0.f, 0.f, 0.f};
  f4 acc[4][7];
  const unsigned tag = *(const __attribute__((address_space(4))) unsigned*)(ws + WS_EP) + 1u;
  const int wks = bid / 5, wcb = bid - wks * 5;
  const int jj = bid >> 3, hf = (bid >> 2) & 1, qd = bid & 3, sel = jj >> 1, srow = hf * 1024 + qd * 256 + jj * 8;
  const bool cjob = jj < 27;
  const int cq = jj < 24 ? 2 * hf + (jj & 1) : (qd & 1) * 2 + hf;
  const int cl = jj >= 24 ? 10 * (jj - 24) + 5 * (qd >> 1) + 2
                          : (sel < 6 ? 10 * (sel >> 1) + 5 * (qd >> 1) + 3 * (qd & 1) + (sel & 1) : (sel < 10 ? 24 + 4 * qd + sel : 36 + 2 * qd + sel));
  if (w >= 4) {
    const int lw = w - 4;
    __builtin_amdgcn_s_setprio(3);
    const rsrc_t rx = mkrsrc(x, S_ * D_ * 2), rq = mkrsrc(wqkv, D_ * N3 * 2), r1 = mkrsrc(w1, D_ * DFF * 2);
    int vo[11];
#pragma unroll
    for (int n = 0; n < 4; ++n) {
      const int row = (n * 4 + lw) * 8 + (lane >> 3);
      vo[n] = (rb * 128 + row) * (D_ * 2) + (((lane & 7) ^ ((row >> 1) & 7)) << 4);
      if (n == 0) { for (int m = 1; m < 4; ++m) vo[m] = vo[0] + m * 32 * D_ * 2; break; }
    }
#define ROT1 3
    const int rot = ROT1 == 1 ? ((jj & 7) + 3 * (jj >> 3)) % 10 : (ROT1 == 2 ? ((jj & 1) * 5 + (jj >> 4) * 2) : (ROT1 == 3 ? ((jj & 3) * 5 / 2 + (jj >> 4)) : 0));
    auto ldA = [&](int stg, int t) {
      t += rot;
      t = t >= 10 ? t - 10 : t;
#pragma unroll
      for (int n = 0; n < 4; ++n) blds16<0>(rx, smem + stg * A_BYTES + n * 4096 + lw * 1024, vo[n], t * 128);
    };
    ldA(0, 0);
#pragma unroll
    for (int n = 0; n < 7; ++n) {
      if (n < NQ / 4) {
        const int c = (n * 4 + lw) * 64 + lane, k = c / NQ, ch = (c - k * NQ) ^ ((k >> 2) & 2);
        vo[4 + n] = (k * N3 + 80 * ci + (KV ? (ch >= 10 ? 1200 : 640) + 8 * ch : 8 * (ch >= 10 ? ch - 2 : ch))) * 2;
      } else {
        const int c = ((n - NQ / 4) * 4 + lw) * 64 + lane, k = c / NH;
        const int f = KV ? (((k >> 1) & 1) | ((k >> 2) & 2)) : ((k & 3) | ((k >> 1) & 4));
        vo[4 + n] = (k * DFF + (KV ? 1024 + 64 * ci : 128 * ci) + 8 * ((c - k * NH) ^ (f << 1))) * 2;
      }
    }
    auto ldB = [&](int stg, int t) {
      t += rot;
      t = t >= 10 ? t - 10 : t;
#pragma unroll
      for (int n = 0; n < 7; ++n) {
        if (n < NQ / 4) blds16<0>(rq, smem + L_Q + stg * QB + n * 4096 + lw * 1024, vo[4 + n], t * (64 * N3 * 2));
        else blds16<0>(r1, smem + L_H + stg * HB + (n - NQ / 4) * 4096 + lw * 1024, vo[4 + n], t * (64 * DFF * 2));
      }
    };
    ldB(0, 0);
    ldA(1, 1);
    ldB(1, 1);
    int voG[2];
#pragma unroll
    for (int n = 0; n < 2; ++n) {
      const int c = (n * 4 + lw) * 64 + lane, qq = c / 112, rem = c - qq * 112, tl = rem >> 3, cw = tl >= 7, j = tl - 7 * cw, pr = (rem & 7) * 2;
      int col;
      if (KV) { int cc = j * 16 + pr; col = j < 5 ? 640 + 80 * ci + 40 * cw + (cc >= 40 ? 600 + cc : cc) : N3 + 1024 + 64 * ci + 32 * cw + cc - 80; }
      else col = j < 3 ? 80 * ci + 48 * cw + 16 * (cw && j == 2 ? 1 : j) + pr : N3 + 128 * ci + 64 * cw + 16 * (j - 3) + pr;
      voG[n] = c < 448 ? WS_CG + (qq * 3456 + col) * 8 : WS_SG + rb * 1024 + lane * 16;
    }
    unsigned pf0 = 0, pf1 = 0, pf2 = 0;
    auto side_loads = [&]() {
      const bool q = cl < 30;
      const rsrc_t rc = mkrsrc(q ? wqkv : w1, cjob ? (q ? D_ * N3 * 2 : D_ * DFF * 2) : 0);
      const int st = q ? N3 * 2 : DFF * 2;
      const int vb = (cq * 160 + lw * 8 + (lane >> 3)) * st + (q ? cl : cl - 30) * 128 + (lane & 7) * 16;
#pragma unroll
      for (int n = 0; n < 5; ++n) blds16<0>(rc, smem + L_CB + n * 4096 + lw * 1024, vb, n * 32 * st);
      const rsrc_t r2 = mkrsrc(w2, bid < 240 ? DFF * D_ * 2 : 0);
#pragma unroll
      for (int n = 0; n < 2; ++n) blds16<0>(r2, smem + L_ROW + n * 4096 + lw * 1024, ((wks * 32 + lw * 4 + g) * D_ + wcb * 128 + l15 * 8) * 2, n * 16 * D_ * 2);
      const int to = lane * 280000;
      asm volatile("global_load_dword %0, %3, off\n\tglobal_load_dword %1, %4, off\n\tglobal_load_dword %2, %5, off"
                   : "+v"(pf0), "+v"(pf1), "+v"(pf2)
                   : "v"(pc + (tid - 256) * 128), "v"(pc + 32768 + (tid - 256 < PF2 ? tid - 256 : 0) * 128), "v"(ws + (to < WS_EP ? to : WS_EP)) : "memory");
    };
    int stg = 2;
#pragma unroll 1
    for (int t = 0; t < 10; ++t) {
      {
        if (t == 1 || t == 2) WAITV(21); else if (t < 9) WAITV(11); else WAITV(0);
        __builtin_amdgcn_s_barrier();
        __builtin_amdgcn_s_setprio(3);
        if (t == 8) {
#pragma unroll
          for (int n = 0; n < 2; ++n) blds16<16>(rs, smem + L_ROW + n * 4096 + lw * 1024, voG[n], 0);
        }
        if (t < 8) { ldA(stg, t + 2); ldB(stg, t + 2); }
        if (t == 0) side_loads();
        stg = stg == 2 ? 0 : stg + 1;
      }
      __builtin_amdgcn_s_setprio(0);
      if (t == 4) {
        const bf8 ones = __builtin_bit_cast(bf8, u4{0x3f803f80u, 0x3f803f80u, 0x3f803f80u, 0x3f803f80u});
        const char* c0 = smem + L_CB + (8 * g + q4) * 128 + lw * 32 + 8 * p4;
        f4 c = zero4;
#pragma unroll
        for (int k = 0; k < 5; ++k) c = mfma(ones, trfrag(c0 + k * 4096, c0 + k * 4096 + 512), c);
        if (g == 0 && cjob) st8(u2{__builtin_bit_cast(unsigned, c[0]), tag}, rs, l15 * 8, WS_CG + (cq * 3456 + cl * 64 + lw * 16) * 8);
      } else if (t == 5) {
#pragma unroll
        for (int n = 0; n < 2; ++n) {
          const char* p0 = smem + L_ROW + (4 * g + q4) * 256 + (lw * 2 + n) * 32 + 8 * p4;
          const bf8 f = trfrag(p0, p0 + 4096);
          const int col = wcb * 128 + (lw * 2 + n) * 16;
          if (bid < 240) pst16(__builtin_bit_cast(u4, f), rs, lane * 16, WS_W2 + (wks * 40 + (col >> 4)) * 1024);
        }
      }
      WAITL0();
    }
    asm volatile("" :: "v"(pf0), "v"(pf1), "v"(pf2));
#pragma unroll
    for (int n = 0; n < 2; ++n) {
#pragma unroll 1
      for (unsigned spin = 0; spin < SPIN; ++spin) {
        const u4 v = *(const u4*)(smem + L_ROW + n * 4096 + lw * 1024 + lane * 16);
        if (__all(v[1] == tag && v[3] == tag)) break;
        __builtin_amdgcn_s_sleep(1);
        asm volatile("" : "+v"(voG[n]) :: "memory");
        *(u4*)(smem + L_ROW + n * 4096 + lw * 1024 + lane * 16) = ld16(rs, voG[n], 0);
      }
    }
    WAITL0();
    __builtin_amdgcn_s_barrier();
    if (bid == 0 && w == 7) {
      wait16(rs, lane * 16, WS_FW, tag);
      if (lane == 0) st4(tag, rs, 0, WS_EP);
    }
  } else {
    u4 xa[2], xb[2];
    {
      const char* xr = (const char*)x + (srow + w * 2) * (D_ * 2);
      const char *p0 = xr + lane * 16, *p1 = xr + 1024 + l15 * 16;
      asm volatile("global_load_dwordx4 %0, %4, off\n\tglobal_load_dwordx4 %1, %5, off\n\tglobal_load_dwordx4 %2, %4, off offset:1280\n\t"
                   "global_load_dwordx4 %3, %5, off offset:1280"
                   : "=&v"(xa[0]), "=&v"(xb[0]), "=&v"(xa[1]), "=&v"(xb[1]) : "v"(p0), "v"(p1) : "memory");
      asm volatile("s_waitcnt vmcnt(0)" : "+v"(xa[0]), "+v"(xb[0]), "+v"(xa[1]), "+v"(xb[1]) :: "memory");
        float su[2] = {0.f, 0.f}, sq[2] = {0.f, 0.f}, sh[2];
#pragma unroll
        for (int n = 0; n < 2; ++n) {
          sh[n] = lo16(__builtin_amdgcn_readfirstlane(xa[n][0]));
          float s2 = 0.f, q2 = 0.f;
#pragma unroll
          for (int e = 0; e < 4; ++e) {
            const float a0 = lo16(xa[n][e]) - sh[n], a1 = hi16(xa[n][e]) - sh[n], b0 = lo16(xb[n][e]) - sh[n], b1 = hi16(xb[n][e]) - sh[n];
            su[n] += a0 + a1; sq[n] += a0 * a0; sq[n] += a1 * a1; s2 += b0 + b1; q2 += b0 * b0; q2 += b1 * b1;
          }
          if (lane < 16) { su[n] += s2; sq[n] += q2; }
        }
        const float m0 = wave_sum(su[0]) * (1.f / 640.f), m1 = wave_sum(su[1]) * (1.f / 640.f);
        const float e0 = wave_sum(sq[0]) * (1.f / 640.f), e1 = wave_sum(sq[1]) * (1.f / 640.f);
        const float m = lane ? m1 : m0, var = fmaxf((lane ? e1 : e0) - m * m, 0.f);
        const float sd = __builtin_amdgcn_sqrtf(bfr(bfr(var) + 1e-5f));
        if (lane < 2) st8(u2{pk2(m + (lane ? sh[1] : sh[0]), sd), tag}, rs, lane * 8, WS_SG + (srow + w * 2) * 8);
        if (tid == 0) { st4(tag, rs, 0, WS_FW + bid * 4); __builtin_amdgcn_raw_buffer_store_b32(0u, rs, 0, WS_F2 + bid * 128, 0); }
    }
    unsigned ra[2], rb_[7];
    {
      const unsigned lb = (unsigned)(size_t)(AS3 char*)smem;
      const int f = KV ? (((q4 >> 1) & 1) | ((g & 1) << 1)) : (q4 | ((g & 1) << 2));
#pragma unroll
      for (int s = 0; s < 2; ++s) ra[s] = lb + (wm * 64 + l15) * 128 + (((s * 4 + g) ^ (l15 >> 1)) << 4);
#pragma unroll
      for (int j = 0; j < 7; ++j) {
        const int cc = 2 * j + (p4 >> 1);
        const int pos = KV ? wn * 5 + cc + (cc >= 5 ? 5 : 0) : wn * 6 + cc;
        rb_[j] = lb + (j < JQ ? L_Q + (8 * g + q4) * (NQ * 16) + ((pos ^ ((g & 1) << 1)) << 4) + 8 * (p4 & 1)
                              : L_H + (8 * g + q4) * (NH * 16) + (((wn * (7 - JQ) + j - JQ) ^ f) << 5) + 8 * p4);
      }
    }
#pragma unroll
    for (int i = 0; i < 4; ++i)
#pragma unroll
      for (int j = 0; j < 7; ++j) acc[i][j] = zero4;
    bf8 fa[2][4], fb[2][7];
    s4 hb[2][7][2];
    auto rd1 = [&](int s, int stg, int n) {
      if (n < 4) fa[s][n] = *(const AS3 bf8*)(size_t)(ra[s] + stg * A_BYTES + n * 2048);
      else {
        const int j = (n - 4) >> 1, h = (n - 4) & 1;
        const AS3 char* p = (const AS3 char*)(size_t)rb_[j] + (j < JQ ? stg * QB + (s * 32 + 4 * h) * NQ * 16 : stg * HB + (s * 32 + 4 * h) * NH * 16);
        hb[s][j][h] = __builtin_amdgcn_ds_read_tr16_b64_v4i16((AS3 s4*)p);
        if (h) fb[s][j] = __builtin_bit_cast(bf8, __builtin_shufflevector(hb[s][j][0], hb[s][j][1], 0, 1, 2, 3, 4, 5, 6, 7));
      }
    };
    auto mm1 = [&](int s, int n) {
      const int j = n >> 2, i = n & 3;
      if (KV && j < 5) asm volatile("v_mfma_f32_16x16x32_bf16 %0, %1, %2, %0" : "+v"(acc[i][j]) : "v"(fa[s][i]), "v"(fb[s][j]));
      else asm volatile("v_mfma_f32_16x16x32_bf16 %0, %1, %2, %0" : "+v"(acc[i][j]) : "v"(fb[s][j]), "v"(fa[s][i]));
    };
    auto phase = [&](int sm, int sr, int stg, bool domm, bool dord) {
#pragma unroll
      for (int n = 0; n < 28; ++n) {
        if (domm) mm1(sm, n);
        if (dord && n < 18) rd1(sr, stg, n);
        __builtin_amdgcn_sched_barrier(0);
      }
      if (domm)
        asm volatile("" :: "v"(fa[sm][0]), "v"(fa[sm][1]), "v"(fa[sm][2]), "v"(fa[sm][3]), "v"(fb[sm][0]), "v"(fb[sm][1]), "v"(fb[sm][2]),
                     "v"(fb[sm][3]), "v"(fb[sm][4]), "v"(fb[sm][5]), "v"(fb[sm][6]));
    };
    auto body = [&](int stg, bool first) {
      __builtin_amdgcn_s_barrier();
      __builtin_amdgcn_sched_barrier(0);
      phase(1, 0, stg, !first, true);
      phase(0, 1, stg, true, true);
      WAITL0();
    };
#pragma unroll
    for (int j = 0; j < 7; ++j) asm volatile("" : "+v"(acc[0][j]), "+v"(acc[1][j]), "+v"(acc[2][j]), "+v"(acc[3][j]));
    STAMP(0, 1);
    body(0, true);
    STAMP(0, 2);
#pragma unroll 1
    for (int t = 0; t < 3; ++t) {
      body(1, false);
      body(2, false);
      body(0, false);
    }
    phase(1, 0, 0, true, false);
    asm volatile("s_nop 15" ::: "memory");
    __builtin_amdgcn_sched_barrier(0);
    STAMP(0, 4);
    __builtin_amdgcn_s_barrier();
    const char* sg = smem + L_ROW + 7168 + wm * 512;
    unsigned ms[4];
    bf8 am[4];
#pragma unroll
    for (int i = 0; i < 4; ++i) {
      ms[i] = *(const unsigned*)(sg + (i * 16 + l15) * 8);
      const unsigned nm = (ms[i] & 0xffffu) ^ 0x8000u;
      am[i] = __builtin_bit_cast(bf8, u4{nm | (nm << 16), nm, 0u, 0u});
    }
#pragma unroll
    for (int j = 0; j < 7; ++j) {
      const float c = *(const float*)(smem + L_ROW + (g * 224 + (wn * 7 + j) * 16 + l15) * 8);
      const float r1 = c - bfr(c), r2 = r1 - bfr(r1);
      const bf8 bm = __builtin_bit_cast(bf8, u4{pk2(c, r1), pk2(r2, 0.f), 0u, 0u});
#pragma unroll
      for (int i = 0; i < 4; ++i) acc[i][j] = (KV && j < 5) ? mfma(am[i], bm, acc[i][j]) : mfma(bm, am[i], acc[i][j]);
    }
    STAMP(0, 5);
    const int rt0 = rb * 8 + wm * 4;
    typedef __attribute__((ext_vector_type(2))) short s2_t;
    auto relu2 = [&](float a, float b) {
      s2_t v = __builtin_bit_cast(s2_t, pk2(a, b));
      return __builtin_bit_cast(unsigned, __builtin_elementwise_max(v, s2_t{0, 0}));
    };
#pragma unroll
    for (int i = 0; i < 4; ++i) {
      const float rstd = __builtin_amdgcn_rcpf(hi16(ms[i]));
#pragma unroll
      for (int j = JQ; j < 7; j += 2) {
        const int ks = (KV ? 1024 + 64 * ci + 32 * wn : 128 * ci + 64 * wn + 16 * (j - JQ)) >> 5;
        const f4 y0 = acc[i][j] * rstd, y1 = acc[i][j + 1] * rstd;
        pst16(u4{relu2(y0[0], y0[1]), relu2(y0[2], y0[3]), relu2(y1[0], y1[1]), relu2(y1[2], y1[3])}, rs, lane * 16,
              WS_HF + (ks * 128 + rt0 + i) * 1024);
      }
      if (!KV) {
#pragma unroll
        for (int j = 0; j < 3; ++j) {
          if (j == 2 && wn) break;
          const int col = 80 * ci + 48 * wn + 16 * j;
          const f4 y = acc[i][j] * rstd;
          pst8(u2{pk2(phi(y[0]), phi(y[1])), pk2(phi(y[2]), phi(y[3]))}, rs, lane * 16,
               WS_PQ + ((col >> 5) * 128 + rt0 + i) * 1024 + ((col >> 4) & 1) * 8);
        }
      }
    }
    STAMP(0, 6);
    if (KV) {
      f4 kv[8];
#pragma unroll
      for (int hp = 0; hp < 2; ++hp) {
        u4 fr[6];
#pragma unroll
        for (int ii = 0; ii < 2; ++ii) {
          const int i = hp * 2 + ii;
          const u4 s0 = *(const u4*)(sg + (i * 16 + 4 * g) * 8), s1 = *(const u4*)(sg + (i * 16 + 4 * g) * 8 + 16);
          const f4 rr = {__builtin_amdgcn_rcpf(hi16(s0[0])), __builtin_amdgcn_rcpf(hi16(s0[2])), __builtin_amdgcn_rcpf(hi16(s1[0])),
                         __builtin_amdgcn_rcpf(hi16(s1[2]))};
#pragma unroll
          for (int j = 0; j < 5; ++j) {
            f4 y = acc[i][j] * rr;
            if (j < 3) {
#pragma unroll
              for (int e = 0; e < 4; ++e) y[e] = (j < 2 || l15 < 8) ? phi(y[e]) : y[e];
            }
            fr[j][2 * ii] = pk2(y[0], y[1]);
            fr[j][2 * ii + 1] = pk2(y[2], y[3]);
          }
        }
#pragma unroll
        for (int e = 0; e < 4; ++e) fr[5][e] = l15 < 8 ? 0x3f803f80u : fr[2][e];
        const int ta[8] = {0, 0, 1, 1, 1, 2, 2, 2}, tb[8] = {5, 3, 5, 3, 4, 5, 3, 4};
#pragma unroll
        for (int m = 0; m < 8; ++m) kv[m] = mfma(__builtin_bit_cast(bf8, fr[tb[m]]), __builtin_bit_cast(bf8, fr[ta[m]]), hp ? kv[m] : zero4);
      }
#pragma unroll
      for (int m = 0; m < 8; ++m) *(f4*)(smem + (w * 8 + m) * 1024 + lane * 16) = kv[m];
      WAITL0();
    }
  }
  if (KV) {
    __builtin_amdgcn_s_barrier();
    if (tid < 480) {
      const bool isz = tid >= 400;
      int hl = isz ? (tid - 400) / 20 : tid / 100, rem = isz ? (tid - 400 - hl * 20) * 5 : tid - hl * 100, d = rem / 5, grp = rem - d * 5;
      int hk = hl & 1, kc = 20 * hk + d, a = kc >> 4;
      int pos = isz ? 32 : 40 + 20 * hk + 4 * grp, b = pos >> 4;
      int m = a == 0 ? b - 2 : (a == 1 ? b : b + 3);
      const char* p = smem + ((hl >> 1) * 8 + m) * 1024 + (((pos >> 2) & 3) * 16 + (kc & 15)) * 16;
      f4 v = *(const f4*)p + *(const f4*)(p + 16384);
      if (isz) __builtin_amdgcn_raw_buffer_store_b32(__builtin_bit_cast(unsigned, v[0]), rs, (tid - 400) * 4, WS_Z + (rb * 640 + ci * 80) * 4, 0);
      else pst16(__builtin_bit_cast(u4, v), rs, tid * 16, WS_KV + (rb * 32 + ci * 4) * 1600);
    }
  }
  STAMP(0, 7);
}

__global__ __launch_bounds__(512, 2) void k1_kernel(const __bf16* __restrict__ x, const __bf16* __restrict__ wqkv,
                                                    const __bf16* __restrict__ w1, const __bf16* __restrict__ w2, char* ws) {
  extern __shared__ __attribute__((aligned(16))) char smem[];
  const int bid = blockIdx.x, xcd = bid & 7, idx = bid >> 3;
  STAMP(0, 0);
  const char* pc;
  asm volatile("s_getpc_b64 %0" : "=s"(pc));
  const int rb = (xcd >> 2) * 8 + (idx & 7), cgi = idx >> 3;
  const int ci = (xcd & 3) * 2 + (cgi >> 1);
  if (cgi & 1) k1_body<false>(x, wqkv, w1, w2, ws, smem, rb, ci, bid, pc);
  else k1_body<true>(x, wqkv, w1, w2, ws, smem, rb, ci, bid, pc);
}

template <int AUX>
DI bf8 ldfrag(rsrc_t r, int voff, int soff) {
  return __builtin_bit_cast(bf8, __builtin_amdgcn_raw_buffer_load_b128(r, voff, soff, AUX));
}
DI void st8_sc1(char* p, unsigned a, unsigned b) {
  __hip_atomic_store((AS1 unsigned long long*)p, (unsigned long long)a | ((unsigned long long)b << 32), __ATOMIC_RELAXED,
                     __HIP_MEMORY_SCOPE_AGENT);
}
constexpr int L2_ACC = 0;
constexpr int L2_WO = 61440;
constexpr int L2_KV = L2_WO + 5120;
constexpr int L2_Z = L2_KV + 2048;
constexpr int L2_CNT = L2_Z + 2048;
constexpr int K2_LDS = 98304;

#define RING 3
DI void k2_publish(char* ws, char* smem, int flag, int lane) {
  WAITV(0);
  if (lane == 0) {
    unsigned old = __hip_atomic_fetch_add((AS3 unsigned*)(smem + L2_CNT), 1u, __ATOMIC_RELAXED, __HIP_MEMORY_SCOPE_WORKGROUP);
    if (old == 2u) __hip_atomic_store((AS1 unsigned*)(ws + WS_F2 + flag * 128), 1u, __ATOMIC_RELAXED, __HIP_MEMORY_SCOPE_AGENT);
  }
}
__global__ __launch_bounds__(512, 2) void k2_kernel(const __bf16* __restrict__ x, const __bf16* __restrict__ wo,
                                                    char* ws, __bf16* __restrict__ out) {
  extern __shared__ __attribute__((aligned(16))) char smem[];
  const int tid = threadIdx.x, lane = tid & 63, w = __builtin_amdgcn_readfirstlane(tid >> 6);
  const int g = lane >> 4, l15 = lane & 15;
  const int xcd = blockIdx.x & 7, idx = blockIdx.x >> 3;
  const int r = (xcd >> 1) * 8 + (idx & 7), c = (xcd & 1) * 4 + (idx >> 3);
  STAMP(1, 0);
  if (tid == 0) *(unsigned*)(smem + L2_CNT) = 0u;
  WAITL0();
  __builtin_amdgcn_s_barrier();
  const rsrc_t rs = mkrsrc(ws, (unsigned)WS_END);
  const int vo = lane * 16;
  const f4 zero4 = {0.f, 0.f, 0.f, 0.f};
  if (w >= 4) __builtin_amdgcn_s_setprio(3);
  if (w == 4) {
    const f4* p = (const f4*)(ws + WS_KV) + r * 100;
    const int i1 = lane < 36 ? lane + 64 : lane;
    f4 v0[16], v1[16];
#pragma unroll
    for (int i = 0; i < 16; ++i) { v0[i] = p[i * 3200 + lane]; v1[i] = p[i * 3200 + i1]; }
    u4 wv[4];
#pragma unroll
    for (int i = 0; i < 4; ++i) {
      int ch = i * 64 + lane, row = ch / 10;
      wv[i] = u4{0u, 0u, 0u, 0u};
      if (ch < 200) wv[i] = *(const u4*)(wo + (20 * r + row) * D_ + 80 * c + (ch - row * 10) * 8);
    }
#pragma unroll
    for (int st = 1; st < 16; st *= 2)
#pragma unroll
      for (int i = 0; i < 16; i += 2 * st) { v0[i] += v0[i + st]; v1[i] += v1[i + st]; }
    *(f4*)(smem + L2_KV + vo) = v0[0];
    *(f4*)(smem + L2_KV + 1024 + vo) = v1[0];
#pragma unroll
    for (int i = 0; i < 4; ++i) *(u4*)(smem + L2_WO + i * 1024 + vo) = wv[i];
    *(u4*)(smem + L2_WO + 4096 + vo) = u4{0u, 0u, 0u, 0u};
    u4 au[2];
#pragma unroll
    for (int dt = 0; dt < 2; ++dt)
#pragma unroll
      for (int hh = 0; hh < 2; ++hh) {
        int d = dt * 16 + l15, grp = 2 * g + hh;
        bool ok = d < 20 && grp < 5;
        f4 v = *(const f4*)(smem + L2_KV + (ok ? d * 5 + grp : 0) * 16);
        au[dt][2 * hh] = ok ? pk2(v[0], v[1]) : 0u;
        au[dt][2 * hh + 1] = ok ? pk2(v[2], v[3]) : 0u;
      }
    const int k0 = 20 * r + 4 * g, k1 = 20 * r + 16;
    const char* p0 = smem + L2_WO + (8 * g + (l15 >> 2)) * 160 + (lane & 3) * 8;
    char* mf = ws + WS_MF + c * 5120 + l15 * 16;
    const int o0 = (k0 >> 5) * 40960 + ((k0 >> 2) & 3) * 256 + ((k0 >> 4) & 1) * 8;
    const int o1 = (k1 >> 5) * 40960 + ((k1 >> 2) & 3) * 256 + ((k1 >> 4) & 1) * 8;
#pragma unroll
    for (int t = 0; t < 5; ++t) {
      bf8 b = trfrag(p0 + t * 32, p0 + t * 32 + 640);
      f4 m0 = mfma(__builtin_bit_cast(bf8, au[0]), b, zero4), m1 = mfma(__builtin_bit_cast(bf8, au[1]), b, zero4);
      st8_sc1(mf + t * 1024 + o0, pk2(m0[0], m0[1]), pk2(m0[2], m0[3]));
      if (g == 0) st8_sc1(mf + t * 1024 + o1, pk2(m1[0], m1[1]), pk2(m1[2], m1[3]));
    }
    k2_publish(ws, smem, r * 8 + c, lane);
    STAMPW(1, 8);
  } else if (w == 5 || w == 6) {
    const int fo = ((c >> 1) * 5 * 128 + r * 4 + (c & 1) * 2 + w - 5) * 1024;
    const f4* zp = (const f4*)(ws + WS_Z) + (c >> 1) * 40 + (lane < 40 ? lane : 0);
    f4 zv[16];
#pragma unroll
    for (int i = 0; i < 16; ++i) zv[i] = zp[i * 160];
    u4 pq[5];
#pragma unroll
    for (int k = 0; k < 5; ++k) pq[k] = __builtin_bit_cast(u4, ldfrag<0>(rs, vo, (int)WS_PQ + fo + k * 131072));
#pragma unroll
    for (int st = 1; st < 16; st *= 2)
#pragma unroll
      for (int i = 0; i < 16; i += 2 * st) zv[i] += zv[i + st];
    char* zl = smem + L2_Z + (w - 5) * 1024;
    if (lane < 40) *(u2*)(zl + lane * 8) = u2{pk2(zv[0][0], zv[0][1]), pk2(zv[0][2], zv[0][3])};
    f4 den = zero4;
#pragma unroll
    for (int k = 0; k < 5; ++k) {
      u4 zf;
#pragma unroll
      for (int p = 0; p < 2; ++p) {
        int gi = 8 * k + 4 * p + g;
        u2 z2 = *(const u2*)(zl + gi * 8);
        bool mine = gi / 5 == l15;
        zf[2 * p] = mine ? z2[0] : 0u;
        zf[2 * p + 1] = mine ? z2[1] : 0u;
      }
      den = mfma(__builtin_bit_cast(bf8, zf), __builtin_bit_cast(bf8, pq[k]), den);
    }
    f4 rd;
#pragma unroll
    for (int e = 0; e < 4; ++e) rd[e] = __builtin_amdgcn_rcpf(bfr(bfr(den[e]) + 1e-6f));
    if (g < 2) *(f4*)(zl + 512 + l15 * 32 + g * 16) = rd;
#pragma unroll
    for (int k = 0; k < 5; ++k) {
      u4 o;
#pragma unroll
      for (int p = 0; p < 2; ++p) {
        float s = *(const float*)(zl + 512 + l15 * 32 + ((8 * k + 4 * p + g) / 5) * 4);
        o[2 * p] = pk2(lo16(pq[k][2 * p]) * s, hi16(pq[k][2 * p]) * s);
        o[2 * p + 1] = pk2(lo16(pq[k][2 * p + 1]) * s, hi16(pq[k][2 * p + 1]) * s);
      }
      st16(o, rs, vo, WS_AF + fo + k * 131072);
    }
    k2_publish(ws, smem, r * 8 + c, lane);
    STAMPW(1, 9);
  }
  if (w >= 4) {
    __builtin_amdgcn_s_barrier();
    return;
  }
  const int hA = (int)WS_HF + (w * 128 + r * 4) * 1024, hB = (int)WS_W2 + (w * 8 + c) * 5120;
  const int aA = (int)WS_AF + (w * 128 + r * 4) * 1024, aB = (int)WS_MF + (w * 8 + c) * 5120;
#define ROT 0
  const int rot = ROT ? ((idx >> 3) * 3 + (idx & 7) * 5) % 12 : 0, rot2 = ROT ? idx % 5 : 0;
  bf8 fa[RING][4], fb[RING][5];
  auto ld = [&](int i) {
    const int sl = i % RING;
    if (i < 12) {
      int ii = i + rot;
      ii = ii >= 12 ? ii - 12 : ii;
#pragma unroll
      for (int j = 0; j < 5; ++j) fb[sl][j] = ldfrag<LAUX>(rs, vo + j * 1024, hB + ii * 163840);
#pragma unroll
      for (int j = 0; j < 4; ++j) fa[sl][j] = ldfrag<LAUX>(rs, vo + j * 1024, hA + ii * 524288);
    } else {
      int ii = i - 12 + rot2;
      ii = ii >= 5 ? ii - 5 : ii;
#pragma unroll
      for (int j = 0; j < 5; ++j) fb[sl][j] = ldfrag<16>(rs, vo + j * 1024, aB + ii * 163840);
#pragma unroll
      for (int j = 0; j < 4; ++j) fa[sl][j] = ldfrag<16>(rs, vo + j * 1024, aA + ii * 524288);
    }
  };
#pragma unroll
  for (int i = 0; i < RING; ++i) ld(i);
  const int orow = (r * 64 + w * 16 + l15) * D_ + c * 80 + 4 * g;
  u2 xr[5];
#pragma unroll
  for (int j = 0; j < 5; ++j) xr[j] = *(const u2*)(x + orow + 16 * j);
  const AS1 unsigned* fl = (const AS1 unsigned*)(ws + WS_F2 + (lane < 8 ? r * 8 + lane : (lane < 40 ? (lane - 8) * 8 + c : r * 8 + c)) * 128);
  STAMP(1, 1);
  __builtin_amdgcn_sched_barrier(0);
  f4 acc[4][5];
#pragma unroll
  for (int i = 0; i < 4; ++i)
#pragma unroll
    for (int j = 0; j < 5; ++j) acc[i][j] = zero4;
  unsigned flv = 0;
#pragma unroll
  for (int i = 0; i < 17; ++i) {
    const int sl = i % RING;
#pragma unroll
    for (int j = 0; j < 5; ++j)
#pragma unroll
      for (int m = 0; m < 4; ++m) acc[m][j] = mfma(fb[sl][j], fa[sl][m], acc[m][j]);
    if (i + RING == 11) flv = __hip_atomic_load(fl, __ATOMIC_RELAXED, __HIP_MEMORY_SCOPE_AGENT);
    if (i + RING == 12) {
      for (unsigned spin = 0; !__all(flv == 1u) && spin < (1u << 22); ++spin) {
        __builtin_amdgcn_s_sleep(1);
        flv = __hip_atomic_load(fl, __ATOMIC_RELAXED, __HIP_MEMORY_SCOPE_AGENT);
      }
      __builtin_amdgcn_fence(__ATOMIC_ACQUIRE, "wavefront");
    }
    if (i + RING < 17) ld(i + RING);
    __builtin_amdgcn_sched_barrier(0);
  }
  STAMP(1, 6);
#pragma unroll
  for (int m = 0; m < 4; ++m) {
    if (m != w) {
      int slot = m < w ? m : m - 1;
#pragma unroll
      for (int j = 0; j < 5; ++j) *(f4*)(smem + L2_ACC + ((w * 3 + slot) * 5 + j) * 1024 + vo) = acc[m][j];
    }
  }
  WAITL0();
  __builtin_amdgcn_s_barrier();
  f4 own[5];
#pragma unroll
  for (int j = 0; j < 5; ++j) own[j] = w == 0 ? acc[0][j] : (w == 1 ? acc[1][j] : (w == 2 ? acc[2][j] : acc[3][j]));
  f4 tot[5];
#pragma unroll
  for (int j = 0; j < 5; ++j) tot[j] = f4{lo16(xr[j][0]), hi16(xr[j][0]), lo16(xr[j][1]), hi16(xr[j][1])};
#pragma unroll
  for (int s = 0; s < 4; ++s) {
    int slot = w < s ? w : w - 1;
#pragma unroll
    for (int j = 0; j < 5; ++j) {
      f4 v = own[j];
      if (s != w) v = *(const f4*)(smem + L2_ACC + ((s * 3 + slot) * 5 + j) * 1024 + vo);
      tot[j] += v;
    }
  }
#pragma unroll
  for (int j = 0; j < 5; ++j) *(u2*)(out + orow + 16 * j) = u2{pk2(tot[j][0], tot[j][1]), pk2(tot[j][2], tot[j][3])};
  STAMP(1, 7);
}

extern "C" void kernel_launch(void* const* d_in, const int* in_sizes, int n_in, void* d_out, int out_size, void* d_ws,
                              size_t ws_size, hipStream_t stream) {
  const __bf16* x = (const __bf16*)d_in[0];
  const __bf16* wqkv = (const __bf16*)d_in[1];
  const __bf16* wo = (const __bf16*)d_in[2];
  const __bf16* w1 = (const __bf16*)d_in[4];
  const __bf16* w2 = (const __bf16*)d_in[5];
  char* ws = (char*)d_ws;
  (void)hipFuncSetAttribute((const void*)k1_kernel, hipFuncAttributeMaxDynamicSharedMemorySize, K1_LDS);
  (void)hipFuncSetAttribute((const void*)k2_kernel, hipFuncAttributeMaxDynamicSharedMemorySize, K2_LDS);
  k1_kernel<<<256, 512, K1_LDS, stream>>>(x, wqkv, w1, w2, ws);
  k2_kernel<<<256, 512, K2_LDS, stream>>>(x, wo, ws, (__bf16*)d_out);
}
```

```cpp
#include <hip/hip_runtime.h>
#include <hip/hip_bf16.h>
#include <stdint.h>
#include <type_traits>

typedef __attribute__((ext_vector_type(8))) __bf16 bf8;
typedef __attribute__((ext_vector_type(4))) __bf16 bf4;
typedef __attribute__((ext_vector_type(2))) __bf16 bf2;
typedef __attribute__((ext_vector_type(4))) short s4;
typedef __attribute__((ext_vector_type(4))) float f4;
typedef __attribute__((ext_vector_type(2))) float f2;
typedef __attribute__((ext_vector_type(4))) unsigned u4;
typedef __attribute__((ext_vector_type(2))) unsigned u2;
#define AS1 __attribute__((address_space(1)))
#define AS3 __attribute__((address_space(3)))
#define DI __device__ __forceinline__

constexpr int S_ = 2048, D_ = 640, DFF = 1536, N3 = 1920;
constexpr int WS_HF = 0;
constexpr int WS_PQ = WS_HF + 128 * 48 * 1024;
constexpr int WS_AF = WS_PQ + 128 * 20 * 1024;
constexpr int WS_W2 = WS_AF + 128 * 20 * 1024;
constexpr int WS_MF = WS_W2 + 8 * 48 * 5 * 1024;
constexpr int WS_KV = WS_MF + 8 * 20 * 5 * 1024;
constexpr int WS_Z = WS_KV + 16 * 32 * 400 * 4;
constexpr int WS_CG = WS_Z + 16 * 640 * 4;
constexpr int WS_SG = WS_CG + 4 * 3456 * 8;
constexpr int WS_FW = WS_SG + 2048 * 8;
constexpr int WS_F1 = WS_FW + 1024;
constexpr int WS_F2 = WS_F1 + 1024;
constexpr int WS_EP = WS_F2 + 256 * 128;
constexpr int WS_END = WS_EP + 128;
#define STAMPW(k, i) do {} while (0)
#define STAMP(k, i) do {} while (0)

DI float bfr(float v) { return (float)(__bf16)v; }
DI unsigned pk2(float a, float b) { f2 v = {a, b}; return __builtin_bit_cast(unsigned, __builtin_convertvector(v, bf2)); }
DI float lo16(unsigned u) { return __builtin_bit_cast(float, u << 16); }
DI float hi16(unsigned u) { return __builtin_bit_cast(float, u & 0xffff0000u); }
DI f4 mfma(bf8 a, bf8 b, f4 c) { return __builtin_amdgcn_mfma_f32_16x16x32_bf16(a, b, c, 0, 0, 0); }
DI void glds16(const void* g, char* l) {
  __builtin_amdgcn_global_load_lds((const AS1 void*)g, (AS3 void*)l, 16, 0, 0);
}
DI bf8 trfrag(const char* p0, const char* p1) {
  s4 a = __builtin_amdgcn_ds_read_tr16_b64_v4i16((AS3 s4*)p0);
  s4 b = __builtin_amdgcn_ds_read_tr16_b64_v4i16((AS3 s4*)p1);
  return __builtin_bit_cast(bf8, __builtin_shufflevector(a, b, 0, 1, 2, 3, 4, 5, 6, 7));
}
DI bf8 trfrag3(const AS3 char* p0, const AS3 char* p1) {
  s4 a = __builtin_amdgcn_ds_read_tr16_b64_v4i16((AS3 s4*)p0);
  s4 b = __builtin_amdgcn_ds_read_tr16_b64_v4i16((AS3 s4*)p1);
  return __builtin_bit_cast(bf8, __builtin_shufflevector(a, b, 0, 1, 2, 3, 4, 5, 6, 7));
}
DI float phi(float t) {
  return fmaxf(t, 0.f) + 1e-6f + __builtin_amdgcn_exp2f(fminf(t, 0.f) * 1.44269504088896341f);
}
#define MT(i) do {} while (0)
#define WAITV(n) asm volatile("s_waitcnt vmcnt(" #n ")" ::: "memory")
#define WAITL0() asm volatile("s_waitcnt lgkmcnt(0)" ::: "memory")

#define PAUX 16
#define LAUX 0
typedef __amdgpu_buffer_rsrc_t rsrc_t;
DI rsrc_t mkrsrc(const void* p, unsigned bytes) { return __builtin_amdgcn_make_buffer_rsrc((void*)p, 0, bytes, 0x00020000); }
template <int AUX>
DI void blds16(rsrc_t r, char* l, int voff, int soff) {
  __builtin_amdgcn_raw_ptr_buffer_load_lds(r, (AS3 void*)l, 16, voff, soff, 0, AUX);
}
DI u4 ld16(rsrc_t r, int voff, int soff) { return __builtin_amdgcn_raw_buffer_load_b128(r, voff, soff, 16); }
DI void st16(u4 v, rsrc_t r, int voff, int soff) { __builtin_amdgcn_raw_buffer_store_b128(v, r, voff + soff, 0, 16); }
DI void pst16(u4 v, rsrc_t r, int voff, int soff) { __builtin_amdgcn_raw_buffer_store_b128(v, r, voff + soff, 0, PAUX); }
DI void pst8(u2 v, rsrc_t r, int voff, int soff) { __builtin_amdgcn_raw_buffer_store_b64(v, r, voff, soff, PAUX); }
DI void st8(u2 v, rsrc_t r, int voff, int soff) { __builtin_amdgcn_raw_buffer_store_b64(v, r, voff, soff, 16); }
DI void st4(unsigned v, rsrc_t r, int voff, int soff) { __builtin_amdgcn_raw_buffer_store_b32(v, r, voff, soff, 16); }
DI unsigned ld4(rsrc_t r, int voff, int soff) { return __builtin_amdgcn_raw_buffer_load_b32(r, voff, soff, 16); }
DI float wave_sum(float v) {
#define DPP_ADD(c) v += __builtin_bit_cast(float, __builtin_amdgcn_update_dpp(0, __builtin_bit_cast(int, v), c, 0xf, 0xf, true))
  DPP_ADD(0x111); DPP_ADD(0x112); DPP_ADD(0x114); DPP_ADD(0x118);
#undef DPP_ADD
  const int u = __builtin_bit_cast(int, v);
  return (__builtin_bit_cast(float, __builtin_amdgcn_readlane(u, 15)) + __builtin_bit_cast(float, __builtin_amdgcn_readlane(u, 31))) +
         (__builtin_bit_cast(float, __builtin_amdgcn_readlane(u, 47)) + __builtin_bit_cast(float, __builtin_amdgcn_readlane(u, 63)));
}
constexpr unsigned SPIN = 1u << 20;
#define PSLEEP 8
DI void wait16(rsrc_t rs, int voff, int soff, unsigned tag) {
#pragma unroll 1
  for (unsigned spin = 0; spin < SPIN; ++spin) {
    asm volatile("" : "+v"(voff) :: "memory");
    u4 v = ld16(rs, voff, soff);
    if (__all(v[0] == tag && v[1] == tag && v[2] == tag && v[3] == tag)) break;
    __builtin_amdgcn_s_sleep(PSLEEP);
  }
  asm volatile("" ::: "memory");
  __builtin_amdgcn_fence(__ATOMIC_ACQUIRE, "wavefront");
}
DI void wait4(rsrc_t rs, int voff, int soff, unsigned tag) {
#pragma unroll 1
  for (unsigned spin = 0; spin < SPIN; ++spin) {
    asm volatile("" : "+v"(voff) :: "memory");
    if (__all(ld4(rs, voff, soff) == tag)) break;
    __builtin_amdgcn_s_sleep(PSLEEP);
  }
  asm volatile("" ::: "memory");
  __builtin_amdgcn_fence(__ATOMIC_ACQUIRE, "wavefront");
}

DI void wait_lds(char* p, unsigned lvl) {
#pragma unroll 1
  for (unsigned spin = 0; spin < SPIN; ++spin) {
    if (__hip_atomic_load((AS3 unsigned*)p, __ATOMIC_RELAXED, __HIP_MEMORY_SCOPE_WORKGROUP) >= lvl) break;
    __builtin_amdgcn_s_sleep(1);
  }
  asm volatile("" ::: "memory");
  __builtin_amdgcn_fence(__ATOMIC_ACQUIRE, "wavefront");
}
DI void set_lds(char* p, unsigned lvl) { __hip_atomic_store((AS3 unsigned*)p, lvl, __ATOMIC_RELAXED, __HIP_MEMORY_SCOPE_WORKGROUP); }

constexpr int A_BYTES = 128 * 128;
constexpr int L_Q = 3 * A_BYTES;
constexpr int L_CB = L_Q + 3 * 64 * 448;
constexpr int L_ROW = L_CB + 20480;
constexpr int K1_LDS = L_ROW + 8192;

template <bool KV>
DI void k1_compute(char* ws, char* smem, int rb, int ci) {
  constexpr int NQ = KV ? 20 : 12, NH = KV ? 8 : 16;
  constexpr int QB = 64 * NQ * 16, HB = 64 * NH * 16;
  constexpr int L_H = L_Q + 3 * QB;
  constexpr int JQ = KV ? 5 : 3;
  const int lane = threadIdx.x & 63;
  const int w = __builtin_amdgcn_readfirstlane(threadIdx.x >> 6), wm = (w >> 1) & 1, wn = w & 1;
  const int g = lane >> 4, l15 = lane & 15, q4 = l15 >> 2, p4 = lane & 3;
  const rsrc_t rs = mkrsrc(ws, WS_END);
  const f4 zero4 = {0.f, 0.f, 0.f, 0.f};
  f4 acc[4][7];
    unsigned ra[2], rb_[7];
    {
      const unsigned lb = (unsigned)(size_t)(AS3 char*)smem;
      const int f = KV ? (((q4 >> 1) & 1) | ((g & 1) << 1)) : (q4 | ((g & 1) << 2));
#pragma unroll
      for (int s = 0; s < 2; ++s) ra[s] = lb + (wm * 64 + l15) * 128 + (((s * 4 + g) ^ (l15 >> 1)) << 4);
#pragma unroll
      for (int j = 0; j < 7; ++j) {
        const int cc = 2 * j + (p4 >> 1);
        const int pos = KV ? wn * 5 + cc + (cc >= 5 ? 5 : 0) : wn * 6 + cc;
        rb_[j] = lb + (j < JQ ? L_Q + (8 * g + q4) * (NQ * 16) + ((pos ^ ((g & 1) << 1)) << 4) + 8 * (p4 & 1)
                              : L_H + (8 * g + q4) * (NH * 16) + (((wn * (7 - JQ) + j - JQ) ^ f) << 5) + 8 * p4);
      }
    }
#pragma unroll
    for (int i = 0; i < 4; ++i)
#pragma unroll
      for (int j = 0; j < 7; ++j) acc[i][j] = zero4;
    bf8 fa[2][4], fb[2][7];
    s4 hb[2][7][2];
    auto rd1 = [&](int s, int stg, int n) {
      if (n < 4) fa[s][n] = *(const AS3 bf8*)(size_t)(ra[s] + stg * A_BYTES + n * 2048);
      else {
        const int j = (n - 4) >> 1, h = (n - 4) & 1;
        const AS3 char* p = (const AS3 char*)(size_t)rb_[j] + (j < JQ ? stg * QB + (s * 32 + 4 * h) * NQ * 16 : stg * HB + (s * 32 + 4 * h) * NH * 16);
        hb[s][j][h] = __builtin_amdgcn_ds_read_tr16_b64_v4i16((AS3 s4*)p);
        if (h) fb[s][j] = __builtin_bit_cast(bf8, __builtin_shufflevector(hb[s][j][0], hb[s][j][1], 0, 1, 2, 3, 4, 5, 6, 7));
      }
    };
    auto mm1 = [&](int s, int n) {
      const int j = n >> 2, i = n & 3;
      if (KV && j < 5) asm volatile("v_mfma_f32_16x16x32_bf16 %0, %1, %2, %0" : "+v"(acc[i][j]) : "v"(fa[s][i]), "v"(fb[s][j]));
      else asm volatile("v_mfma_f32_16x16x32_bf16 %0, %1, %2, %0" : "+v"(acc[i][j]) : "v"(fb[s][j]), "v"(fa[s][i]));
    };
    auto phase = [&](int sm, int sr, int stg, bool domm, bool dord) {
#pragma unroll
      for (int n = 0; n < 28; ++n) {
        if (domm) mm1(sm, n);
        if (dord && n < 18) rd1(sr, stg, n);
        __builtin_amdgcn_sched_barrier(0);
      }
      if (domm)
        asm volatile("" :: "v"(fa[sm][0]), "v"(fa[sm][1]), "v"(fa[sm][2]), "v"(fa[sm][3]), "v"(fb[sm][0]), "v"(fb[sm][1]), "v"(fb[sm][2]),
                     "v"(fb[sm][3]), "v"(fb[sm][4]), "v"(fb[sm][5]), "v"(fb[sm][6]));
    };
    auto body = [&](int stg, bool first) {
      __builtin_amdgcn_s_barrier();
      __builtin_amdgcn_sched_barrier(0);
      phase(1, 0, stg, !first, true);
      phase(0, 1, stg, true, true);
      WAITL0();
    };
#pragma unroll
    for (int j = 0; j < 7; ++j) asm volatile("" : "+v"(acc[0][j]), "+v"(acc[1][j]), "+v"(acc[2][j]), "+v"(acc[3][j]));
    STAMP(0, 1);
    body(0, true);
    STAMP(0, 2);
#pragma unroll 1
    for (int t = 0; t < 3; ++t) {
      body(1, false);
      body(2, false);
      body(0, false);
    }
    phase(1, 0, 0, true, false);
    asm volatile("s_nop 15" ::: "memory");
    __builtin_amdgcn_sched_barrier(0);
    STAMP(0, 4);
    __builtin_amdgcn_s_barrier();
    const char* sg = smem + L_ROW + 7168 + wm * 512;
    unsigned ms[4];
    bf8 am[4];
#pragma unroll
    for (int i = 0; i < 4; ++i) {
      ms[i] = *(const unsigned*)(sg + (i * 16 + l15) * 8);
      const unsigned nm = (ms[i] & 0xffffu) ^ 0x8000u;
      am[i] = __builtin_bit_cast(bf8, u4{nm | (nm << 16), nm, 0u, 0u});
    }
#pragma unroll
    for (int j = 0; j < 7; ++j) {
      const float c = *(const float*)(smem + L_ROW + (g * 224 + (wn * 7 + j) * 16 + l15) * 8);
      const float r1 = c - bfr(c), r2 = r1 - bfr(r1);
      const bf8 bm = __builtin_bit_cast(bf8, u4{pk2(c, r1), pk2(r2, 0.f), 0u, 0u});
#pragma unroll
      for (int i = 0; i < 4; ++i) acc[i][j] = (KV && j < 5) ? mfma(am[i], bm, acc[i][j]) : mfma(bm, am[i], acc[i][j]);
    }
    STAMP(0, 5);
    const int rt0 = rb * 8 + wm * 4;
    typedef __attribute__((ext_vector_type(2))) short s2_t;
    auto relu2 = [&](float a, float b) {
      s2_t v = __builtin_bit_cast(s2_t, pk2(a, b));
      return __builtin_bit_cast(unsigned, __builtin_elementwise_max(v, s2_t{0, 0}));
    };
#pragma unroll
    for (int i = 0; i < 4; ++i) {
      const float rstd = __builtin_amdgcn_rcpf(hi16(ms[i]));
#pragma unroll
      for (int j = JQ; j < 7; j += 2) {
        const int ks = (KV ? 1024 + 64 * ci + 32 * wn : 128 * ci + 64 * wn + 16 * (j - JQ)) >> 5;
        const f4 y0 = acc[i][j] * rstd, y1 = acc[i][j + 1] * rstd;
        pst16(u4{relu2(y0[0], y0[1]), relu2(y0[2], y0[3]), relu2(y1[0], y1[1]), relu2(y1[2], y1[3])}, rs, lane * 16,
              WS_HF + (ks * 128 + rt0 + i) * 1024);
      }
      if (!KV) {
#pragma unroll
        for (int j = 0; j < 3; ++j) {
          if (j == 2 && wn) break;
          const int col = 80 * ci + 48 * wn + 16 * j;
          const f4 y = acc[i][j] * rstd;
          pst8(u2{pk2(phi(y[0]), phi(y[1])), pk2(phi(y[2]), phi(y[3]))}, rs, lane * 16,
               WS_PQ + ((col >> 5) * 128 + rt0 + i) * 1024 + ((col >> 4) & 1) * 8);
        }
      }
    }
    STAMP(0, 6);
    if (KV) {
      f4 kv[8];
#pragma unroll
      for (int hp = 0; hp < 2; ++hp) {
        u4 fr[6];
#pragma unroll
        for (int ii = 0; ii < 2; ++ii) {
          const int i = hp * 2 + ii;
          const u4 s0 = *(const u4*)(sg + (i * 16 + 4 * g) * 8), s1 = *(const u4*)(sg + (i * 16 + 4 * g) * 8 + 16);
          const f4 rr = {__builtin_amdgcn_rcpf(hi16(s0[0])), __builtin_amdgcn_rcpf(hi16(s0[2])), __builtin_amdgcn_rcpf(hi16(s1[0])),
                         __builtin_amdgcn_rcpf(hi16(s1[2]))};
#pragma unroll
          for (int j = 0; j < 5; ++j) {
            f4 y = acc[i][j] * rr;
            if (j < 3) {
#pragma unroll
              for (int e = 0; e < 4; ++e) y[e] = (j < 2 || l15 < 8) ? phi(y[e]) : y[e];
            }
            fr[j][2 * ii] = pk2(y[0], y[1]);
            fr[j][2 * ii + 1] = pk2(y[2], y[3]);
          }
        }
#pragma unroll
        for (int e = 0; e < 4; ++e) fr[5][e] = l15 < 8 ? 0x3f803f80u : fr[2][e];
        const int ta[8] = {0, 0, 1, 1, 1, 2, 2, 2}, tb[8] = {5, 3, 5, 3, 4, 5, 3, 4};
#pragma unroll
        for (int m = 0; m < 8; ++m) kv[m] = mfma(__builtin_bit_cast(bf8, fr[tb[m]]), __builtin_bit_cast(bf8, fr[ta[m]]), hp ? kv[m] : zero4);
      }
#pragma unroll
      for (int m = 0; m < 8; ++m) *(f4*)(smem + (w * 8 + m) * 1024 + lane * 16) = kv[m];
      WAITL0();
    }
}

constexpr int CODE_LINES = 300;
__global__ __launch_bounds__(512, 2) void k1_kernel(const __bf16* __restrict__ x, const __bf16* __restrict__ wqkv,
                                                    const __bf16* __restrict__ w1, const __bf16* __restrict__ w2, char* ws) {
  extern __shared__ __attribute__((aligned(16))) char smem[];
  const int bid = blockIdx.x, xcd = bid & 7, idx = bid >> 3;
  const int tid = threadIdx.x, lane = tid & 63, w = __builtin_amdgcn_readfirstlane(tid >> 6);
  STAMP(0, 0);
  const int rb = (xcd >> 2) * 8 + (idx & 7), cgi = idx >> 3;
  const int ci = (xcd & 3) * 2 + (cgi >> 1);
  const bool kv = !(cgi & 1);
  unsigned pf0;
  {
    const char* pc;
    asm volatile("s_getpc_b64 %0" : "=s"(pc));
    asm volatile("global_load_dword %0, %1, off" : "=&v"(pf0) : "v"(pc + (tid < CODE_LINES ? tid : 0) * 128) : "memory");
  }
  const int g = lane >> 4, l15 = lane & 15, q4 = l15 >> 2, p4 = lane & 3;
  const rsrc_t rs = mkrsrc(ws, WS_END);
  const f4 zero4 = {0.f, 0.f, 0.f, 0.f};
  const unsigned tag = *(const __attribute__((address_space(4))) unsigned*)(ws + WS_EP) + 1u;
  const int wks = bid / 5, wcb = bid - wks * 5;
  const int jj = bid >> 3, hf = (bid >> 2) & 1, qd = bid & 3, sel = jj >> 1, srow = hf * 1024 + qd * 256 + jj * 8;
  const bool cjob = jj < 27;
  const int cq = jj < 24 ? 2 * hf + (jj & 1) : (qd & 1) * 2 + hf;
  const int cl = jj >= 24 ? 10 * (jj - 24) + 5 * (qd >> 1) + 2
                          : (sel < 6 ? 10 * (sel >> 1) + 5 * (qd >> 1) + 3 * (qd & 1) + (sel & 1) : (sel < 10 ? 24 + 4 * qd + sel : 36 + 2 * qd + sel));
  if (__builtin_expect(w >= 4, 1)) {
    const int lw = w - 4;
    __builtin_amdgcn_s_setprio(3);
    const rsrc_t rx = mkrsrc(x, S_ * D_ * 2), rq = mkrsrc(wqkv, D_ * N3 * 2), r1 = mkrsrc(w1, D_ * DFF * 2);
    int vo[11];
#pragma unroll
    for (int n = 0; n < 4; ++n) {
      const int row = (n * 4 + lw) * 8 + (lane >> 3);
      vo[n] = (rb * 128 + row) * (D_ * 2) + (((lane & 7) ^ ((row >> 1) & 7)) << 4);
      if (n == 0) { for (int m = 1; m < 4; ++m) vo[m] = vo[0] + m * 32 * D_ * 2; break; }
    }
#define ROT1 3
    const int rot = ROT1 == 1 ? ((jj & 7) + 3 * (jj >> 3)) % 10 : (ROT1 == 2 ? ((jj & 1) * 5 + (jj >> 4) * 2) : (ROT1 == 3 ? ((jj & 3) * 5 / 2 + (jj >> 4)) : 0));
    auto ldA = [&](int stg, int t) {
      t += rot;
      t = t >= 10 ? t - 10 : t;
#pragma unroll
      for (int n = 0; n < 4; ++n) blds16<0>(rx, smem + stg * A_BYTES + n * 4096 + lw * 1024, vo[n], t * 128);
    };
    ldA(0, 0);
    const int nq = kv ? 20 : 12, sq = kv ? 5 : 3, qb = nq * 1024, hb = 28672 - qb;
    auto offs = [&](auto KVc) {
      constexpr bool KV = decltype(KVc)::value;
      constexpr int NQ = KV ? 20 : 12, NH = KV ? 8 : 16;
#pragma unroll
      for (int n = 0; n < 7; ++n) {
        if (n < NQ / 4) {
          const unsigned c = (n * 4 + lw) * 64 + lane, k = (c * (KV ? 3277u : 5462u)) >> 16, ch = (c - k * NQ) ^ ((k >> 2) & 2);
          vo[4 + n] = (k * N3 + 80 * ci + (KV ? (ch >= 10 ? 1200 : 640) + 8 * ch : 8 * (ch >= 10 ? ch - 2 : ch))) * 2;
        } else if (n == NQ / 4) {
          const unsigned c = lw * 64 + lane, k = c / NH;
          const int f = KV ? (((k >> 1) & 1) | ((k >> 2) & 2)) : ((k & 3) | ((k >> 1) & 4));
          vo[4 + n] = (k * DFF + (KV ? 1024 + 64 * ci : 128 * ci) + 8 * ((c - k * NH) ^ (f << 1))) * 2;
        } else vo[4 + n] = vo[4 + NQ / 4] + (n - NQ / 4) * (256 / NH) * DFF * 2;
      }
    };
    if (kv) offs(std::true_type{}); else offs(std::false_type{});
    const rsrc_t rm = mkrsrc(kv ? wqkv : w1, kv ? D_ * N3 * 2 : D_ * DFF * 2);
    auto ldB = [&](int stg, int t) {
      t += rot;
      t = t >= 10 ? t - 10 : t;
      char* dq = smem + L_Q + stg * qb + lw * 1024;
      char* dh = smem + L_Q + 3 * qb + stg * hb + lw * 1024 - sq * 4096;
      const int tq = t * (64 * N3 * 2), th = t * (64 * DFF * 2);
#pragma unroll
      for (int n = 0; n < 7; ++n) {
        if (n < 3) blds16<0>(rq, dq + n * 4096, vo[4 + n], tq);
        else if (n < 5) blds16<0>(rm, (kv ? dq : dh) + n * 4096, vo[4 + n], kv ? tq : th);
        else blds16<0>(r1, dh + n * 4096, vo[4 + n], th);
      }
    };
    ldB(0, 0);
    ldA(1, 1);
    ldB(1, 1);
    int voG[2];
#pragma unroll
    for (int n = 0; n < 2; ++n) {
      const int c = (n * 4 + lw) * 64 + lane, qq = c / 112, rem = c - qq * 112, tl = rem >> 3, cw = tl >= 7, j = tl - 7 * cw, pr = (rem & 7) * 2;
      int col;
      if (kv) { int cc = j * 16 + pr; col = j < 5 ? 640 + 80 * ci + 40 * cw + (cc >= 40 ? 600 + cc : cc) : N3 + 1024 + 64 * ci + 32 * cw + cc - 80; }
      else col = j < 3 ? 80 * ci + 48 * cw + 16 * (cw && j == 2 ? 1 : j) + pr : N3 + 128 * ci + 64 * cw + 16 * (j - 3) + pr;
      voG[n] = c < 448 ? WS_CG + (qq * 3456 + col) * 8 : WS_SG + rb * 1024 + lane * 16;
    }
    auto side_loads = [&]() {
      const bool q = cl < 30;
      const rsrc_t rc = mkrsrc(q ? wqkv : w1, cjob ? (q ? D_ * N3 * 2 : D_ * DFF * 2) : 0);
      const int st = q ? N3 * 2 : DFF * 2;
      const int vb = (cq * 160 + lw * 8 + (lane >> 3)) * st + (q ? cl : cl - 30) * 128 + (lane & 7) * 16;
#pragma unroll
      for (int n = 0; n < 5; ++n) blds16<0>(rc, smem + L_CB + n * 4096 + lw * 1024, vb, n * 32 * st);
      const rsrc_t r2 = mkrsrc(w2, bid < 240 ? DFF * D_ * 2 : 0);
#pragma unroll
      for (int n = 0; n < 2; ++n) blds16<0>(r2, smem + L_ROW + n * 4096 + lw * 1024, ((wks * 32 + lw * 4 + g) * D_ + wcb * 128 + l15 * 8) * 2, n * 16 * D_ * 2);
    };
    int stg = 2;
#pragma unroll 1
    for (int t = 0; t < 10; ++t) {
      {
        if (t == 1 || t == 2) WAITV(18); else if (t < 9) WAITV(11); else WAITV(0);
        __builtin_amdgcn_s_barrier();
        __builtin_amdgcn_s_setprio(3);
        if (t == 8) {
#pragma unroll
          for (int n = 0; n < 2; ++n) blds16<16>(rs, smem + L_ROW + n * 4096 + lw * 1024, voG[n], 0);
        }
        if (t < 8) { ldA(stg, t + 2); ldB(stg, t + 2); }
        if (t == 0) side_loads();
        stg = stg == 2 ? 0 : stg + 1;
      }
      __builtin_amdgcn_s_setprio(0);
      if (t == 4) {
        const bf8 ones = __builtin_bit_cast(bf8, u4{0x3f803f80u, 0x3f803f80u, 0x3f803f80u, 0x3f803f80u});
        const char* c0 = smem + L_CB + (8 * g + q4) * 128 + lw * 32 + 8 * p4;
        f4 c = zero4;
#pragma unroll
        for (int k = 0; k < 5; ++k) c = mfma(ones, trfrag(c0 + k * 4096, c0 + k * 4096 + 512), c);
        if (g == 0 && cjob) st8(u2{__builtin_bit_cast(unsigned, c[0]), tag}, rs, l15 * 8, WS_CG + (cq * 3456 + cl * 64 + lw * 16) * 8);
      } else if (t == 5) {
#pragma unroll
        for (int n = 0; n < 2; ++n) {
          const char* p0 = smem + L_ROW + (4 * g + q4) * 256 + (lw * 2 + n) * 32 + 8 * p4;
          const bf8 f = trfrag(p0, p0 + 4096);
          const int col = wcb * 128 + (lw * 2 + n) * 16;
          if (bid < 240) pst16(__builtin_bit_cast(u4, f), rs, lane * 16, WS_W2 + (wks * 40 + (col >> 4)) * 1024);
        }
      }
      WAITL0();
    }
    asm volatile("" :: "v"(pf0));
#pragma unroll
    for (int n = 0; n < 2; ++n) {
#pragma unroll 1
      for (unsigned spin = 0; spin < SPIN; ++spin) {
        const u4 v = *(const u4*)(smem + L_ROW + n * 4096 + lw * 1024 + lane * 16);
        if (__all(v[1] == tag && v[3] == tag)) break;
        __builtin_amdgcn_s_sleep(1);
        asm volatile("" : "+v"(voG[n]) :: "memory");
        *(u4*)(smem + L_ROW + n * 4096 + lw * 1024 + lane * 16) = ld16(rs, voG[n], 0);
      }
    }
    WAITL0();
    __builtin_amdgcn_s_barrier();
    if (bid == 0 && w == 7) {
      wait16(rs, lane * 16, WS_FW, tag);
      if (lane == 0) st4(tag, rs, 0, WS_EP);
    }
  } else {
    u4 xa[2], xb[2];
    {
      const char* xr = (const char*)x + (srow + w * 2) * (D_ * 2);
      const char *p0 = xr + lane * 16, *p1 = xr + 1024 + l15 * 16;
      asm volatile("global_load_dwordx4 %0, %4, off\n\tglobal_load_dwordx4 %1, %5, off\n\tglobal_load_dwordx4 %2, %4, off offset:1280\n\t"
                   "global_load_dwordx4 %3, %5, off offset:1280"
                   : "=&v"(xa[0]), "=&v"(xb[0]), "=&v"(xa[1]), "=&v"(xb[1]) : "v"(p0), "v"(p1) : "memory");
      asm volatile("s_waitcnt vmcnt(0)" : "+v"(pf0), "+v"(xa[0]), "+v"(xb[0]), "+v"(xa[1]), "+v"(xb[1]) :: "memory");
        float su[2] = {0.f, 0.f}, sq[2] = {0.f, 0.f}, sh[2];
#pragma unroll
        for (int n = 0; n < 2; ++n) {
          sh[n] = lo16(__builtin_amdgcn_readfirstlane(xa[n][0]));
          float s2 = 0.f, q2 = 0.f;
#pragma unroll
          for (int e = 0; e < 4; ++e) {
            const float a0 = lo16(xa[n][e]) - sh[n], a1 = hi16(xa[n][e]) - sh[n], b0 = lo16(xb[n][e]) - sh[n], b1 = hi16(xb[n][e]) - sh[n];
            su[n] += a0 + a1; sq[n] += a0 * a0; sq[n] += a1 * a1; s2 += b0 + b1; q2 += b0 * b0; q2 += b1 * b1;
          }
          if (lane < 16) { su[n] += s2; sq[n] += q2; }
        }
        const float m0 = wave_sum(su[0]) * (1.f / 640.f), m1 = wave_sum(su[1]) * (1.f / 640.f);
        const float e0 = wave_sum(sq[0]) * (1.f / 640.f), e1 = wave_sum(sq[1]) * (1.f / 640.f);
        const float m = lane ? m1 : m0, var = fmaxf((lane ? e1 : e0) - m * m, 0.f);
        const float sd = __builtin_amdgcn_sqrtf(bfr(bfr(var) + 1e-5f));
        if (lane < 2) st8(u2{pk2(m + (lane ? sh[1] : sh[0]), sd), tag}, rs, lane * 8, WS_SG + (srow + w * 2) * 8);
        if (tid == 0) { st4(tag, rs, 0, WS_FW + bid * 4); __builtin_amdgcn_raw_buffer_store_b32(0u, rs, 0, WS_F2 + bid * 128, 0); }
    }
    if (kv) k1_compute<true>(ws, smem, rb, ci);
    else k1_compute<false>(ws, smem, rb, ci);
  }
  if (kv) {
    __builtin_amdgcn_s_barrier();
    if (tid < 480) {
      const bool isz = tid >= 400;
      int hl = isz ? (tid - 400) / 20 : tid / 100, rem = isz ? (tid - 400 - hl * 20) * 5 : tid - hl * 100, d = rem / 5, grp = rem - d * 5;
      int hk = hl & 1, kc = 20 * hk + d, a = kc >> 4;
      int pos = isz ? 32 : 40 + 20 * hk + 4 * grp, b = pos >> 4;
      int m = a == 0 ? b - 2 : (a == 1 ? b : b + 3);
      const char* p = smem + ((hl >> 1) * 8 + m) * 1024 + (((pos >> 2) & 3) * 16 + (kc & 15)) * 16;
      f4 v = *(const f4*)p + *(const f4*)(p + 16384);
      if (isz) __builtin_amdgcn_raw_buffer_store_b32(__builtin_bit_cast(unsigned, v[0]), rs, (tid - 400) * 4, WS_Z + (rb * 640 + ci * 80) * 4, 0);
      else pst16(__builtin_bit_cast(u4, v), rs, tid * 16, WS_KV + (rb * 32 + ci * 4) * 1600);
    }
  }
  STAMP(0, 7);
}

template <int AUX>
DI bf8 ldfrag(rsrc_t r, int voff, int soff) {
  return __builtin_bit_cast(bf8, __builtin_amdgcn_raw_buffer_load_b128(r, voff, soff, AUX));
}
DI void st8_sc1(char* p, unsigned a, unsigned b) {
  __hip_atomic_store((AS1 unsigned long long*)p, (unsigned long long)a | ((unsigned long long)b << 32), __ATOMIC_RELAXED,
                     __HIP_MEMORY_SCOPE_AGENT);
}
constexpr int L2_ACC = 0;
constexpr int L2_WO = 61440;
constexpr int L2_KV = L2_WO + 5120;
constexpr int L2_Z = L2_KV + 2048;
constexpr int L2_CNT = L2_Z + 2048;
constexpr int K2_LDS = 98304;

#define RING 3
DI void k2_publish(char* ws, char* smem, int flag, int lane) {
  WAITV(0);
  if (lane == 0) {
    unsigned old = __hip_atomic_fetch_add((AS3 unsigned*)(smem + L2_CNT), 1u, __ATOMIC_RELAXED, __HIP_MEMORY_SCOPE_WORKGROUP);
    if (old == 2u) __hip_atomic_store((AS1 unsigned*)(ws + WS_F2 + flag * 128), 1u, __ATOMIC_RELAXED, __HIP_MEMORY_SCOPE_AGENT);
  }
}
__global__ __launch_bounds__(512, 2) void k2_kernel(const __bf16* __restrict__ x, const __bf16* __restrict__ wo,
                                                    char* ws, __bf16* __restrict__ out) {
  extern __shared__ __attribute__((aligned(16))) char smem[];
  const int tid = threadIdx.x, lane = tid & 63, w = __builtin_amdgcn_readfirstlane(tid >> 6);
  const int g = lane >> 4, l15 = lane & 15;
  const int xcd = blockIdx.x & 7, idx = blockIdx.x >> 3;
  const int r = (xcd >> 1) * 8 + (idx & 7), c = (xcd & 1) * 4 + (idx >> 3);
  STAMP(1, 0);
  if (tid == 0) *(unsigned*)(smem + L2_CNT) = 0u;
  WAITL0();
  __builtin_amdgcn_s_barrier();
  const rsrc_t rs = mkrsrc(ws, (unsigned)WS_END);
  const int vo = lane * 16;
  const f4 zero4 = {0.f, 0.f, 0.f, 0.f};
  if (w >= 4) __builtin_amdgcn_s_setprio(3);
  if (w == 4) {
    const f4* p = (const f4*)(ws + WS_KV) + r * 100;
    const int i1 = lane < 36 ? lane + 64 : lane;
    f4 v0[16], v1[16];
#pragma unroll
    for (int i = 0; i < 16; ++i) { v0[i] = p[i * 3200 + lane]; v1[i] = p[i * 3200 + i1]; }
    u4 wv[4];
#pragma unroll
    for (int i = 0; i < 4; ++i) {
      int ch = i * 64 + lane, row = ch / 10;
      wv[i] = u4{0u, 0u, 0u, 0u};
      if (ch < 200) wv[i] = *(const u4*)(wo + (20 * r + row) * D_ + 80 * c + (ch - row * 10) * 8);
    }
#pragma unroll
    for (int st = 1; st < 16; st *= 2)
#pragma unroll
      for (int i = 0; i < 16; i += 2 * st) { v0[i] += v0[i + st]; v1[i] += v1[i + st]; }
    *(f4*)(smem + L2_KV + vo) = v0[0];
    *(f4*)(smem + L2_KV + 1024 + vo) = v1[0];
#pragma unroll
    for (int i = 0; i < 4; ++i) *(u4*)(smem + L2_WO + i * 1024 + vo) = wv[i];
    *(u4*)(smem + L2_WO + 4096 + vo) = u4{0u, 0u, 0u, 0u};
    u4 au[2];
#pragma unroll
    for (int dt = 0; dt < 2; ++dt)
#pragma unroll
      for (int hh = 0; hh < 2; ++hh) {
        int d = dt * 16 + l15, grp = 2 * g + hh;
        bool ok = d < 20 && grp < 5;
        f4 v = *(const f4*)(smem + L2_KV + (ok ? d * 5 + grp : 0) * 16);
        au[dt][2 * hh] = ok ? pk2(v[0], v[1]) : 0u;
        au[dt][2 * hh + 1] = ok ? pk2(v[2], v[3]) : 0u;
      }
    const int k0 = 20 * r + 4 * g, k1 = 20 * r + 16;
    const char* p0 = smem + L2_WO + (8 * g + (l15 >> 2)) * 160 + (lane & 3) * 8;
    char* mf = ws + WS_MF + c * 5120 + l15 * 16;
    const int o0 = (k0 >> 5) * 40960 + ((k0 >> 2) & 3) * 256 + ((k0 >> 4) & 1) * 8;
    const int o1 = (k1 >> 5) * 40960 + ((k1 >> 2) & 3) * 256 + ((k1 >> 4) & 1) * 8;
#pragma unroll
    for (int t = 0; t < 5; ++t) {
      bf8 b = trfrag(p0 + t * 32, p0 + t * 32 + 640);
      f4 m0 = mfma(__builtin_bit_cast(bf8, au[0]), b, zero4), m1 = mfma(__builtin_bit_cast(bf8, au[1]), b, zero4);
      st8_sc1(mf + t * 1024 + o0, pk2(m0[0], m0[1]), pk2(m0[2], m0[3]));
      if (g == 0) st8_sc1(mf + t * 1024 + o1, pk2(m1[0], m1[1]), pk2(m1[2], m1[3]));
    }
    k2_publish(ws, smem, r * 8 + c, lane);
    STAMPW(1, 8);
  } else if (w == 5 || w == 6) {
    const int fo = ((c >> 1) * 5 * 128 + r * 4 + (c & 1) * 2 + w - 5) * 1024;
    const f4* zp = (const f4*)(ws + WS_Z) + (c >> 1) * 40 + (lane < 40 ? lane : 0);
    f4 zv[16];
#pragma unroll
    for (int i = 0; i < 16; ++i) zv[i] = zp[i * 160];
    u4 pq[5];
#pragma unroll
    for (int k = 0; k < 5; ++k) pq[k] = __builtin_bit_cast(u4, ldfrag<0>(rs, vo, (int)WS_PQ + fo + k * 131072));
#pragma unroll
    for (int st = 1; st < 16; st *= 2)
#pragma unroll
      for (int i = 0; i < 16; i += 2 * st) zv[i] += zv[i + st];
    char* zl = smem + L2_Z + (w - 5) * 1024;
    if (lane < 40) *(u2*)(zl + lane * 8) = u2{pk2(zv[0][0], zv[0][1]), pk2(zv[0][2], zv[0][3])};
    f4 den = zero4;
#pragma unroll
    for (int k = 0; k < 5; ++k) {
      u4 zf;
#pragma unroll
      for (int p = 0; p < 2; ++p) {
        int gi = 8 * k + 4 * p + g;
        u2 z2 = *(const u2*)(zl + gi * 8);
        bool mine = gi / 5 == l15;
        zf[2 * p] = mine ? z2[0] : 0u;
        zf[2 * p + 1] = mine ? z2[1] : 0u;
      }
      den = mfma(__builtin_bit_cast(bf8, zf), __builtin_bit_cast(bf8, pq[k]), den);
    }
    f4 rd;
#pragma unroll
    for (int e = 0; e < 4; ++e) rd[e] = __builtin_amdgcn_rcpf(bfr(bfr(den[e]) + 1e-6f));
    if (g < 2) *(f4*)(zl + 512 + l15 * 32 + g * 16) = rd;
#pragma unroll
    for (int k = 0; k < 5; ++k) {
      u4 o;
#pragma unroll
      for (int p = 0; p < 2; ++p) {
        float s = *(const float*)(zl + 512 + l15 * 32 + ((8 * k + 4 * p + g) / 5) * 4);
        o[2 * p] = pk2(lo16(pq[k][2 * p]) * s, hi16(pq[k][2 * p]) * s);
        o[2 * p + 1] = pk2(lo16(pq[k][2 * p + 1]) * s, hi16(pq[k][2 * p + 1]) * s);
      }
      st16(o, rs, vo, WS_AF + fo + k * 131072);
    }
    k2_publish(ws, smem, r * 8 + c, lane);
    STAMPW(1, 9);
  }
  if (w >= 4) {
    __builtin_amdgcn_s_barrier();
    return;
  }
  const int hA = (int)WS_HF + (w * 128 + r * 4) * 1024, hB = (int)WS_W2 + (w * 8 + c) * 5120;
  const int aA = (int)WS_AF + (w * 128 + r * 4) * 1024, aB = (int)WS_MF + (w * 8 + c) * 5120;
#define ROT 0
  const int rot = ROT ? ((idx >> 3) * 3 + (idx & 7) * 5) % 12 : 0, rot2 = ROT ? idx % 5 : 0;
  bf8 fa[RING][4], fb[RING][5];
  auto ld = [&](int i) {
    const int sl = i % RING;
    if (i < 12) {
      int ii = i + rot;
      ii = ii >= 12 ? ii - 12 : ii;
#pragma unroll
      for (int j = 0; j < 5; ++j) fb[sl][j] = ldfrag<LAUX>(rs, vo + j * 1024, hB + ii * 163840);
#pragma unroll
      for (int j = 0; j < 4; ++j) fa[sl][j] = ldfrag<LAUX>(rs, vo + j * 1024, hA + ii * 524288);
    } else {
      int ii = i - 12 + rot2;
      ii = ii >= 5 ? ii - 5 : ii;
#pragma unroll
      for (int j = 0; j < 5; ++j) fb[sl][j] = ldfrag<16>(rs, vo + j * 1024, aB + ii * 163840);
#pragma unroll
      for (int j = 0; j < 4; ++j) fa[sl][j] = ldfrag<16>(rs, vo + j * 1024, aA + ii * 524288);
    }
  };
#pragma unroll
  for (int i = 0; i < RING; ++i) ld(i);
  const int orow = (r * 64 + w * 16 + l15) * D_ + c * 80 + 4 * g;
  u2 xr[5];
#pragma unroll
  for (int j = 0; j < 5; ++j) xr[j] = *(const u2*)(x + orow + 16 * j);
  const AS1 unsigned* fl = (const AS1 unsigned*)(ws + WS_F2 + (lane < 8 ? r * 8 + lane : (lane < 40 ? (lane - 8) * 8 + c : r * 8 + c)) * 128);
  STAMP(1, 1);
  __builtin_amdgcn_sched_barrier(0);
  f4 acc[4][5];
#pragma unroll
  for (int i = 0; i < 4; ++i)
#pragma unroll
    for (int j = 0; j < 5; ++j) acc[i][j] = zero4;
  unsigned flv = 0;
#pragma unroll
  for (int i = 0; i < 17; ++i) {
    const int sl = i % RING;
#pragma unroll
    for (int j = 0; j < 5; ++j)
#pragma unroll
      for (int m = 0; m < 4; ++m) acc[m][j] = mfma(fb[sl][j], fa[sl][m], acc[m][j]);
    if (i + RING == 11) flv = __hip_atomic_load(fl, __ATOMIC_RELAXED, __HIP_MEMORY_SCOPE_AGENT);
    if (i + RING == 12) {
      for (unsigned spin = 0; !__all(flv == 1u) && spin < (1u << 22); ++spin) {
        __builtin_amdgcn_s_sleep(1);
        flv = __hip_atomic_load(fl, __ATOMIC_RELAXED, __HIP_MEMORY_SCOPE_AGENT);
      }
      __builtin_amdgcn_fence(__ATOMIC_ACQUIRE, "wavefront");
    }
    if (i + RING < 17) ld(i + RING);
    __builtin_amdgcn_sched_barrier(0);
  }
  STAMP(1, 6);
#pragma unroll
  for (int m = 0; m < 4; ++m) {
    if (m != w) {
      int slot = m < w ? m : m - 1;
#pragma unroll
      for (int j = 0; j < 5; ++j) *(f4*)(smem + L2_ACC + ((w * 3 + slot) * 5 + j) * 1024 + vo) = acc[m][j];
    }
  }
  WAITL0();
  __builtin_amdgcn_s_barrier();
  f4 own[5];
#pragma unroll
  for (int j = 0; j < 5; ++j) own[j] = w == 0 ? acc[0][j] : (w == 1 ? acc[1][j] : (w == 2 ? acc[2][j] : acc[3][j]));
  f4 tot[5];
#pragma unroll
  for (int j = 0; j < 5; ++j) tot[j] = f4{lo16(xr[j][0]), hi16(xr[j][0]), lo16(xr[j][1]), hi16(xr[j][1])};
#pragma unroll
  for (int s = 0; s < 4; ++s) {
    int slot = w < s ? w : w - 1;
#pragma unroll
    for (int j = 0; j < 5; ++j) {
      f4 v = own[j];
      if (s != w) v = *(const f4*)(smem + L2_ACC + ((s * 3 + slot) * 5 + j) * 1024 + vo);
      tot[j] += v;
    }
  }
#pragma unroll
  for (int j = 0; j < 5; ++j) *(u2*)(out + orow + 16 * j) = u2{pk2(tot[j][0], tot[j][1]), pk2(tot[j][2], tot[j][3])};
  STAMP(1, 7);
}

extern "C" void kernel_launch(void* const* d_in, const int* in_sizes, int n_in, void* d_out, int out_size, void* d_ws,
                              size_t ws_size, hipStream_t stream) {
  const __bf16* x = (const __bf16*)d_in[0];
  const __bf16* wqkv = (const __bf16*)d_in[1];
  const __bf16* wo = (const __bf16*)d_in[2];
  const __bf16* w1 = (const __bf16*)d_in[4];
  const __bf16* w2 = (const __bf16*)d_in[5];
  char* ws = (char*)d_ws;
  (void)hipFuncSetAttribute((const void*)k1_kernel, hipFuncAttributeMaxDynamicSharedMemorySize, K1_LDS);
  (void)hipFuncSetAttribute((const void*)k2_kernel, hipFuncAttributeMaxDynamicSharedMemorySize, K2_LDS);
  k1_kernel<<<256, 512, K1_LDS, stream>>>(x, wqkv, w1, w2, ws);
  k2_kernel<<<256, 512, K2_LDS, stream>>>(x, wo, ws, (__bf16*)d_out);
}
```

```cpp
#include <hip/hip_runtime.h>
#include <hip/hip_bf16.h>
#include <stdint.h>
#include <type_traits>

typedef __attribute__((ext_vector_type(8))) __bf16 bf8;
typedef __attribute__((ext_vector_type(4))) __bf16 bf4;
typedef __attribute__((ext_vector_type(2))) __bf16 bf2;
typedef __attribute__((ext_vector_type(4))) short s4;
typedef __attribute__((ext_vector_type(4))) float f4;
typedef __attribute__((ext_vector_type(2))) float f2;
typedef __attribute__((ext_vector_type(4))) unsigned u4;
typedef __attribute__((ext_vector_type(2))) unsigned u2;
#define AS1 __attribute__((address_space(1)))
#define AS3 __attribute__((address_space(3)))
#define DI __device__ __forceinline__

constexpr int S_ = 2048, D_ = 640, DFF = 1536, N3 = 1920;
constexpr int WS_HF = 0;
constexpr int WS_PQ = WS_HF + 128 * 48 * 1024;
constexpr int WS_AF = WS_PQ + 128 * 20 * 1024;
constexpr int WS_W2 = WS_AF + 128 * 20 * 1024;
constexpr int WS_MF = WS_W2 + 8 * 48 * 5 * 1024;
constexpr int WS_KV = WS_MF + 8 * 20 * 5 * 1024;
constexpr int WS_Z = WS_KV + 16 * 32 * 400 * 4;
constexpr int WS_CG = WS_Z + 16 * 640 * 4;
constexpr int WS_SG = WS_CG + 4 * 3456 * 8;
constexpr int WS_FW = WS_SG + 2048 * 8;
constexpr int WS_F1 = WS_FW + 1024;
constexpr int WS_F2 = WS_F1 + 1024;
constexpr int WS_EP = WS_F2 + 256 * 128;
constexpr int WS_END = WS_EP + 128;
#define STAMPW(k, i) do {} while (0)
#define STAMP(k, i) do {} while (0)

DI float bfr(float v) { return (float)(__bf16)v; }
DI unsigned pk2(float a, float b) { f2 v = {a, b}; return __builtin_bit_cast(unsigned, __builtin_convertvector(v, bf2)); }
DI float lo16(unsigned u) { return __builtin_bit_cast(float, u << 16); }
DI float hi16(unsigned u) { return __builtin_bit_cast(float, u & 0xffff0000u); }
DI f4 mfma(bf8 a, bf8 b, f4 c) { return __builtin_amdgcn_mfma_f32_16x16x32_bf16(a, b, c, 0, 0, 0); }
DI void glds16(const void* g, char* l) {
  __builtin_amdgcn_global_load_lds((const AS1 void*)g, (AS3 void*)l, 16, 0, 0);
}
DI bf8 trfrag(const char* p0, const char* p1) {
  s4 a = __builtin_amdgcn_ds_read_tr16_b64_v4i16((AS3 s4*)p0);
  s4 b = __builtin_amdgcn_ds_read_tr16_b64_v4i16((AS3 s4*)p1);
  return __builtin_bit_cast(bf8, __builtin_shufflevector(a, b, 0, 1, 2, 3, 4, 5, 6, 7));
}
DI bf8 trfrag3(const AS3 char* p0, const AS3 char* p1) {
  s4 a = __builtin_amdgcn_ds_read_tr16_b64_v4i16((AS3 s4*)p0);
  s4 b = __builtin_amdgcn_ds_read_tr16_b64_v4i16((AS3 s4*)p1);
  return __builtin_bit_cast(bf8, __builtin_shufflevector(a, b, 0, 1, 2, 3, 4, 5, 6, 7));
}
DI float phi(float t) {
  return fmaxf(t, 0.f) + 1e-6f + __builtin_amdgcn_exp2f(fminf(t, 0.f) * 1.44269504088896341f);
}
#define MT(i) do {} while (0)
#define WAITV(n) asm volatile("s_waitcnt vmcnt(" #n ")" ::: "memory")
#define WAITL0() asm volatile("s_waitcnt lgkmcnt(0)" ::: "memory")

#define PAUX 16
#define LAUX 0
typedef __amdgpu_buffer_rsrc_t rsrc_t;
DI rsrc_t mkrsrc(const void* p, unsigned bytes) { return __builtin_amdgcn_make_buffer_rsrc((void*)p, 0, bytes, 0x00020000); }
template <int AUX>
DI void blds16(rsrc_t r, char* l, int voff, int soff) {
  __builtin_amdgcn_raw_ptr_buffer_load_lds(r, (AS3 void*)l, 16, voff, soff, 0, AUX);
}
DI u4 ld16(rsrc_t r, int voff, int soff) { return __builtin_amdgcn_raw_buffer_load_b128(r, voff, soff, 16); }
DI void st16(u4 v, rsrc_t r, int voff, int soff) { __builtin_amdgcn_raw_buffer_store_b128(v, r, voff + soff, 0, 16); }
DI void pst16(u4 v, rsrc_t r, int voff, int soff) { __builtin_amdgcn_raw_buffer_store_b128(v, r, voff + soff, 0, PAUX); }
DI void pst8(u2 v, rsrc_t r, int voff, int soff) { __builtin_amdgcn_raw_buffer_store_b64(v, r, voff, soff, PAUX); }
DI void st8(u2 v, rsrc_t r, int voff, int soff) { __builtin_amdgcn_raw_buffer_store_b64(v, r, voff, soff, 16); }
DI void st4(unsigned v, rsrc_t r, int voff, int soff) { __builtin_amdgcn_raw_buffer_store_b32(v, r, voff, soff, 16); }
DI unsigned ld4(rsrc_t r, int voff, int soff) { return __builtin_amdgcn_raw_buffer_load_b32(r, voff, soff, 16); }
DI float wave_sum(float v) {
#define DPP_ADD(c) v += __builtin_bit_cast(float, __builtin_amdgcn_update_dpp(0, __builtin_bit_cast(int, v), c, 0xf, 0xf, true))
  DPP_ADD(0x111); DPP_ADD(0x112); DPP_ADD(0x114); DPP_ADD(0x118);
#undef DPP_ADD
  const int u = __builtin_bit_cast(int, v);
  return (__builtin_bit_cast(float, __builtin_amdgcn_readlane(u, 15)) + __builtin_bit_cast(float, __builtin_amdgcn_readlane(u, 31))) +
         (__builtin_bit_cast(float, __builtin_amdgcn_readlane(u, 47)) + __builtin_bit_cast(float, __builtin_amdgcn_readlane(u, 63)));
}
constexpr unsigned SPIN = 1u << 20;
#define PSLEEP 8
DI void wait16(rsrc_t rs, int voff, int soff, unsigned tag) {
#pragma unroll 1
  for (unsigned spin = 0; spin < SPIN; ++spin) {
    asm volatile("" : "+v"(voff) :: "memory");
    u4 v = ld16(rs, voff, soff);
    if (__all(v[0] == tag && v[1] == tag && v[2] == tag && v[3] == tag)) break;
    __builtin_amdgcn_s_sleep(PSLEEP);
  }
  asm volatile("" ::: "memory");
  __builtin_amdgcn_fence(__ATOMIC_ACQUIRE, "wavefront");
}
DI void wait4(rsrc_t rs, int voff, int soff, unsigned tag) {
#pragma unroll 1
  for (unsigned spin = 0; spin < SPIN; ++spin) {
    asm volatile("" : "+v"(voff) :: "memory");
    if (__all(ld4(rs, voff, soff) == tag)) break;
    __builtin_amdgcn_s_sleep(PSLEEP);
  }
  asm volatile("" ::: "memory");
  __builtin_amdgcn_fence(__ATOMIC_ACQUIRE, "wavefront");
}

DI void wait_lds(char* p, unsigned lvl) {
#pragma unroll 1
  for (unsigned spin = 0; spin < SPIN; ++spin) {
    if (__hip_atomic_load((AS3 unsigned*)p, __ATOMIC_RELAXED, __HIP_MEMORY_SCOPE_WORKGROUP) >= lvl) break;
    __builtin_amdgcn_s_sleep(1);
  }
  asm volatile("" ::: "memory");
  __builtin_amdgcn_fence(__ATOMIC_ACQUIRE, "wavefront");
}
DI void set_lds(char* p, unsigned lvl) { __hip_atomic_store((AS3 unsigned*)p, lvl, __ATOMIC_RELAXED, __HIP_MEMORY_SCOPE_WORKGROUP); }

constexpr int A_BYTES = 128 * 128;
constexpr int L_Q = 3 * A_BYTES;
constexpr int L_CB = L_Q + 3 * 64 * 448;
constexpr int L_ROW = L_CB + 20480;
constexpr int K1_LDS = L_ROW + 8192;

template <bool KV>
DI void k1_compute(char* ws, char* smem, int rb, int ci) {
  constexpr int NQ = KV ? 20 : 12, NH = KV ? 8 : 16;
  constexpr int QB = 64 * NQ * 16, HB = 64 * NH * 16;
  constexpr int L_H = L_Q + 3 * QB;
  constexpr int JQ = KV ? 5 : 3;
  const int lane = threadIdx.x & 63;
  const int w = __builtin_amdgcn_readfirstlane(threadIdx.x >> 6), wm = (w >> 1) & 1, wn = w & 1;
  const int g = lane >> 4, l15 = lane & 15, q4 = l15 >> 2, p4 = lane & 3;
  const rsrc_t rs = mkrsrc(ws, WS_END);
  const f4 zero4 = {0.f, 0.f, 0.f, 0.f};
  f4 acc[4][7];
    unsigned ra[2], rb_[7];
    {
      const unsigned lb = (unsigned)(size_t)(AS3 char*)smem;
      const int f = KV ? (((q4 >> 1) & 1) | ((g & 1) << 1)) : (q4 | ((g & 1) << 2));
#pragma unroll
      for (int s = 0; s < 2; ++s) ra[s] = lb + (wm * 64 + l15) * 128 + (((s * 4 + g) ^ (l15 >> 1)) << 4);
#pragma unroll
      for (int j = 0; j < 7; ++j) {
        const int cc = 2 * j + (p4 >> 1);
        const int pos = KV ? wn * 5 + cc + (cc >= 5 ? 5 : 0) : wn * 6 + cc;
        rb_[j] = lb + (j < JQ ? L_Q + (8 * g + q4) * (NQ * 16) + ((pos ^ ((g & 1) << 1)) << 4) + 8 * (p4 & 1)
                              : L_H + (8 * g + q4) * (NH * 16) + (((wn * (7 - JQ) + j - JQ) ^ f) << 5) + 8 * p4);
      }
    }
#pragma unroll
    for (int i = 0; i < 4; ++i)
#pragma unroll
      for (int j = 0; j < 7; ++j) acc[i][j] = zero4;
    bf8 fa[2][4], fb[2][7];
    s4 hb[2][7][2];
    auto rd1 = [&](int s, int stg, int n) {
      if (n < 4) fa[s][n] = *(const AS3 bf8*)(size_t)(ra[s] + stg * A_BYTES + n * 2048);
      else {
        const int j = (n - 4) >> 1, h = (n - 4) & 1;
        const AS3 char* p = (const AS3 char*)(size_t)rb_[j] + (j < JQ ? stg * QB + (s * 32 + 4 * h) * NQ * 16 : stg * HB + (s * 32 + 4 * h) * NH * 16);
        hb[s][j][h] = __builtin_amdgcn_ds_read_tr16_b64_v4i16((AS3 s4*)p);
        if (h) fb[s][j] = __builtin_bit_cast(bf8, __builtin_shufflevector(hb[s][j][0], hb[s][j][1], 0, 1, 2, 3, 4, 5, 6, 7));
      }
    };
    auto mm1 = [&](int s, int n) {
      const int j = n >> 2, i = n & 3;
      if (KV && j < 5) asm volatile("v_mfma_f32_16x16x32_bf16 %0, %1, %2, %0" : "+v"(acc[i][j]) : "v"(fa[s][i]), "v"(fb[s][j]));
      else asm volatile("v_mfma_f32_16x16x32_bf16 %0, %1, %2, %0" : "+v"(acc[i][j]) : "v"(fb[s][j]), "v"(fa[s][i]));
    };
    auto phase = [&](int sm, int sr, int stg, bool domm, bool dord) {
#pragma unroll
      for (int n = 0; n < 28; ++n) {
        if (domm) mm1(sm, n);
        if (dord && n < 18) rd1(sr, stg, n);
        __builtin_amdgcn_sched_barrier(0);
      }
      if (domm)
        asm volatile("" :: "v"(fa[sm][0]), "v"(fa[sm][1]), "v"(fa[sm][2]), "v"(fa[sm][3]), "v"(fb[sm][0]), "v"(fb[sm][1]), "v"(fb[sm][2]),
                     "v"(fb[sm][3]), "v"(fb[sm][4]), "v"(fb[sm][5]), "v"(fb[sm][6]));
    };
    auto body = [&](int stg, bool first) {
      __builtin_amdgcn_s_barrier();
      __builtin_amdgcn_sched_barrier(0);
      phase(1, 0, stg, !first, true);
      phase(0, 1, stg, true, true);
      WAITL0();
    };
#pragma unroll
    for (int j = 0; j < 7; ++j) asm volatile("" : "+v"(acc[0][j]), "+v"(acc[1][j]), "+v"(acc[2][j]), "+v"(acc[3][j]));
    STAMP(0, 1);
    body(0, true);
    STAMP(0, 2);
#pragma unroll 1
    for (int t = 0; t < 3; ++t) {
      body(1, false);
      body(2, false);
      body(0, false);
    }
    phase(1, 0, 0, true, false);
    asm volatile("s_nop 15" ::: "memory");
    __builtin_amdgcn_sched_barrier(0);
    STAMP(0, 4);
    __builtin_amdgcn_s_barrier();
    const char* sg = smem + L_ROW + 7168 + wm * 512;
    unsigned ms[4];
    bf8 am[4];
#pragma unroll
    for (int i = 0; i < 4; ++i) {
      ms[i] = *(const unsigned*)(sg + (i * 16 + l15) * 8);
      const unsigned nm = (ms[i] & 0xffffu) ^ 0x8000u;
      am[i] = __builtin_bit_cast(bf8, u4{nm | (nm << 16), nm, 0u, 0u});
    }
#pragma unroll
    for (int j = 0; j < 7; ++j) {
      const float c = *(const float*)(smem + L_ROW + (g * 224 + (wn * 7 + j) * 16 + l15) * 8);
      const float r1 = c - bfr(c), r2 = r1 - bfr(r1);
      const bf8 bm = __builtin_bit_cast(bf8, u4{pk2(c, r1), pk2(r2, 0.f), 0u, 0u});
#pragma unroll
      for (int i = 0; i < 4; ++i) acc[i][j] = (KV && j < 5) ? mfma(am[i], bm, acc[i][j]) : mfma(bm, am[i], acc[i][j]);
    }
    STAMP(0, 5);
    const int rt0 = rb * 8 + wm * 4;
    typedef __attribute__((ext_vector_type(2))) short s2_t;
    auto relu2 = [&](float a, float b) {
      s2_t v = __builtin_bit_cast(s2_t, pk2(a, b));
      return __builtin_bit_cast(unsigned, __builtin_elementwise_max(v, s2_t{0, 0}));
    };
#pragma unroll
    for (int i = 0; i < 4; ++i) {
      const float rstd = __builtin_amdgcn_rcpf(hi16(ms[i]));
#pragma unroll
      for (int j = JQ; j < 7; j += 2) {
        const int ks = (KV ? 1024 + 64 * ci + 32 * wn : 128 * ci + 64 * wn + 16 * (j - JQ)) >> 5;
        const f4 y0 = acc[i][j] * rstd, y1 = acc[i][j + 1] * rstd;
        pst16(u4{relu2(y0[0], y0[1]), relu2(y0[2], y0[3]), relu2(y1[0], y1[1]), relu2(y1[2], y1[3])}, rs, lane * 16,
              WS_HF + (ks * 128 + rt0 + i) * 1024);
      }
      if (!KV) {
#pragma unroll
        for (int j = 0; j < 3; ++j) {
          if (j == 2 && wn) break;
          const int col = 80 * ci + 48 * wn + 16 * j;
          const f4 y = acc[i][j] * rstd;
          pst8(u2{pk2(phi(y[0]), phi(y[1])), pk2(phi(y[2]), phi(y[3]))}, rs, lane * 16,
               WS_PQ + ((col >> 5) * 128 + rt0 + i) * 1024 + ((col >> 4) & 1) * 8);
        }
      }
    }
    STAMP(0, 6);
    if (KV) {
      f4 kv[8];
#pragma unroll
      for (int hp = 0; hp < 2; ++hp) {
        u4 fr[6];
#pragma unroll
        for (int ii = 0; ii < 2; ++ii) {
          const int i = hp * 2 + ii;
          const u4 s0 = *(const u4*)(sg + (i * 16 + 4 * g) * 8), s1 = *(const u4*)(sg + (i * 16 + 4 * g) * 8 + 16);
          const f4 rr = {__builtin_amdgcn_rcpf(hi16(s0[0])), __builtin_amdgcn_rcpf(hi16(s0[2])), __builtin_amdgcn_rcpf(hi16(s1[0])),
                         __builtin_amdgcn_rcpf(hi16(s1[2]))};
#pragma unroll
          for (int j = 0; j < 5; ++j) {
            f4 y = acc[i][j] * rr;
            if (j < 3) {
#pragma unroll
              for (int e = 0; e < 4; ++e) y[e] = (j < 2 || l15 < 8) ? phi(y[e]) : y[e];
            }
            fr[j][2 * ii] = pk2(y[0], y[1]);
            fr[j][2 * ii + 1] = pk2(y[2], y[3]);
          }
        }
#pragma unroll
        for (int e = 0; e < 4; ++e) fr[5][e] = l15 < 8 ? 0x3f803f80u : fr[2][e];
        const int ta[8] = {0, 0, 1, 1, 1, 2, 2, 2}, tb[8] = {5, 3, 5, 3, 4, 5, 3, 4};
#pragma unroll
        for (int m = 0; m < 8; ++m) kv[m] = mfma(__builtin_bit_cast(bf8, fr[tb[m]]), __builtin_bit_cast(bf8, fr[ta[m]]), hp ? kv[m] : zero4);
      }
#pragma unroll
      for (int m = 0; m < 8; ++m) *(f4*)(smem + (w * 8 + m) * 1024 + lane * 16) = kv[m];
      WAITL0();
    }
}

constexpr int CODE_LINES = 300;
__global__ __launch_bounds__(512, 2) void k1_kernel(const __bf16* __restrict__ x, const __bf16* __restrict__ wqkv,
                                                    const __bf16* __restrict__ w1, const __bf16* __restrict__ w2, char* ws) {
  extern __shared__ __attribute__((aligned(16))) char smem[];
  const int bid = blockIdx.x, xcd = bid & 7, idx = bid >> 3;
  const int tid = threadIdx.x, lane = tid & 63, w = __builtin_amdgcn_readfirstlane(tid >> 6);
  STAMP(0, 0);
  const int rb = (xcd >> 2) * 8 + (idx & 7), cgi = idx >> 3;
  const int ci = (xcd & 3) * 2 + (cgi >> 1);
  const bool kv = !(cgi & 1);
  unsigned pf0;
  {
    const char* pc;
    asm volatile("s_getpc_b64 %0" : "=s"(pc));
    asm volatile("global_load_dword %0, %1, off" : "=&v"(pf0) : "v"(pc + (tid < CODE_LINES ? tid : 0) * 128) : "memory");
  }
  const int g = lane >> 4, l15 = lane & 15, q4 = l15 >> 2, p4 = lane & 3;
  const rsrc_t rs = mkrsrc(ws, WS_END);
  const f4 zero4 = {0.f, 0.f, 0.f, 0.f};
  const unsigned tag = *(const __attribute__((address_space(4))) unsigned*)(ws + WS_EP) + 1u;
  const int jj = bid >> 3;
  if (__builtin_expect(w >= 4, 1)) {
    const int lw = w - 4;
    __builtin_amdgcn_s_setprio(3);
    const rsrc_t rx = mkrsrc(x, S_ * D_ * 2), rq = mkrsrc(wqkv, D_ * N3 * 2), r1 = mkrsrc(w1, D_ * DFF * 2);
    int vo[11];
#pragma unroll
    for (int n = 0; n < 4; ++n) {
      const int row = (n * 4 + lw) * 8 + (lane >> 3);
      vo[n] = (rb * 128 + row) * (D_ * 2) + (((lane & 7) ^ ((row >> 1) & 7)) << 4);
      if (n == 0) { for (int m = 1; m < 4; ++m) vo[m] = vo[0] + m * 32 * D_ * 2; break; }
    }
#define ROT1 3
    const int rot = ROT1 == 1 ? ((jj & 7) + 3 * (jj >> 3)) % 10 : (ROT1 == 2 ? ((jj & 1) * 5 + (jj >> 4) * 2) : (ROT1 == 3 ? ((jj & 3) * 5 / 2 + (jj >> 4)) : 0));
    auto ldA = [&](int stg, int t) {
      t += rot;
      t = t >= 10 ? t - 10 : t;
#pragma unroll
      for (int n = 0; n < 4; ++n) blds16<0>(rx, smem + stg * A_BYTES + n * 4096 + lw * 1024, vo[n], t * 128);
    };
    ldA(0, 0);
    const int nq = kv ? 20 : 12, sq = kv ? 5 : 3, qb = nq * 1024, hb = 28672 - qb;
    auto offs = [&](auto KVc) {
      constexpr bool KV = decltype(KVc)::value;
      constexpr int NQ = KV ? 20 : 12, NH = KV ? 8 : 16;
#pragma unroll
      for (int n = 0; n < 7; ++n) {
        if (n < NQ / 4) {
          const unsigned c = (n * 4 + lw) * 64 + lane, k = (c * (KV ? 3277u : 5462u)) >> 16, ch = (c - k * NQ) ^ ((k >> 2) & 2);
          vo[4 + n] = (k * N3 + 80 * ci + (KV ? (ch >= 10 ? 1200 : 640) + 8 * ch : 8 * (ch >= 10 ? ch - 2 : ch))) * 2;
        } else if (n == NQ / 4) {
          const unsigned c = lw * 64 + lane, k = c / NH;
          const int f = KV ? (((k >> 1) & 1) | ((k >> 2) & 2)) : ((k & 3) | ((k >> 1) & 4));
          vo[4 + n] = (k * DFF + (KV ? 1024 + 64 * ci : 128 * ci) + 8 * ((c - k * NH) ^ (f << 1))) * 2;
        } else vo[4 + n] = vo[4 + NQ / 4] + (n - NQ / 4) * (256 / NH) * DFF * 2;
      }
    };
    if (kv) offs(std::true_type{}); else offs(std::false_type{});
    const rsrc_t rm = mkrsrc(kv ? wqkv : w1, kv ? D_ * N3 * 2 : D_ * DFF * 2);
    auto ldB = [&](int stg, int t) {
      t += rot;
      t = t >= 10 ? t - 10 : t;
      char* dq = smem + L_Q + stg * qb + lw * 1024;
      char* dh = smem + L_Q + 3 * qb + stg * hb + lw * 1024 - sq * 4096;
      const int tq = t * (64 * N3 * 2), th = t * (64 * DFF * 2);
#pragma unroll
      for (int n = 0; n < 7; ++n) {
        if (n < 3) blds16<0>(rq, dq + n * 4096, vo[4 + n], tq);
        else if (n < 5) blds16<0>(rm, (kv ? dq : dh) + n * 4096, vo[4 + n], kv ? tq : th);
        else blds16<0>(r1, dh + n * 4096, vo[4 + n], th);
      }
    };
    ldB(0, 0);
    ldA(1, 1);
    ldB(1, 1);
    int bid2 = bid;
    asm volatile("" : "+s"(bid2));
  const int wks = bid2 / 5, wcb = bid2 - wks * 5;
  const int hf = (bid2 >> 2) & 1, qd = bid2 & 3, sel = (bid2 >> 3) >> 1;
  const bool cjob = (bid2 >> 3) < 27;
  const int cq = (bid2 >> 3) < 24 ? 2 * hf + ((bid2 >> 3) & 1) : (qd & 1) * 2 + hf;
  const int cl = (bid2 >> 3) >= 24 ? 10 * ((bid2 >> 3) - 24) + 5 * (qd >> 1) + 2
                          : (sel < 6 ? 10 * (sel >> 1) + 5 * (qd >> 1) + 3 * (qd & 1) + (sel & 1) : (sel < 10 ? 24 + 4 * qd + sel : 36 + 2 * qd + sel));
    int voG[2];
#pragma unroll
    for (int n = 0; n < 2; ++n) {
      const int c = (n * 4 + lw) * 64 + lane, qq = c / 112, rem = c - qq * 112, tl = rem >> 3, cw = tl >= 7, j = tl - 7 * cw, pr = (rem & 7) * 2;
      int col;
      if (kv) { int cc = j * 16 + pr; col = j < 5 ? 640 + 80 * ci + 40 * cw + (cc >= 40 ? 600 + cc : cc) : N3 + 1024 + 64 * ci + 32 * cw + cc - 80; }
      else col = j < 3 ? 80 * ci + 48 * cw + 16 * (cw && j == 2 ? 1 : j) + pr : N3 + 128 * ci + 64 * cw + 16 * (j - 3) + pr;
      voG[n] = c < 448 ? WS_CG + (qq * 3456 + col) * 8 : WS_SG + rb * 1024 + lane * 16;
    }
    auto side_loads = [&]() {
      const bool q = cl < 30;
      const rsrc_t rc = mkrsrc(q ? wqkv : w1, cjob ? (q ? D_ * N3 * 2 : D_ * DFF * 2) : 0);
      const int st = q ? N3 * 2 : DFF * 2;
      const int vb = (cq * 160 + lw * 8 + (lane >> 3)) * st + (q ? cl : cl - 30) * 128 + (lane & 7) * 16;
#pragma unroll
      for (int n = 0; n < 5; ++n) blds16<0>(rc, smem + L_CB + n * 4096 + lw * 1024, vb, n * 32 * st);
      const rsrc_t r2 = mkrsrc(w2, bid < 240 ? DFF * D_ * 2 : 0);
#pragma unroll
      for (int n = 0; n < 2; ++n) blds16<0>(r2, smem + L_ROW + n * 4096 + lw * 1024, ((wks * 32 + lw * 4 + g) * D_ + wcb * 128 + l15 * 8) * 2, n * 16 * D_ * 2);
    };
    int stg = 2;
#pragma unroll 1
    for (int t = 0; t < 10; ++t) {
      {
        if (t == 1 || t == 2) WAITV(18); else if (t < 9) WAITV(11); else WAITV(0);
        __builtin_amdgcn_s_barrier();
        __builtin_amdgcn_s_setprio(3);
        if (t == 8) {
#pragma unroll
          for (int n = 0; n < 2; ++n) blds16<16>(rs, smem + L_ROW + n * 4096 + lw * 1024, voG[n], 0);
        }
        if (t < 8) { ldA(stg, t + 2); ldB(stg, t + 2); }
        if (t == 0) side_loads();
        stg = stg == 2 ? 0 : stg + 1;
      }
      __builtin_amdgcn_s_setprio(0);
      if (t == 4) {
        const bf8 ones = __builtin_bit_cast(bf8, u4{0x3f803f80u, 0x3f803f80u, 0x3f803f80u, 0x3f803f80u});
        const char* c0 = smem + L_CB + (8 * g + q4) * 128 + lw * 32 + 8 * p4;
        f4 c = zero4;
#pragma unroll
        for (int k = 0; k < 5; ++k) c = mfma(ones, trfrag(c0 + k * 4096, c0 + k * 4096 + 512), c);
        if (g == 0 && cjob) st8(u2{__builtin_bit_cast(unsigned, c[0]), tag}, rs, l15 * 8, WS_CG + (cq * 3456 + cl * 64 + lw * 16) * 8);
      } else if (t == 5) {
#pragma unroll
        for (int n = 0; n < 2; ++n) {
          const char* p0 = smem + L_ROW + (4 * g + q4) * 256 + (lw * 2 + n) * 32 + 8 * p4;
          const bf8 f = trfrag(p0, p0 + 4096);
          const int col = wcb * 128 + (lw * 2 + n) * 16;
          if (bid < 240) pst16(__builtin_bit_cast(u4, f), rs, lane * 16, WS_W2 + (wks * 40 + (col >> 4)) * 1024);
        }
      }
      WAITL0();
    }
    asm volatile("" :: "v"(pf0));
#pragma unroll
    for (int n = 0; n < 2; ++n) {
#pragma unroll 1
      for (unsigned spin = 0; spin < SPIN; ++spin) {
        const u4 v = *(const u4*)(smem + L_ROW + n * 4096 + lw * 1024 + lane * 16);
        if (__all(v[1] == tag && v[3] == tag)) break;
        __builtin_amdgcn_s_sleep(1);
        asm volatile("" : "+v"(voG[n]) :: "memory");
        *(u4*)(smem + L_ROW + n * 4096 + lw * 1024 + lane * 16) = ld16(rs, voG[n], 0);
      }
    }
    WAITL0();
    __builtin_amdgcn_s_barrier();
    if (bid == 0 && w == 7) {
      wait16(rs, lane * 16, WS_FW, tag);
      if (lane == 0) st4(tag, rs, 0, WS_EP);
    }
  } else {
    u4 xa[2], xb[2];
    {
      const int srow = ((bid >> 2) & 1) * 1024 + (bid & 3) * 256 + jj * 8;
      const char* xr = (const char*)x + (srow + w * 2) * (D_ * 2);
      const char *p0 = xr + lane * 16, *p1 = xr + 1024 + l15 * 16;
      asm volatile("global_load_dwordx4 %0, %4, off\n\tglobal_load_dwordx4 %1, %5, off\n\tglobal_load_dwordx4 %2, %4, off offset:1280\n\t"
                   "global_load_dwordx4 %3, %5, off offset:1280"
                   : "=&v"(xa[0]), "=&v"(xb[0]), "=&v"(xa[1]), "=&v"(xb[1]) : "v"(p0), "v"(p1) : "memory");
      asm volatile("s_waitcnt vmcnt(0)" : "+v"(pf0), "+v"(xa[0]), "+v"(xb[0]), "+v"(xa[1]), "+v"(xb[1]) :: "memory");
        float su[2] = {0.f, 0.f}, sq[2] = {0.f, 0.f}, sh[2];
#pragma unroll
        for (int n = 0; n < 2; ++n) {
          sh[n] = lo16(__builtin_amdgcn_readfirstlane(xa[n][0]));
          float s2 = 0.f, q2 = 0.f;
#pragma unroll
          for (int e = 0; e < 4; ++e) {
            const float a0 = lo16(xa[n][e]) - sh[n], a1 = hi16(xa[n][e]) - sh[n], b0 = lo16(xb[n][e]) - sh[n], b1 = hi16(xb[n][e]) - sh[n];
            su[n] += a0 + a1; sq[n] += a0 * a0; sq[n] += a1 * a1; s2 += b0 + b1; q2 += b0 * b0; q2 += b1 * b1;
          }
          if (lane < 16) { su[n] += s2; sq[n] += q2; }
        }
        const float m0 = wave_sum(su[0]) * (1.f / 640.f), m1 = wave_sum(su[1]) * (1.f / 640.f);
        const float e0 = wave_sum(sq[0]) * (1.f / 640.f), e1 = wave_sum(sq[1]) * (1.f / 640.f);
        const float m = lane ? m1 : m0, var = fmaxf((lane ? e1 : e0) - m * m, 0.f);
        const float sd = __builtin_amdgcn_sqrtf(bfr(bfr(var) + 1e-5f));
        if (lane < 2) st8(u2{pk2(m + (lane ? sh[1] : sh[0]), sd), tag}, rs, lane * 8, WS_SG + (srow + w * 2) * 8);
        if (tid == 0) { st4(tag, rs, 0, WS_FW + bid * 4); pst16(u4{0u, 0u, 0u, 0u}, rs, 0, WS_F2 + bid * 128); }
    }
    if (kv) k1_compute<true>(ws, smem, rb, ci);
    else k1_compute<false>(ws, smem, rb, ci);
  }
  if (kv) {
    __builtin_amdgcn_s_barrier();
    if (tid < 480) {
      const bool isz = tid >= 400;
      int hl = isz ? (tid - 400) / 20 : tid / 100, rem = isz ? (tid - 400 - hl * 20) * 5 : tid - hl * 100, d = rem / 5, grp = rem - d * 5;
      int hk = hl & 1, kc = 20 * hk + d, a = kc >> 4;
      int pos = isz ? 32 : 40 + 20 * hk + 4 * grp, b = pos >> 4;
      int m = a == 0 ? b - 2 : (a == 1 ? b : b + 3);
      const char* p = smem + ((hl >> 1) * 8 + m) * 1024 + (((pos >> 2) & 3) * 16 + (kc & 15)) * 16;
      f4 v = *(const f4*)p + *(const f4*)(p + 16384);
      if (isz) __builtin_amdgcn_raw_buffer_store_b32(__builtin_bit_cast(unsigned, v[0]), rs, (tid - 400) * 4, WS_Z + (rb * 640 + ci * 80) * 4, 0);
      else pst16(__builtin_bit_cast(u4, v), rs, tid * 16, WS_KV + (rb * 32 + ci * 4) * 1600);
    }
  }
  STAMP(0, 7);
}

template <int AUX>
DI bf8 ldfrag(rsrc_t r, int voff, int soff) {
  return __builtin_bit_cast(bf8, __builtin_amdgcn_raw_buffer_load_b128(r, voff, soff, AUX));
}
DI void st8_sc1(char* p, unsigned a, unsigned b) {
  __hip_atomic_store((AS1 unsigned long long*)p, (unsigned long long)a | ((unsigned long long)b << 32), __ATOMIC_RELAXED,
                     __HIP_MEMORY_SCOPE_AGENT);
}
constexpr int L2_ACC = 0;
constexpr int L2_WO = 61440;
constexpr int L2_KV = L2_WO + 5120;
constexpr int L2_Z = L2_KV + 2048;
constexpr int L2_CNT = L2_Z + 2048;
constexpr int K2_LDS = 98304;

#define RING 3
DI void k2_publish(char* ws, int flag, int lane) {
  WAITV(0);
  if (lane == 0) __hip_atomic_store((AS1 unsigned*)(ws + WS_F2 + flag), 1u, __ATOMIC_RELAXED, __HIP_MEMORY_SCOPE_AGENT);
}
__global__ __launch_bounds__(512, 2) void k2_kernel(const __bf16* __restrict__ x, const __bf16* __restrict__ wo,
                                                    char* ws, __bf16* __restrict__ out) {
  extern __shared__ __attribute__((aligned(16))) char smem[];
  const int tid = threadIdx.x, lane = tid & 63, w = __builtin_amdgcn_readfirstlane(tid >> 6);
  const int g = lane >> 4, l15 = lane & 15;
  const int xcd = blockIdx.x & 7, idx = blockIdx.x >> 3;
  const int r = (xcd >> 1) * 8 + (idx & 7), c = (xcd & 1) * 4 + (idx >> 3);
  STAMP(1, 0);
  unsigned pf0 = 0;
  if (w == 7 || w == 6) {
    const char* pc;
    asm volatile("s_getpc_b64 %0" : "=s"(pc));
    asm volatile("global_load_dword %0, %1, off" : "=&v"(pf0) : "v"(pc + (tid - 384 < 96 ? tid - 384 : 0) * 128) : "memory");
  }
  const rsrc_t rs = mkrsrc(ws, (unsigned)WS_END);
  const int vo = lane * 16;
  const f4 zero4 = {0.f, 0.f, 0.f, 0.f};
  if (__builtin_expect(w >= 4, 0)) {
  __builtin_amdgcn_s_setprio(3);
  if (w == 4) {
    const f4* p = (const f4*)(ws + WS_KV) + r * 100;
    const int i1 = lane < 36 ? lane + 64 : lane;
    f4 v0[16], v1[16];
#pragma unroll
    for (int i = 0; i < 16; ++i) { v0[i] = p[i * 3200 + lane]; v1[i] = p[i * 3200 + i1]; }
    u4 wv[4];
#pragma unroll
    for (int i = 0; i < 4; ++i) {
      int ch = i * 64 + lane, row = ch / 10;
      wv[i] = u4{0u, 0u, 0u, 0u};
      if (ch < 200) wv[i] = *(const u4*)(wo + (20 * r + row) * D_ + 80 * c + (ch - row * 10) * 8);
    }
#pragma unroll
    for (int st = 1; st < 16; st *= 2)
#pragma unroll
      for (int i = 0; i < 16; i += 2 * st) { v0[i] += v0[i + st]; v1[i] += v1[i + st]; }
    *(f4*)(smem + L2_KV + vo) = v0[0];
    *(f4*)(smem + L2_KV + 1024 + vo) = v1[0];
#pragma unroll
    for (int i = 0; i < 4; ++i) *(u4*)(smem + L2_WO + i * 1024 + vo) = wv[i];
    *(u4*)(smem + L2_WO + 4096 + vo) = u4{0u, 0u, 0u, 0u};
    u4 au[2];
#pragma unroll
    for (int dt = 0; dt < 2; ++dt)
#pragma unroll
      for (int hh = 0; hh < 2; ++hh) {
        int d = dt * 16 + l15, grp = 2 * g + hh;
        bool ok = d < 20 && grp < 5;
        f4 v = *(const f4*)(smem + L2_KV + (ok ? d * 5 + grp : 0) * 16);
        au[dt][2 * hh] = ok ? pk2(v[0], v[1]) : 0u;
        au[dt][2 * hh + 1] = ok ? pk2(v[2], v[3]) : 0u;
      }
    const int k0 = 20 * r + 4 * g, k1 = 20 * r + 16;
    const char* p0 = smem + L2_WO + (8 * g + (l15 >> 2)) * 160 + (lane & 3) * 8;
    char* mf = ws + WS_MF + c * 5120 + l15 * 16;
    const int o0 = (k0 >> 5) * 40960 + ((k0 >> 2) & 3) * 256 + ((k0 >> 4) & 1) * 8;
    const int o1 = (k1 >> 5) * 40960 + ((k1 >> 2) & 3) * 256 + ((k1 >> 4) & 1) * 8;
#pragma unroll
    for (int t = 0; t < 5; ++t) {
      bf8 b = trfrag(p0 + t * 32, p0 + t * 32 + 640);
      f4 m0 = mfma(__builtin_bit_cast(bf8, au[0]), b, zero4), m1 = mfma(__builtin_bit_cast(bf8, au[1]), b, zero4);
      st8_sc1(mf + t * 1024 + o0, pk2(m0[0], m0[1]), pk2(m0[2], m0[3]));
      if (g == 0) st8_sc1(mf + t * 1024 + o1, pk2(m1[0], m1[1]), pk2(m1[2], m1[3]));
    }
    k2_publish(ws, (r * 8 + c) * 128, lane);
    STAMPW(1, 8);
  } else if (w == 5 || w == 6) {
    const int fo = ((c >> 1) * 5 * 128 + r * 4 + (c & 1) * 2 + w - 5) * 1024;
    const f4* zp = (const f4*)(ws + WS_Z) + (c >> 1) * 40 + (lane < 40 ? lane : 0);
    f4 zv[16];
#pragma unroll
    for (int i = 0; i < 16; ++i) zv[i] = zp[i * 160];
    u4 pq[5];
#pragma unroll
    for (int k = 0; k < 5; ++k) pq[k] = __builtin_bit_cast(u4, ldfrag<0>(rs, vo, (int)WS_PQ + fo + k * 131072));
#pragma unroll
    for (int st = 1; st < 16; st *= 2)
#pragma unroll
      for (int i = 0; i < 16; i += 2 * st) zv[i] += zv[i + st];
    char* zl = smem + L2_Z + (w - 5) * 1024;
    if (lane < 40) *(u2*)(zl + lane * 8) = u2{pk2(zv[0][0], zv[0][1]), pk2(zv[0][2], zv[0][3])};
    f4 den = zero4;
#pragma unroll
    for (int k = 0; k < 5; ++k) {
      u4 zf;
#pragma unroll
      for (int p = 0; p < 2; ++p) {
        int gi = 8 * k + 4 * p + g;
        u2 z2 = *(const u2*)(zl + gi * 8);
        bool mine = gi / 5 == l15;
        zf[2 * p] = mine ? z2[0] : 0u;
        zf[2 * p + 1] = mine ? z2[1] : 0u;
      }
      den = mfma(__builtin_bit_cast(bf8, zf), __builtin_bit_cast(bf8, pq[k]), den);
    }
    f4 rd;
#pragma unroll
    for (int e = 0; e < 4; ++e) rd[e] = __builtin_amdgcn_rcpf(bfr(bfr(den[e]) + 1e-6f));
    if (g < 2) *(f4*)(zl + 512 + l15 * 32 + g * 16) = rd;
#pragma unroll
    for (int k = 0; k < 5; ++k) {
      u4 o;
#pragma unroll
      for (int p = 0; p < 2; ++p) {
        float s = *(const float*)(zl + 512 + l15 * 32 + ((8 * k + 4 * p + g) / 5) * 4);
        o[2 * p] = pk2(lo16(pq[k][2 * p]) * s, hi16(pq[k][2 * p]) * s);
        o[2 * p + 1] = pk2(lo16(pq[k][2 * p + 1]) * s, hi16(pq[k][2 * p + 1]) * s);
      }
      st16(o, rs, vo, WS_AF + fo + k * 131072);
    }
    k2_publish(ws, (r * 8 + c) * 128 + (w - 4) * 4, lane);
    STAMPW(1, 9);
  }
    asm volatile("s_waitcnt vmcnt(0)" : "+v"(pf0) :: "memory");
    __builtin_amdgcn_s_barrier();
    return;
  }
  const int hA = (int)WS_HF + (w * 128 + r * 4) * 1024, hB = (int)WS_W2 + (w * 8 + c) * 5120;
  const int aA = (int)WS_AF + (w * 128 + r * 4) * 1024, aB = (int)WS_MF + (w * 8 + c) * 5120;
#define ROT 0
  const int rot = ROT ? ((idx >> 3) * 3 + (idx & 7) * 5) % 12 : 0, rot2 = ROT ? idx % 5 : 0;
  bf8 fa[RING][4], fb[RING][5];
  auto ld = [&](int i) {
    const int sl = i % RING;
    if (i < 12) {
      int ii = i + rot;
      ii = ii >= 12 ? ii - 12 : ii;
#pragma unroll
      for (int j = 0; j < 5; ++j) fb[sl][j] = ldfrag<LAUX>(rs, vo + j * 1024, hB + ii * 163840);
#pragma unroll
      for (int j = 0; j < 4; ++j) fa[sl][j] = ldfrag<LAUX>(rs, vo + j * 1024, hA + ii * 524288);
    } else {
      int ii = i - 12 + rot2;
      ii = ii >= 5 ? ii - 5 : ii;
#pragma unroll
      for (int j = 0; j < 5; ++j) fb[sl][j] = ldfrag<16>(rs, vo + j * 1024, aB + ii * 163840);
#pragma unroll
      for (int j = 0; j < 4; ++j) fa[sl][j] = ldfrag<16>(rs, vo + j * 1024, aA + ii * 524288);
    }
  };
#pragma unroll
  for (int i = 0; i < RING; ++i) ld(i);
  const int orow = (r * 64 + w * 16 + l15) * D_ + c * 80 + 4 * g;
  u2 xr[5];
  const AS1 unsigned* fl = (const AS1 unsigned*)(ws + WS_F2 + (lane < 16 ? (r * 8 + (lane >> 1)) * 128 + 4 + (lane & 1) * 4 : (((lane - 16) & 31) * 8 + c) * 128));
  STAMP(1, 1);
  __builtin_amdgcn_sched_barrier(0);
  f4 acc[4][5];
#pragma unroll
  for (int i = 0; i < 4; ++i)
#pragma unroll
    for (int j = 0; j < 5; ++j) acc[i][j] = zero4;
  unsigned flv = 0;
#pragma unroll
  for (int i = 0; i < 17; ++i) {
    const int sl = i % RING;
#pragma unroll
    for (int j = 0; j < 5; ++j)
#pragma unroll
      for (int m = 0; m < 4; ++m) acc[m][j] = mfma(fb[sl][j], fa[sl][m], acc[m][j]);
    if (i + RING == 11) flv = __hip_atomic_load(fl, __ATOMIC_RELAXED, __HIP_MEMORY_SCOPE_AGENT);
    if (i + RING == 12) {
      for (unsigned spin = 0; !__all(flv == 1u) && spin < (1u << 22); ++spin) {
        __builtin_amdgcn_s_sleep(1);
        flv = __hip_atomic_load(fl, __ATOMIC_RELAXED, __HIP_MEMORY_SCOPE_AGENT);
      }
      __builtin_amdgcn_fence(__ATOMIC_ACQUIRE, "wavefront");
    }
    if (i + RING < 17) ld(i + RING);
    if (i + RING == 17) {
#pragma unroll
      for (int j = 0; j < 5; ++j) xr[j] = *(const u2*)(x + orow + 16 * j);
    }
    __builtin_amdgcn_sched_barrier(0);
  }
  STAMP(1, 6);
#pragma unroll
  for (int m = 0; m < 4; ++m) {
    if (m != w) {
      int slot = m < w ? m : m - 1;
#pragma unroll
      for (int j = 0; j < 5; ++j) *(f4*)(smem + L2_ACC + ((w * 3 + slot) * 5 + j) * 1024 + vo) = acc[m][j];
    }
  }
  WAITL0();
  __builtin_amdgcn_s_barrier();
  f4 own[5];
#pragma unroll
  for (int j = 0; j < 5; ++j) own[j] = w == 0 ? acc[0][j] : (w == 1 ? acc[1][j] : (w == 2 ? acc[2][j] : acc[3][j]));
  f4 tot[5];
#pragma unroll
  for (int j = 0; j < 5; ++j) tot[j] = f4{lo16(xr[j][0]), hi16(xr[j][0]), lo16(xr[j][1]), hi16(xr[j][1])};
#pragma unroll
  for (int s = 0; s < 4; ++s) {
    int slot = w < s ? w : w - 1;
#pragma unroll
    for (int j = 0; j < 5; ++j) {
      f4 v = own[j];
      if (s != w) v = *(const f4*)(smem + L2_ACC + ((s * 3 + slot) * 5 + j) * 1024 + vo);
      tot[j] += v;
    }
  }
#pragma unroll
  for (int j = 0; j < 5; ++j) *(u2*)(out + orow + 16 * j) = u2{pk2(tot[j][0], tot[j][1]), pk2(tot[j][2], tot[j][3])};
  STAMP(1, 7);
}

extern "C" void kernel_launch(void* const* d_in, const int* in_sizes, int n_in, void* d_out, int out_size, void* d_ws,
                              size_t ws_size, hipStream_t stream) {
  const __bf16* x = (const __bf16*)d_in[0];
  const __bf16* wqkv = (const __bf16*)d_in[1];
  const __bf16* wo = (const __bf16*)d_in[2];
  const __bf16* w1 = (const __bf16*)d_in[4];
  const __bf16* w2 = (const __bf16*)d_in[5];
  char* ws = (char*)d_ws;
  (void)hipFuncSetAttribute((const void*)k1_kernel, hipFuncAttributeMaxDynamicSharedMemorySize, K1_LDS);
  (void)hipFuncSetAttribute((const void*)k2_kernel, hipFuncAttributeMaxDynamicSharedMemorySize, K2_LDS);
  k1_kernel<<<256, 512, K1_LDS, stream>>>(x, wqkv, w1, w2, ws);
  k2_kernel<<<256, 512, K2_LDS, stream>>>(x, wo, ws, (__bf16*)d_out);
}
```

```cpp
#include <hip/hip_runtime.h>
#include <hip/hip_bf16.h>
#include <stdint.h>
#include <type_traits>

typedef __attribute__((ext_vector_type(8))) __bf16 bf8;
typedef __attribute__((ext_vector_type(4))) __bf16 bf4;
typedef __attribute__((ext_vector_type(2))) __bf16 bf2;
typedef __attribute__((ext_vector_type(4))) short s4;
typedef __attribute__((ext_vector_type(4))) float f4;
typedef __attribute__((ext_vector_type(2))) float f2;
typedef __attribute__((ext_vector_type(4))) unsigned u4;
typedef __attribute__((ext_vector_type(2))) unsigned u2;
#define AS1 __attribute__((address_space(1)))
#define AS3 __attribute__((address_space(3)))
#define DI __device__ __forceinline__

constexpr int S_ = 2048, D_ = 640, DFF = 1536, N3 = 1920;
constexpr int WS_HF = 0;
constexpr int WS_PQ = WS_HF + 128 * 48 * 1024;
constexpr int WS_AF = WS_PQ + 128 * 20 * 1024;
constexpr int WS_W2 = WS_AF + 128 * 20 * 1024;
constexpr int WS_MF = WS_W2 + 8 * 48 * 5 * 1024;
constexpr int WS_KV = WS_MF + 8 * 20 * 5 * 1024;
constexpr int WS_Z = WS_KV + 16 * 32 * 400 * 4;
constexpr int WS_CG = WS_Z + 16 * 640 * 4;
constexpr int WS_SG = WS_CG + 4 * 3456 * 8;
constexpr int WS_FW = WS_SG + 2048 * 8;
constexpr int WS_F1 = WS_FW + 1024;
constexpr int WS_F2 = WS_F1 + 1024;
constexpr int WS_EP = WS_F2 + 256 * 128;
constexpr int WS_END = WS_EP + 128;
#define STAMPW(k, i) do {} while (0)
#define STAMP(k, i) do {} while (0)

DI float bfr(float v) { return (float)(__bf16)v; }
DI unsigned pk2(float a, float b) { f2 v = {a, b}; return __builtin_bit_cast(unsigned, __builtin_convertvector(v, bf2)); }
DI float asf(unsigned u) { return __builtin_bit_cast(float, u); }
DI float lo16(unsigned u) { return __builtin_bit_cast(float, u << 16); }
DI float hi16(unsigned u) { return __builtin_bit_cast(float, u & 0xffff0000u); }
DI f4 mfma(bf8 a, bf8 b, f4 c) { return __builtin_amdgcn_mfma_f32_16x16x32_bf16(a, b, c, 0, 0, 0); }
DI void glds16(const void* g, char* l) {
  __builtin_amdgcn_global_load_lds((const AS1 void*)g, (AS3 void*)l, 16, 0, 0);
}
DI bf8 trfrag(const char* p0, const char* p1) {
  s4 a = __builtin_amdgcn_ds_read_tr16_b64_v4i16((AS3 s4*)p0);
  s4 b = __builtin_amdgcn_ds_read_tr16_b64_v4i16((AS3 s4*)p1);
  return __builtin_bit_cast(bf8, __builtin_shufflevector(a, b, 0, 1, 2, 3, 4, 5, 6, 7));
}
DI bf8 trfrag3(const AS3 char* p0, const AS3 char* p1) {
  s4 a = __builtin_amdgcn_ds_read_tr16_b64_v4i16((AS3 s4*)p0);
  s4 b = __builtin_amdgcn_ds_read_tr16_b64_v4i16((AS3 s4*)p1);
  return __builtin_bit_cast(bf8, __builtin_shufflevector(a, b, 0, 1, 2, 3, 4, 5, 6, 7));
}
DI f4 phi4(f4 a, f4 r) {
  const f4 t = __builtin_elementwise_fma(a, r, f4{1e-6f, 1e-6f, 1e-6f, 1e-6f}), u = a * (r * 1.44269504088896341f);
  f4 m, e;
#pragma unroll
  for (int i = 0; i < 4; ++i) {
    m[i] = __builtin_amdgcn_fmed3f(t[i], 1e-6f, __builtin_inff());
    e[i] = __builtin_amdgcn_fmed3f(__builtin_amdgcn_exp2f(u[i]), 0.f, 1.f);
  }
  return m + e;
}
#define MT(i) do {} while (0)
#define WAITV(n) asm volatile("s_waitcnt vmcnt(" #n ")" ::: "memory")
#define WAITL0() asm volatile("s_waitcnt lgkmcnt(0)" ::: "memory")

#define PAUX 16
#define LAUX 0
typedef __amdgpu_buffer_rsrc_t rsrc_t;
DI rsrc_t mkrsrc(const void* p, unsigned bytes) { return __builtin_amdgcn_make_buffer_rsrc((void*)p, 0, bytes, 0x00020000); }
template <int AUX>
DI void blds16(rsrc_t r, char* l, int voff, int soff) {
  __builtin_amdgcn_raw_ptr_buffer_load_lds(r, (AS3 void*)l, 16, voff, soff, 0, AUX);
}
DI u4 ld16(rsrc_t r, int voff, int soff) { return __builtin_amdgcn_raw_buffer_load_b128(r, voff, soff, 16); }
DI void st16(u4 v, rsrc_t r, int voff, int soff) { __builtin_amdgcn_raw_buffer_store_b128(v, r, voff + soff, 0, 16); }
DI void pst16(u4 v, rsrc_t r, int voff, int soff) { __builtin_amdgcn_raw_buffer_store_b128(v, r, voff + soff, 0, PAUX); }
DI void pst8(u2 v, rsrc_t r, int voff, int soff) { __builtin_amdgcn_raw_buffer_store_b64(v, r, voff, soff, PAUX); }
DI void st8(u2 v, rsrc_t r, int voff, int soff) { __builtin_amdgcn_raw_buffer_store_b64(v, r, voff, soff, 16); }
DI void st4(unsigned v, rsrc_t r, int voff, int soff) { __builtin_amdgcn_raw_buffer_store_b32(v, r, voff, soff, 16); }
DI unsigned ld4(rsrc_t r, int voff, int soff) { return __builtin_amdgcn_raw_buffer_load_b32(r, voff, soff, 16); }
DI float wave_sum(float v) {
#define DPP_ADD(c) v += __builtin_bit_cast(float, __builtin_amdgcn_update_dpp(0, __builtin_bit_cast(int, v), c, 0xf, 0xf, true))
  DPP_ADD(0x111); DPP_ADD(0x112); DPP_ADD(0x114); DPP_ADD(0x118);
#undef DPP_ADD
  const int u = __builtin_bit_cast(int, v);
  return (__builtin_bit_cast(float, __builtin_amdgcn_readlane(u, 15)) + __builtin_bit_cast(float, __builtin_amdgcn_readlane(u, 31))) +
         (__builtin_bit_cast(float, __builtin_amdgcn_readlane(u, 47)) + __builtin_bit_cast(float, __builtin_amdgcn_readlane(u, 63)));
}
constexpr unsigned SPIN = 1u << 20;
#define PSLEEP 8
DI void wait16(rsrc_t rs, int voff, int soff, unsigned tag) {
#pragma unroll 1
  for (unsigned spin = 0; spin < SPIN; ++spin) {
    asm volatile("" : "+v"(voff) :: "memory");
    u4 v = ld16(rs, voff, soff);
    if (__all(v[0] == tag && v[1] == tag && v[2] == tag && v[3] == tag)) break;
    __builtin_amdgcn_s_sleep(PSLEEP);
  }
  asm volatile("" ::: "memory");
  __builtin_amdgcn_fence(__ATOMIC_ACQUIRE, "wavefront");
}
DI void wait4(rsrc_t rs, int voff, int soff, unsigned tag) {
#pragma unroll 1
  for (unsigned spin = 0; spin < SPIN; ++spin) {
    asm volatile("" : "+v"(voff) :: "memory");
    if (__all(ld4(rs, voff, soff) == tag)) break;
    __builtin_amdgcn_s_sleep(PSLEEP);
  }
  asm volatile("" ::: "memory");
  __builtin_amdgcn_fence(__ATOMIC_ACQUIRE, "wavefront");
}

DI void wait_lds(char* p, unsigned lvl) {
#pragma unroll 1
  for (unsigned spin = 0; spin < SPIN; ++spin) {
    if (__hip_atomic_load((AS3 unsigned*)p, __ATOMIC_RELAXED, __HIP_MEMORY_SCOPE_WORKGROUP) >= lvl) break;
    __builtin_amdgcn_s_sleep(1);
  }
  asm volatile("" ::: "memory");
  __builtin_amdgcn_fence(__ATOMIC_ACQUIRE, "wavefront");
}
DI void set_lds(char* p, unsigned lvl) { __hip_atomic_store((AS3 unsigned*)p, lvl, __ATOMIC_RELAXED, __HIP_MEMORY_SCOPE_WORKGROUP); }

constexpr int A_BYTES = 128 * 128;
constexpr int L_Q = 3 * A_BYTES;
constexpr int L_CB = L_Q + 3 * 64 * 448;
constexpr int L_ROW = L_CB + 20480;
constexpr int K1_LDS = L_ROW + 8192;

template <bool KV>
DI void k1_compute(char* ws, char* smem, int rb, int ci) {
  constexpr int NQ = KV ? 20 : 12, NH = KV ? 8 : 16;
  constexpr int QB = 64 * NQ * 16, HB = 64 * NH * 16;
  constexpr int L_H = L_Q + 3 * QB;
  constexpr int JQ = KV ? 5 : 3;
  const int lane = threadIdx.x & 63;
  const int w = __builtin_amdgcn_readfirstlane(threadIdx.x >> 6), wm = (w >> 1) & 1, wn = w & 1;
  const int g = lane >> 4, l15 = lane & 15, q4 = l15 >> 2, p4 = lane & 3;
  const rsrc_t rs = mkrsrc(ws, WS_END);
  const f4 zero4 = {0.f, 0.f, 0.f, 0.f};
  f4 acc[4][7];
    unsigned ra[2], rb_[7];
    {
      const unsigned lb = (unsigned)(size_t)(AS3 char*)smem;
      const int f = KV ? (((q4 >> 1) & 1) | ((g & 1) << 1)) : (q4 | ((g & 1) << 2));
#pragma unroll
      for (int s = 0; s < 2; ++s) ra[s] = lb + (wm * 64 + l15) * 128 + (((s * 4 + g) ^ (l15 >> 1)) << 4);
#pragma unroll
      for (int j = 0; j < 7; ++j) {
        const int cc = 2 * j + (p4 >> 1);
        const int pos = KV ? wn * 5 + cc + (cc >= 5 ? 5 : 0) : wn * 6 + cc;
        rb_[j] = lb + (j < JQ ? L_Q + (8 * g + q4) * (NQ * 16) + ((pos ^ ((g & 1) << 1)) << 4) + 8 * (p4 & 1)
                              : L_H + (8 * g + q4) * (NH * 16) + (((wn * (7 - JQ) + j - JQ) ^ f) << 5) + 8 * p4);
      }
    }
#pragma unroll
    for (int i = 0; i < 4; ++i)
#pragma unroll
      for (int j = 0; j < 7; ++j) acc[i][j] = zero4;
    bf8 fa[2][4], fb[2][7];
    s4 hb[2][7][2];
    auto rd1 = [&](int s, int stg, int n) {
      if (n < 4) fa[s][n] = *(const AS3 bf8*)(size_t)(ra[s] + stg * A_BYTES + n * 2048);
      else {
        const int j = (n - 4) >> 1, h = (n - 4) & 1;
        const AS3 char* p = (const AS3 char*)(size_t)rb_[j] + (j < JQ ? stg * QB + (s * 32 + 4 * h) * NQ * 16 : stg * HB + (s * 32 + 4 * h) * NH * 16);
        hb[s][j][h] = __builtin_amdgcn_ds_read_tr16_b64_v4i16((AS3 s4*)p);
        if (h) fb[s][j] = __builtin_bit_cast(bf8, __builtin_shufflevector(hb[s][j][0], hb[s][j][1], 0, 1, 2, 3, 4, 5, 6, 7));
      }
    };
    auto mm1 = [&](int s, int n) {
      const int j = n >> 2, i = n & 3;
      if (KV && j < 5) asm volatile("v_mfma_f32_16x16x32_bf16 %0, %1, %2, %0" : "+v"(acc[i][j]) : "v"(fa[s][i]), "v"(fb[s][j]));
      else asm volatile("v_mfma_f32_16x16x32_bf16 %0, %1, %2, %0" : "+v"(acc[i][j]) : "v"(fb[s][j]), "v"(fa[s][i]));
    };
    auto phase = [&](int sm, int sr, int stg, bool domm, bool dord) {
#pragma unroll
      for (int n = 0; n < 28; ++n) {
        if (domm) mm1(sm, n);
        if (dord && n < 18) rd1(sr, stg, n);
        __builtin_amdgcn_sched_barrier(0);
      }
      if (domm)
        asm volatile("" :: "v"(fa[sm][0]), "v"(fa[sm][1]), "v"(fa[sm][2]), "v"(fa[sm][3]), "v"(fb[sm][0]), "v"(fb[sm][1]), "v"(fb[sm][2]),
                     "v"(fb[sm][3]), "v"(fb[sm][4]), "v"(fb[sm][5]), "v"(fb[sm][6]));
    };
    auto body = [&](int stg, bool first) {
      __builtin_amdgcn_s_barrier();
      __builtin_amdgcn_sched_barrier(0);
      phase(1, 0, stg, !first, true);
      phase(0, 1, stg, true, true);
      WAITL0();
    };
#pragma unroll
    for (int j = 0; j < 7; ++j) asm volatile("" : "+v"(acc[0][j]), "+v"(acc[1][j]), "+v"(acc[2][j]), "+v"(acc[3][j]));
    STAMP(0, 1);
    body(0, true);
    STAMP(0, 2);
#pragma unroll 1
    for (int t = 0; t < 3; ++t) {
      body(1, false);
      body(2, false);
      body(0, false);
    }
    phase(1, 0, 0, true, false);
    asm volatile("s_nop 15" ::: "memory");
    __builtin_amdgcn_sched_barrier(0);
    STAMP(0, 4);
    __builtin_amdgcn_s_barrier();
    const char* sg = smem + L_ROW + 7168 + wm * 512;
    u2 ms[4];
    bf8 am[4];
#pragma unroll
    for (int i = 0; i < 4; ++i) {
      ms[i] = *(const u2*)(sg + (i * 16 + l15) * 8);
      am[i] = __builtin_bit_cast(bf8, u4{ms[i][0], ms[i][0], 0u, 0u});
    }
#pragma unroll
    for (int j = 0; j < 7; ++j) {
      const u2 c = *(const u2*)(smem + L_ROW + (g * 224 + (wn * 7 + j) * 16 + l15) * 8);
      const bf8 bm = __builtin_bit_cast(bf8, u4{c[0], c[1], 0u, 0u});
#pragma unroll
      for (int i = 0; i < 4; ++i) acc[i][j] = (KV && j < 5) ? mfma(am[i], bm, acc[i][j]) : mfma(bm, am[i], acc[i][j]);
    }
    STAMP(0, 5);
    const int rt0 = rb * 8 + wm * 4;
    typedef __attribute__((ext_vector_type(2))) short s2_t;
    auto relu2 = [&](float a, float b) {
      s2_t v = __builtin_bit_cast(s2_t, pk2(a, b));
      return __builtin_bit_cast(unsigned, __builtin_elementwise_max(v, s2_t{0, 0}));
    };
    if (KV) {
      f4 kv[8];
#pragma unroll
      for (int hp = 0; hp < 2; ++hp) {
        u4 fr[6];
#pragma unroll
        for (int ii = 0; ii < 2; ++ii) {
          const int i = hp * 2 + ii;
          const u4 s0 = *(const u4*)(sg + (i * 16 + 4 * g) * 8), s1 = *(const u4*)(sg + (i * 16 + 4 * g) * 8 + 16);
          const f4 rr = __builtin_bit_cast(f4, u4{s0[1], s0[3], s1[1], s1[3]});
#pragma unroll
          for (int j = 0; j < 5; ++j) {
            f4 y = j < 2 ? phi4(acc[i][j], rr) : acc[i][j] * rr;
            if (j == 2) {
              const f4 ph = phi4(acc[i][j], rr);
#pragma unroll
              for (int e = 0; e < 4; ++e) y[e] = l15 < 8 ? ph[e] : y[e];
            }
            fr[j][2 * ii] = pk2(y[0], y[1]);
            fr[j][2 * ii + 1] = pk2(y[2], y[3]);
          }
        }
#pragma unroll
        for (int e = 0; e < 4; ++e) fr[5][e] = l15 < 8 ? 0x3f803f80u : fr[2][e];
        const int ta[8] = {0, 0, 1, 1, 1, 2, 2, 2}, tb[8] = {5, 3, 5, 3, 4, 5, 3, 4};
#pragma unroll
        for (int m = 0; m < 8; ++m) kv[m] = mfma(__builtin_bit_cast(bf8, fr[tb[m]]), __builtin_bit_cast(bf8, fr[ta[m]]), hp ? kv[m] : zero4);
      }
#pragma unroll
      for (int m = 0; m < 8; ++m) *(f4*)(smem + (w * 8 + m) * 1024 + lane * 16) = kv[m];
      WAITL0();
      __builtin_amdgcn_s_barrier();
    }
    STAMP(0, 6);
#pragma unroll
    for (int i = 0; i < 4; ++i) {
      const float rstd = asf(ms[i][1]);
#pragma unroll
      for (int j = JQ; j < 7; j += 2) {
        const int ks = (KV ? 1024 + 64 * ci + 32 * wn : 128 * ci + 64 * wn + 16 * (j - JQ)) >> 5;
        const f4 y0 = acc[i][j] * rstd, y1 = acc[i][j + 1] * rstd;
        pst16(u4{relu2(y0[0], y0[1]), relu2(y0[2], y0[3]), relu2(y1[0], y1[1]), relu2(y1[2], y1[3])}, rs, lane * 16,
              WS_HF + (ks * 128 + rt0 + i) * 1024);
      }
      if (!KV) {
#pragma unroll
        for (int j = 0; j < 3; ++j) {
          if (j == 2 && wn) break;
          const int col = 80 * ci + 48 * wn + 16 * j;
          const f4 y = phi4(acc[i][j], f4{rstd, rstd, rstd, rstd});
          pst8(u2{pk2(y[0], y[1]), pk2(y[2], y[3])}, rs, lane * 16,
               WS_PQ + ((col >> 5) * 128 + rt0 + i) * 1024 + ((col >> 4) & 1) * 8);
        }
      }
    }
}

constexpr int CODE_LINES = 300;
__global__ __launch_bounds__(512, 2) void k1_kernel(const __bf16* __restrict__ x, const __bf16* __restrict__ wqkv,
                                                    const __bf16* __restrict__ w1, const __bf16* __restrict__ w2, char* ws) {
  extern __shared__ __attribute__((aligned(16))) char smem[];
  const int bid = blockIdx.x, xcd = bid & 7, idx = bid >> 3;
  const int tid = threadIdx.x, lane = tid & 63, w = __builtin_amdgcn_readfirstlane(tid >> 6);
  STAMP(0, 0);
  const int rb = (xcd >> 2) * 8 + (idx & 7), cgi = idx >> 3;
  const int ci = (xcd & 3) * 2 + (cgi >> 1);
  const bool kv = !(cgi & 1);
  unsigned pf0;
  {
    const char* pc;
    asm volatile("s_getpc_b64 %0" : "=s"(pc));
    asm volatile("global_load_dword %0, %1, off" : "=&v"(pf0) : "v"(pc + (tid < CODE_LINES ? tid : 0) * 128) : "memory");
  }
  const int g = lane >> 4, l15 = lane & 15, q4 = l15 >> 2, p4 = lane & 3;
  const rsrc_t rs = mkrsrc(ws, WS_END);
  const f4 zero4 = {0.f, 0.f, 0.f, 0.f};
  const unsigned tag = *(const __attribute__((address_space(4))) unsigned*)(ws + WS_EP) + 1u;
  const int jj = bid >> 3;
  if (__builtin_expect(w >= 4, 1)) {
    const int lw = w - 4;
    __builtin_amdgcn_s_setprio(3);
    const rsrc_t rx = mkrsrc(x, S_ * D_ * 2), rq = mkrsrc(wqkv, D_ * N3 * 2), r1 = mkrsrc(w1, D_ * DFF * 2);
    int vo[11];
#pragma unroll
    for (int n = 0; n < 4; ++n) {
      const int row = (n * 4 + lw) * 8 + (lane >> 3);
      vo[n] = (rb * 128 + row) * (D_ * 2) + (((lane & 7) ^ ((row >> 1) & 7)) << 4);
      if (n == 0) { for (int m = 1; m < 4; ++m) vo[m] = vo[0] + m * 32 * D_ * 2; break; }
    }
#define ROT1 3
    const int rot = ROT1 == 1 ? ((jj & 7) + 3 * (jj >> 3)) % 10 : (ROT1 == 2 ? ((jj & 1) * 5 + (jj >> 4) * 2) : (ROT1 == 3 ? ((jj & 3) * 5 / 2 + (jj >> 4)) : 0));
    auto ldA = [&](int stg, int t) {
      t += rot;
      t = t >= 10 ? t - 10 : t;
#pragma unroll
      for (int n = 0; n < 4; ++n) blds16<0>(rx, smem + stg * A_BYTES + n * 4096 + lw * 1024, vo[n], t * 128);
    };
    ldA(0, 0);
    const int nq = kv ? 20 : 12, sq = kv ? 5 : 3, qb = nq * 1024, hb = 28672 - qb;
    auto offs = [&](auto KVc) {
      constexpr bool KV = decltype(KVc)::value;
      constexpr int NQ = KV ? 20 : 12, NH = KV ? 8 : 16;
#pragma unroll
      for (int n = 0; n < 7; ++n) {
        if (n < NQ / 4) {
          const unsigned c = (n * 4 + lw) * 64 + lane, k = (c * (KV ? 3277u : 5462u)) >> 16, ch = (c - k * NQ) ^ ((k >> 2) & 2);
          vo[4 + n] = (k * N3 + 80 * ci + (KV ? (ch >= 10 ? 1200 : 640) + 8 * ch : 8 * (ch >= 10 ? ch - 2 : ch))) * 2;
        } else if (n == NQ / 4) {
          const unsigned c = lw * 64 + lane, k = c / NH;
          const int f = KV ? (((k >> 1) & 1) | ((k >> 2) & 2)) : ((k & 3) | ((k >> 1) & 4));
          vo[4 + n] = (k * DFF + (KV ? 1024 + 64 * ci : 128 * ci) + 8 * ((c - k * NH) ^ (f << 1))) * 2;
        } else vo[4 + n] = vo[4 + NQ / 4] + (n - NQ / 4) * (256 / NH) * DFF * 2;
      }
    };
    if (kv) offs(std::true_type{}); else offs(std::false_type{});
    const rsrc_t rm = mkrsrc(kv ? wqkv : w1, kv ? D_ * N3 * 2 : D_ * DFF * 2);
    auto ldB = [&](int stg, int t) {
      t += rot;
      t = t >= 10 ? t - 10 : t;
      char* dq = smem + L_Q + stg * qb + lw * 1024;
      char* dh = smem + L_Q + 3 * qb + stg * hb + lw * 1024 - sq * 4096;
      const int tq = t * (64 * N3 * 2), th = t * (64 * DFF * 2);
#pragma unroll
      for (int n = 0; n < 7; ++n) {
        if (n < 3) blds16<0>(rq, dq + n * 4096, vo[4 + n], tq);
        else if (n < 5) blds16<0>(rm, (kv ? dq : dh) + n * 4096, vo[4 + n], kv ? tq : th);
        else blds16<0>(r1, dh + n * 4096, vo[4 + n], th);
      }
    };
    ldB(0, 0);
    ldA(1, 1);
    ldB(1, 1);
    int bid2 = bid;
    asm volatile("" : "+s"(bid2));
  const int wks = bid2 / 5, wcb = bid2 - wks * 5;
  const int hf = (bid2 >> 2) & 1, qd = bid2 & 3, sel = (bid2 >> 3) >> 1;
  const bool cjob = (bid2 >> 3) < 27;
  const int cq = (bid2 >> 3) < 24 ? 2 * hf + ((bid2 >> 3) & 1) : (qd & 1) * 2 + hf;
  const int cl = (bid2 >> 3) >= 24 ? 10 * ((bid2 >> 3) - 24) + 5 * (qd >> 1) + 2
                          : (sel < 6 ? 10 * (sel >> 1) + 5 * (qd >> 1) + 3 * (qd & 1) + (sel & 1) : (sel < 10 ? 24 + 4 * qd + sel : 36 + 2 * qd + sel));
    int voG[2];
#pragma unroll
    for (int n = 0; n < 2; ++n) {
      const int c = (n * 4 + lw) * 64 + lane, qq = c / 112, rem = c - qq * 112, tl = rem >> 3, cw = tl >= 7, j = tl - 7 * cw, pr = (rem & 7) * 2;
      int col;
      if (kv) { int cc = j * 16 + pr; col = j < 5 ? 640 + 80 * ci + 40 * cw + (cc >= 40 ? 600 + cc : cc) : N3 + 1024 + 64 * ci + 32 * cw + cc - 80; }
      else col = j < 3 ? 80 * ci + 48 * cw + 16 * (cw && j == 2 ? 1 : j) + pr : N3 + 128 * ci + 64 * cw + 16 * (j - 3) + pr;
      voG[n] = c < 448 ? WS_CG + (qq * 3456 + col) * 8 : WS_SG + rb * 1024 + lane * 16;
    }
    auto side_loads = [&]() {
      const bool q = cl < 30;
      const rsrc_t rc = mkrsrc(q ? wqkv : w1, cjob ? (q ? D_ * N3 * 2 : D_ * DFF * 2) : 0);
      const int st = q ? N3 * 2 : DFF * 2;
      const int vb = (cq * 160 + lw * 8 + (lane >> 3)) * st + (q ? cl : cl - 30) * 128 + (lane & 7) * 16;
#pragma unroll
      for (int n = 0; n < 5; ++n) blds16<0>(rc, smem + L_CB + n * 4096 + lw * 1024, vb, n * 32 * st);
      const rsrc_t r2 = mkrsrc(w2, bid < 240 ? DFF * D_ * 2 : 0);
#pragma unroll
      for (int n = 0; n < 2; ++n) blds16<0>(r2, smem + L_ROW + n * 4096 + lw * 1024, ((wks * 32 + lw * 4 + g) * D_ + wcb * 128 + l15 * 8) * 2, n * 16 * D_ * 2);
    };
    int stg = 2;
#pragma unroll 1
    for (int t = 0; t < 10; ++t) {
      {
        if (t == 1 || t == 2) WAITV(18); else if (t < 9) WAITV(11); else WAITV(0);
        __builtin_amdgcn_s_barrier();
        __builtin_amdgcn_s_setprio(3);
        if (t == 8) {
#pragma unroll
          for (int n = 0; n < 2; ++n) blds16<16>(rs, smem + L_ROW + n * 4096 + lw * 1024, voG[n], 0);
        }
        if (t < 8) { ldA(stg, t + 2); ldB(stg, t + 2); }
        if (t == 0) side_loads();
        stg = stg == 2 ? 0 : stg + 1;
      }
      __builtin_amdgcn_s_setprio(0);
      if (t == 4) {
        const bf8 ones = __builtin_bit_cast(bf8, u4{0x3f803f80u, 0x3f803f80u, 0x3f803f80u, 0x3f803f80u});
        const char* c0 = smem + L_CB + (8 * g + q4) * 128 + lw * 32 + 8 * p4;
        f4 c = zero4;
#pragma unroll
        for (int k = 0; k < 5; ++k) c = mfma(ones, trfrag(c0 + k * 4096, c0 + k * 4096 + 512), c);
        if (g == 0 && cjob) st8(u2{__builtin_bit_cast(unsigned, c[0]), tag}, rs, l15 * 8, WS_CG + (cq * 3456 + cl * 64 + lw * 16) * 8);
      } else if (t == 5) {
#pragma unroll
        for (int n = 0; n < 2; ++n) {
          const char* p0 = smem + L_ROW + (4 * g + q4) * 256 + (lw * 2 + n) * 32 + 8 * p4;
          const bf8 f = trfrag(p0, p0 + 4096);
          const int col = wcb * 128 + (lw * 2 + n) * 16;
          if (bid < 240) pst16(__builtin_bit_cast(u4, f), rs, lane * 16, WS_W2 + (wks * 40 + (col >> 4)) * 1024);
        }
      }
      WAITL0();
    }
    asm volatile("" :: "v"(pf0));
#pragma unroll
    for (int n = 0; n < 2; ++n) {
#pragma unroll 1
      for (unsigned spin = 0; spin < SPIN; ++spin) {
        const u4 v = *(const u4*)(smem + L_ROW + n * 4096 + lw * 1024 + lane * 16);
        if (__all(v[1] == tag && v[3] == tag)) break;
        __builtin_amdgcn_s_sleep(1);
        asm volatile("" : "+v"(voG[n]) :: "memory");
        *(u4*)(smem + L_ROW + n * 4096 + lw * 1024 + lane * 16) = ld16(rs, voG[n], 0);
      }
      u4 v = *(const u4*)(smem + L_ROW + n * 4096 + lw * 1024 + lane * 16);
#pragma unroll
      for (int e = 0; e < 4; e += 2) {
        const float c = asf(v[e]), r1 = c - bfr(c), r2 = r1 - bfr(r1);
        const unsigned nm = (v[e] & 0xffffu) ^ 0x8000u;
        const bool st = n == 1 && lw == 3;
        v[e + 1] = st ? __builtin_bit_cast(unsigned, __builtin_amdgcn_rcpf(hi16(v[e]))) : pk2(r2, 0.f);
        v[e] = st ? nm | (nm << 16) : pk2(c, r1);
      }
      *(u4*)(smem + L_ROW + n * 4096 + lw * 1024 + lane * 16) = v;
    }
    WAITL0();
    __builtin_amdgcn_s_barrier();
    if (kv) {
      __builtin_amdgcn_s_barrier();
#pragma unroll 1
      for (int it = tid - 256; it < 480; it += 256) {
      const bool isz = it >= 400;
      int hl = isz ? (it - 400) / 20 : it / 100, rem = isz ? (it - 400 - hl * 20) * 5 : it - hl * 100, d = rem / 5, grp = rem - d * 5;
      int hk = hl & 1, kc = 20 * hk + d, a = kc >> 4;
      int pos = isz ? 32 : 40 + 20 * hk + 4 * grp, b = pos >> 4;
      int m = a == 0 ? b - 2 : (a == 1 ? b : b + 3);
      const char* p = smem + ((hl >> 1) * 8 + m) * 1024 + (((pos >> 2) & 3) * 16 + (kc & 15)) * 16;
      f4 v = *(const f4*)p + *(const f4*)(p + 16384);
      if (isz) __builtin_amdgcn_raw_buffer_store_b32(__builtin_bit_cast(unsigned, v[0]), rs, (it - 400) * 4, WS_Z + (rb * 640 + ci * 80) * 4, 0);
      else pst16(__builtin_bit_cast(u4, v), rs, it * 16, WS_KV + (rb * 32 + ci * 4) * 1600);
      }
    }
    if (bid == 0 && w == 7) {
      wait16(rs, lane * 16, WS_FW, tag);
      if (lane == 0) st4(tag, rs, 0, WS_EP);
    }
  } else {
    u4 xa[2], xb[2];
    {
      const int srow = ((bid >> 2) & 1) * 1024 + (bid & 3) * 256 + jj * 8;
      const char* xr = (const char*)x + (srow + w * 2) * (D_ * 2);
      const char *p0 = xr + lane * 16, *p1 = xr + 1024 + l15 * 16;
      asm volatile("global_load_dwordx4 %0, %4, off\n\tglobal_load_dwordx4 %1, %5, off\n\tglobal_load_dwordx4 %2, %4, off offset:1280\n\t"
                   "global_load_dwordx4 %3, %5, off offset:1280"
                   : "=&v"(xa[0]), "=&v"(xb[0]), "=&v"(xa[1]), "=&v"(xb[1]) : "v"(p0), "v"(p1) : "memory");
      asm volatile("s_waitcnt vmcnt(0)" : "+v"(pf0), "+v"(xa[0]), "+v"(xb[0]), "+v"(xa[1]), "+v"(xb[1]) :: "memory");
        float su[2] = {0.f, 0.f}, sq[2] = {0.f, 0.f}, sh[2];
#pragma unroll
        for (int n = 0; n < 2; ++n) {
          sh[n] = lo16(__builtin_amdgcn_readfirstlane(xa[n][0]));
          float s2 = 0.f, q2 = 0.f;
#pragma unroll
          for (int e = 0; e < 4; ++e) {
            const float a0 = lo16(xa[n][e]) - sh[n], a1 = hi16(xa[n][e]) - sh[n], b0 = lo16(xb[n][e]) - sh[n], b1 = hi16(xb[n][e]) - sh[n];
            su[n] += a0 + a1; sq[n] += a0 * a0; sq[n] += a1 * a1; s2 += b0 + b1; q2 += b0 * b0; q2 += b1 * b1;
          }
          if (lane < 16) { su[n] += s2; sq[n] += q2; }
        }
        const float m0 = wave_sum(su[0]) * (1.f / 640.f), m1 = wave_sum(su[1]) * (1.f / 640.f);
        const float e0 = wave_sum(sq[0]) * (1.f / 640.f), e1 = wave_sum(sq[1]) * (1.f / 640.f);
        const float m = lane ? m1 : m0, var = fmaxf((lane ? e1 : e0) - m * m, 0.f);
        const float sd = __builtin_amdgcn_sqrtf(bfr(bfr(var) + 1e-5f));
        if (lane < 2) st8(u2{pk2(m + (lane ? sh[1] : sh[0]), sd), tag}, rs, lane * 8, WS_SG + (srow + w * 2) * 8);
        if (tid == 0) { st4(tag, rs, 0, WS_FW + bid * 4); pst16(u4{0u, 0u, 0u, 0u}, rs, 0, WS_F2 + bid * 128); }
    }
    if (kv) k1_compute<true>(ws, smem, rb, ci);
    else k1_compute<false>(ws, smem, rb, ci);
  }
  STAMP(0, 7);
}

template <int AUX>
DI bf8 ldfrag(rsrc_t r, int voff, int soff) {
  return __builtin_bit_cast(bf8, __builtin_amdgcn_raw_buffer_load_b128(r, voff, soff, AUX));
}
DI void st8_sc1(char* p, unsigned a, unsigned b) {
  __hip_atomic_store((AS1 unsigned long long*)p, (unsigned long long)a | ((unsigned long long)b << 32), __ATOMIC_RELAXED,
                     __HIP_MEMORY_SCOPE_AGENT);
}
constexpr int L2_ACC = 0;
constexpr int L2_WO = 61440;
constexpr int L2_KV = L2_WO + 5120;
constexpr int L2_Z = L2_KV + 2048;
constexpr int L2_X = L2_Z + 2048;
constexpr int K2_LDS = 98304;

#define RING 2
DI void k2_publish(char* ws, int flag, int lane) {
  WAITV(0);
  if (lane == 0) __hip_atomic_store((AS1 unsigned*)(ws + WS_F2 + flag), 1u, __ATOMIC_RELAXED, __HIP_MEMORY_SCOPE_AGENT);
}
__global__ __launch_bounds__(512, 2) void k2_kernel(const __bf16* __restrict__ x, const __bf16* __restrict__ wo,
                                                    char* ws, __bf16* __restrict__ out) {
  extern __shared__ __attribute__((aligned(16))) char smem[];
  const int tid = threadIdx.x, lane = tid & 63, w = __builtin_amdgcn_readfirstlane(tid >> 6);
  const int g = lane >> 4, l15 = lane & 15;
  const int xcd = blockIdx.x & 7, idx = blockIdx.x >> 3;
  const int r = (xcd >> 1) * 8 + (idx & 7), c = (xcd & 1) * 4 + (idx >> 3);
  STAMP(1, 0);
  unsigned pf0 = 0;
  if (w == 7 || w == 6) {
    const char* pc;
    asm volatile("s_getpc_b64 %0" : "=s"(pc));
    asm volatile("global_load_dword %0, %1, off" : "=&v"(pf0) : "v"(pc + (tid - 384 < 96 ? tid - 384 : 0) * 128) : "memory");
  }
  const rsrc_t rs = mkrsrc(ws, (unsigned)WS_END);
  const int vo = lane * 16;
  const f4 zero4 = {0.f, 0.f, 0.f, 0.f};
  if (__builtin_expect(w >= 4, 0)) {
  __builtin_amdgcn_s_setprio(3);
  if (w == 4) {
    const f4* p = (const f4*)(ws + WS_KV) + r * 100;
    const int i1 = lane < 36 ? lane + 64 : lane;
    f4 v0[16], v1[16];
#pragma unroll
    for (int i = 0; i < 16; ++i) { v0[i] = p[i * 3200 + lane]; v1[i] = p[i * 3200 + i1]; }
    u4 wv[4];
#pragma unroll
    for (int i = 0; i < 4; ++i) {
      int ch = i * 64 + lane, row = ch / 10;
      wv[i] = u4{0u, 0u, 0u, 0u};
      if (ch < 200) wv[i] = *(const u4*)(wo + (20 * r + row) * D_ + 80 * c + (ch - row * 10) * 8);
    }
#pragma unroll
    for (int st = 1; st < 16; st *= 2)
#pragma unroll
      for (int i = 0; i < 16; i += 2 * st) { v0[i] += v0[i + st]; v1[i] += v1[i + st]; }
    *(f4*)(smem + L2_KV + vo) = v0[0];
    *(f4*)(smem + L2_KV + 1024 + vo) = v1[0];
#pragma unroll
    for (int i = 0; i < 4; ++i) *(u4*)(smem + L2_WO + i * 1024 + vo) = wv[i];
    *(u4*)(smem + L2_WO + 4096 + vo) = u4{0u, 0u, 0u, 0u};
    u4 au[2];
#pragma unroll
    for (int dt = 0; dt < 2; ++dt)
#pragma unroll
      for (int hh = 0; hh < 2; ++hh) {
        int d = dt * 16 + l15, grp = 2 * g + hh;
        bool ok = d < 20 && grp < 5;
        f4 v = *(const f4*)(smem + L2_KV + (ok ? d * 5 + grp : 0) * 16);
        au[dt][2 * hh] = ok ? pk2(v[0], v[1]) : 0u;
        au[dt][2 * hh + 1] = ok ? pk2(v[2], v[3]) : 0u;
      }
    const int k0 = 20 * r + 4 * g, k1 = 20 * r + 16;
    const char* p0 = smem + L2_WO + (8 * g + (l15 >> 2)) * 160 + (lane & 3) * 8;
    char* mf = ws + WS_MF + c * 5120 + l15 * 16;
    const int o0 = (k0 >> 5) * 40960 + ((k0 >> 2) & 3) * 256 + ((k0 >> 4) & 1) * 8;
    const int o1 = (k1 >> 5) * 40960 + ((k1 >> 2) & 3) * 256 + ((k1 >> 4) & 1) * 8;
#pragma unroll
    for (int t = 0; t < 5; ++t) {
      bf8 b = trfrag(p0 + t * 32, p0 + t * 32 + 640);
      f4 m0 = mfma(__builtin_bit_cast(bf8, au[0]), b, zero4), m1 = mfma(__builtin_bit_cast(bf8, au[1]), b, zero4);
      st8_sc1(mf + t * 1024 + o0, pk2(m0[0], m0[1]), pk2(m0[2], m0[3]));
      if (g == 0) st8_sc1(mf + t * 1024 + o1, pk2(m1[0], m1[1]), pk2(m1[2], m1[3]));
    }
    k2_publish(ws, (r * 8 + c) * 128, lane);
    STAMPW(1, 8);
  } else if (w == 5 || w == 6) {
    const int fo = ((c >> 1) * 5 * 128 + r * 4 + (c & 1) * 2 + w - 5) * 1024;
    const f4* zp = (const f4*)(ws + WS_Z) + (c >> 1) * 40 + (lane < 40 ? lane : 0);
    f4 zv[16];
#pragma unroll
    for (int i = 0; i < 16; ++i) zv[i] = zp[i * 160];
    u4 pq[5];
#pragma unroll
    for (int k = 0; k < 5; ++k) pq[k] = __builtin_bit_cast(u4, ldfrag<0>(rs, vo, (int)WS_PQ + fo + k * 131072));
#pragma unroll
    for (int st = 1; st < 16; st *= 2)
#pragma unroll
      for (int i = 0; i < 16; i += 2 * st) zv[i] += zv[i + st];
    char* zl = smem + L2_Z + (w - 5) * 1024;
    if (lane < 40) *(u2*)(zl + lane * 8) = u2{pk2(zv[0][0], zv[0][1]), pk2(zv[0][2], zv[0][3])};
    f4 den = zero4;
#pragma unroll
    for (int k = 0; k < 5; ++k) {
      u4 zf;
#pragma unroll
      for (int p = 0; p < 2; ++p) {
        int gi = 8 * k + 4 * p + g;
        u2 z2 = *(const u2*)(zl + gi * 8);
        bool mine = gi / 5 == l15;
        zf[2 * p] = mine ? z2[0] : 0u;
        zf[2 * p + 1] = mine ? z2[1] : 0u;
      }
      den = mfma(__builtin_bit_cast(bf8, zf), __builtin_bit_cast(bf8, pq[k]), den);
    }
    f4 rd;
#pragma unroll
    for (int e = 0; e < 4; ++e) rd[e] = __builtin_amdgcn_rcpf(bfr(bfr(den[e]) + 1e-6f));
    if (g < 2) *(f4*)(zl + 512 + l15 * 32 + g * 16) = rd;
#pragma unroll
    for (int k = 0; k < 5; ++k) {
      u4 o;
#pragma unroll
      for (int p = 0; p < 2; ++p) {
        float s = *(const float*)(zl + 512 + l15 * 32 + ((8 * k + 4 * p + g) / 5) * 4);
        o[2 * p] = pk2(lo16(pq[k][2 * p]) * s, hi16(pq[k][2 * p]) * s);
        o[2 * p + 1] = pk2(lo16(pq[k][2 * p + 1]) * s, hi16(pq[k][2 * p + 1]) * s);
      }
      st16(o, rs, vo, WS_AF + fo + k * 131072);
    }
    k2_publish(ws, (r * 8 + c) * 128 + (w - 4) * 4, lane);
    STAMPW(1, 9);
  }
    if (w == 7) {
      const rsrc_t rx = mkrsrc(x, S_ * D_ * 2);
#pragma unroll 1
      for (int i = 0; i < 10; ++i) {
        const int ch = i * 64 + lane, row = ch / 10;
        blds16<0>(rx, smem + L2_X + i * 1024, ((r * 64 + row) * D_ + c * 80 + (ch - row * 10) * 8) * 2, 0);
      }
    }
    asm volatile("s_waitcnt vmcnt(0)" : "+v"(pf0) :: "memory");
    __builtin_amdgcn_s_barrier();
    return;
  }
  const int hA = (int)WS_HF + (w * 128 + r * 4) * 1024, hB = (int)WS_W2 + (w * 8 + c) * 5120;
  const int aA = (int)WS_AF + (w * 128 + r * 4) * 1024, aB = (int)WS_MF + (w * 8 + c) * 5120;
#define ROT 0
  const int rot = ROT ? ((idx >> 3) * 3 + (idx & 7) * 5) % 12 : 0, rot2 = ROT ? idx % 5 : 0;
  bf8 fa[RING][4], fb[RING][5];
  auto ld = [&](int i) {
    const int sl = i % RING;
    if (i < 12) {
      int ii = i + rot;
      ii = ii >= 12 ? ii - 12 : ii;
#pragma unroll
      for (int j = 0; j < 5; ++j) fb[sl][j] = ldfrag<LAUX>(rs, vo + j * 1024, hB + ii * 163840);
#pragma unroll
      for (int j = 0; j < 4; ++j) fa[sl][j] = ldfrag<LAUX>(rs, vo + j * 1024, hA + ii * 524288);
    } else {
      int ii = i - 12 + rot2;
      ii = ii >= 5 ? ii - 5 : ii;
#pragma unroll
      for (int j = 0; j < 5; ++j) fb[sl][j] = ldfrag<16>(rs, vo + j * 1024, aB + ii * 163840);
#pragma unroll
      for (int j = 0; j < 4; ++j) fa[sl][j] = ldfrag<16>(rs, vo + j * 1024, aA + ii * 524288);
    }
  };
#pragma unroll
  for (int i = 0; i < RING; ++i) ld(i);
  const int orow = (r * 64 + w * 16 + l15) * D_ + c * 80 + 4 * g;
  const AS1 unsigned* fl = (const AS1 unsigned*)(ws + WS_F2 + (lane < 16 ? (r * 8 + (lane >> 1)) * 128 + 4 + (lane & 1) * 4 : (((lane - 16) & 31) * 8 + c) * 128));
  STAMP(1, 1);
  __builtin_amdgcn_sched_barrier(0);
  f4 acc[4][5];
#pragma unroll
  for (int i = 0; i < 4; ++i)
#pragma unroll
    for (int j = 0; j < 5; ++j) acc[i][j] = zero4;
  unsigned flv = 0;
#pragma unroll
  for (int i = 0; i < 17; ++i) {
    const int sl = i % RING;
#pragma unroll
    for (int j = 0; j < 5; ++j)
#pragma unroll
      for (int m = 0; m < 4; ++m) acc[m][j] = mfma(fb[sl][j], fa[sl][m], acc[m][j]);
    if (i + RING == 11) flv = __hip_atomic_load(fl, __ATOMIC_RELAXED, __HIP_MEMORY_SCOPE_AGENT);
    if (i + RING == 12) {
      for (unsigned spin = 0; !__all(flv == 1u) && spin < (1u << 22); ++spin) {
        __builtin_amdgcn_s_sleep(1);
        flv = __hip_atomic_load(fl, __ATOMIC_RELAXED, __HIP_MEMORY_SCOPE_AGENT);
      }
      __builtin_amdgcn_fence(__ATOMIC_ACQUIRE, "wavefront");
    }
    if (i + RING < 17) ld(i + RING);

    __builtin_amdgcn_sched_barrier(0);
  }
  STAMP(1, 6);
#pragma unroll
  for (int m = 0; m < 4; ++m) {
    if (m != w) {
      int slot = m < w ? m : m - 1;
#pragma unroll
      for (int j = 0; j < 5; ++j) *(f4*)(smem + L2_ACC + ((w * 3 + slot) * 5 + j) * 1024 + vo) = acc[m][j];
    }
  }
  WAITL0();
  __builtin_amdgcn_s_barrier();
  f4 own[5];
#pragma unroll
  for (int j = 0; j < 5; ++j) own[j] = w == 0 ? acc[0][j] : (w == 1 ? acc[1][j] : (w == 2 ? acc[2][j] : acc[3][j]));
  f4 tot[5];
  u2 xr[5];
#pragma unroll
  for (int j = 0; j < 5; ++j) xr[j] = *(const u2*)(smem + L2_X + (w * 16 + l15) * 160 + j * 32 + g * 8);
#pragma unroll
  for (int j = 0; j < 5; ++j) tot[j] = f4{lo16(xr[j][0]), hi16(xr[j][0]), lo16(xr[j][1]), hi16(xr[j][1])};
#pragma unroll
  for (int s = 0; s < 4; ++s) {
    int slot = w < s ? w : w - 1;
#pragma unroll
    for (int j = 0; j < 5; ++j) {
      f4 v = own[j];
      if (s != w) v = *(const f4*)(smem + L2_ACC + ((s * 3 + slot) * 5 + j) * 1024 + vo);
      tot[j] += v;
    }
  }
#pragma unroll
  for (int j = 0; j < 5; ++j) *(u2*)(out + orow + 16 * j) = u2{pk2(tot[j][0], tot[j][1]), pk2(tot[j][2], tot[j][3])};
  STAMP(1, 7);
}

extern "C" void kernel_launch(void* const* d_in, const int* in_sizes, int n_in, void* d_out, int out_size, void* d_ws,
                              size_t ws_size, hipStream_t stream) {
  const __bf16* x = (const __bf16*)d_in[0];
  const __bf16* wqkv = (const __bf16*)d_in[1];
  const __bf16* wo = (const __bf16*)d_in[2];
  const __bf16* w1 = (const __bf16*)d_in[4];
  const __bf16* w2 = (const __bf16*)d_in[5];
  char* ws = (char*)d_ws;
  (void)hipFuncSetAttribute((const void*)k1_kernel, hipFuncAttributeMaxDynamicSharedMemorySize, K1_LDS);
  (void)hipFuncSetAttribute((const void*)k2_kernel, hipFuncAttributeMaxDynamicSharedMemorySize, K2_LDS);
  k1_kernel<<<256, 512, K1_LDS, stream>>>(x, wqkv, w1, w2, ws);
  k2_kernel<<<256, 512, K2_LDS, stream>>>(x, wo, ws, (__bf16*)d_out);
}
```

```cpp
#include <hip/hip_runtime.h>
#include <hip/hip_bf16.h>
#include <stdint.h>
#include <type_traits>

typedef __attribute__((ext_vector_type(8))) __bf16 bf8;
typedef __attribute__((ext_vector_type(4))) __bf16 bf4;
typedef __attribute__((ext_vector_type(2))) __bf16 bf2;
typedef __attribute__((ext_vector_type(4))) short s4;
typedef __attribute__((ext_vector_type(4))) float f4;
typedef __attribute__((ext_vector_type(2))) float f2;
typedef __attribute__((ext_vector_type(4))) unsigned u4;
typedef __attribute__((ext_vector_type(2))) unsigned u2;
#define AS1 __attribute__((address_space(1)))
#define AS3 __attribute__((address_space(3)))
#define DI __device__ __forceinline__

constexpr int S_ = 2048, D_ = 640, DFF = 1536, N3 = 1920;
constexpr int WS_HF = 0;
constexpr int WS_PQ = WS_HF + 128 * 48 * 1024;
constexpr int WS_AF = WS_PQ + 128 * 20 * 1024;
constexpr int WS_W2 = WS_AF + 128 * 20 * 1024;
constexpr int WS_MF = WS_W2 + 8 * 48 * 5 * 1024;
constexpr int WS_KV = WS_MF + 8 * 20 * 5 * 1024;
constexpr int WS_Z = WS_KV + 16 * 32 * 400 * 4;
constexpr int WS_CG = WS_Z + 16 * 640 * 4;
constexpr int WS_SG = WS_CG + 4 * 3456 * 8;
constexpr int WS_FW = WS_SG + 2048 * 8;
constexpr int WS_F1 = WS_FW + 1024;
constexpr int WS_F2 = WS_F1 + 1024;
constexpr int WS_EP = WS_F2 + 256 * 128;
constexpr int WS_END = WS_EP + 128;

DI float bfr(float v) { return (float)(__bf16)v; }
DI unsigned pk2(float a, float b) { f2 v = {a, b}; return __builtin_bit_cast(unsigned, __builtin_convertvector(v, bf2)); }
DI float asf(unsigned u) { return __builtin_bit_cast(float, u); }
DI float lo16(unsigned u) { return __builtin_bit_cast(float, u << 16); }
DI float hi16(unsigned u) { return __builtin_bit_cast(float, u & 0xffff0000u); }
DI f4 mfma(bf8 a, bf8 b, f4 c) { return __builtin_amdgcn_mfma_f32_16x16x32_bf16(a, b, c, 0, 0, 0); }
DI void glds16(const void* g, char* l) {
  __builtin_amdgcn_global_load_lds((const AS1 void*)g, (AS3 void*)l, 16, 0, 0);
}
DI bf8 trfrag(const char* p0, const char* p1) {
  s4 a = __builtin_amdgcn_ds_read_tr16_b64_v4i16((AS3 s4*)p0);
  s4 b = __builtin_amdgcn_ds_read_tr16_b64_v4i16((AS3 s4*)p1);
  return __builtin_bit_cast(bf8, __builtin_shufflevector(a, b, 0, 1, 2, 3, 4, 5, 6, 7));
}
DI bf8 trfrag3(const AS3 char* p0, const AS3 char* p1) {
  s4 a = __builtin_amdgcn_ds_read_tr16_b64_v4i16((AS3 s4*)p0);
  s4 b = __builtin_amdgcn_ds_read_tr16_b64_v4i16((AS3 s4*)p1);
  return __builtin_bit_cast(bf8, __builtin_shufflevector(a, b, 0, 1, 2, 3, 4, 5, 6, 7));
}
template <int OFF>
DI bf8 trfrag_asm(const char* p) {
  u2 a, b;
  asm volatile("ds_read_b64_tr_b16 %0, %2\n\tds_read_b64_tr_b16 %1, %2 offset:%3\n\ts_waitcnt lgkmcnt(0)"
               : "=&v"(a), "=&v"(b) : "v"((unsigned)(size_t)(const AS3 char*)p), "i"(OFF) : "memory");
  return __builtin_bit_cast(bf8, u4{a[0], a[1], b[0], b[1]});
}
DI f4 phi4(f4 a, f4 r) {
  const f4 t = __builtin_elementwise_fma(a, r, f4{1e-6f, 1e-6f, 1e-6f, 1e-6f}), u = a * (r * 1.44269504088896341f);
  f4 m, e;
#pragma unroll
  for (int i = 0; i < 4; ++i) {
    m[i] = __builtin_amdgcn_fmed3f(t[i], 1e-6f, __builtin_inff());
    e[i] = __builtin_amdgcn_fmed3f(__builtin_amdgcn_exp2f(u[i]), 0.f, 1.f);
  }
  return m + e;
}
#define WAITV(n) asm volatile("s_waitcnt vmcnt(" #n ")" ::: "memory")
#define WAITL0() asm volatile("s_waitcnt lgkmcnt(0)" ::: "memory")

#define PAUX 16
#define LAUX 0
typedef __amdgpu_buffer_rsrc_t rsrc_t;
DI rsrc_t mkrsrc(const void* p, unsigned bytes) { return __builtin_amdgcn_make_buffer_rsrc((void*)p, 0, bytes, 0x00020000); }
template <int AUX>
DI void blds16(rsrc_t r, char* l, int voff, int soff) {
  __builtin_amdgcn_raw_ptr_buffer_load_lds(r, (AS3 void*)l, 16, voff, soff, 0, AUX);
}
DI u4 ld16(rsrc_t r, int voff, int soff) { return __builtin_amdgcn_raw_buffer_load_b128(r, voff, soff, 16); }
DI void st16(u4 v, rsrc_t r, int voff, int soff) { __builtin_amdgcn_raw_buffer_store_b128(v, r, voff + soff, 0, 16); }
DI void pst16(u4 v, rsrc_t r, int voff, int soff) { __builtin_amdgcn_raw_buffer_store_b128(v, r, voff + soff, 0, PAUX); }
DI void pst8(u2 v, rsrc_t r, int voff, int soff) { __builtin_amdgcn_raw_buffer_store_b64(v, r, voff, soff, PAUX); }
DI void st8(u2 v, rsrc_t r, int voff, int soff) { __builtin_amdgcn_raw_buffer_store_b64(v, r, voff, soff, 16); }
DI void st4(unsigned v, rsrc_t r, int voff, int soff) { __builtin_amdgcn_raw_buffer_store_b32(v, r, voff, soff, 16); }
DI unsigned ld4(rsrc_t r, int voff, int soff) { return __builtin_amdgcn_raw_buffer_load_b32(r, voff, soff, 16); }
DI float wave_sum(float v) {
#define DPP_ADD(c) v += __builtin_bit_cast(float, __builtin_amdgcn_update_dpp(0, __builtin_bit_cast(int, v), c, 0xf, 0xf, true))
  DPP_ADD(0x111); DPP_ADD(0x112); DPP_ADD(0x114); DPP_ADD(0x118);
#undef DPP_ADD
  const int u = __builtin_bit_cast(int, v);
  return (__builtin_bit_cast(float, __builtin_amdgcn_readlane(u, 15)) + __builtin_bit_cast(float, __builtin_amdgcn_readlane(u, 31))) +
         (__builtin_bit_cast(float, __builtin_amdgcn_readlane(u, 47)) + __builtin_bit_cast(float, __builtin_amdgcn_readlane(u, 63)));
}
constexpr unsigned SPIN = 1u << 20;
#define PSLEEP 8
DI void wait16(rsrc_t rs, int voff, int soff, unsigned tag) {
#pragma unroll 1
  for (unsigned spin = 0; spin < SPIN; ++spin) {
    asm volatile("" : "+v"(voff) :: "memory");
    u4 v = ld16(rs, voff, soff);
    if (__all(v[0] == tag && v[1] == tag && v[2] == tag && v[3] == tag)) break;
    __builtin_amdgcn_s_sleep(PSLEEP);
  }
  asm volatile("" ::: "memory");
  __builtin_amdgcn_fence(__ATOMIC_ACQUIRE, "wavefront");
}
DI void wait4(rsrc_t rs, int voff, int soff, unsigned tag) {
#pragma unroll 1
  for (unsigned spin = 0; spin < SPIN; ++spin) {
    asm volatile("" : "+v"(voff) :: "memory");
    if (__all(ld4(rs, voff, soff) == tag)) break;
    __builtin_amdgcn_s_sleep(PSLEEP);
  }
  asm volatile("" ::: "memory");
  __builtin_amdgcn_fence(__ATOMIC_ACQUIRE, "wavefront");
}

DI void wait_lds(char* p, unsigned lvl) {
#pragma unroll 1
  for (unsigned spin = 0; spin < SPIN; ++spin) {
    if (__hip_atomic_load((AS3 unsigned*)p, __ATOMIC_RELAXED, __HIP_MEMORY_SCOPE_WORKGROUP) >= lvl) break;
    __builtin_amdgcn_s_sleep(1);
  }
  asm volatile("" ::: "memory");
  __builtin_amdgcn_fence(__ATOMIC_ACQUIRE, "wavefront");
}
DI void set_lds(char* p, unsigned lvl) { __hip_atomic_store((AS3 unsigned*)p, lvl, __ATOMIC_RELAXED, __HIP_MEMORY_SCOPE_WORKGROUP); }

constexpr int A_BYTES = 128 * 128;
constexpr int L_Q = 3 * A_BYTES;
constexpr int L_CB = L_Q + 3 * 64 * 448;
constexpr int L_ROW = L_CB + 20480;
constexpr int K1_LDS = L_ROW + 8192;

template <bool KV>
DI void k1_compute(char* ws, char* smem, int rb, int ci) {
  constexpr int NQ = KV ? 20 : 12, NH = KV ? 8 : 16;
  constexpr int QB = 64 * NQ * 16, HB = 64 * NH * 16;
  constexpr int L_H = L_Q + 3 * QB;
  constexpr int JQ = KV ? 5 : 3;
  const int lane = threadIdx.x & 63;
  const int w = __builtin_amdgcn_readfirstlane(threadIdx.x >> 6), wm = (w >> 1) & 1, wn = w & 1;
  const int g = lane >> 4, l15 = lane & 15, q4 = l15 >> 2, p4 = lane & 3;
  const rsrc_t rs = mkrsrc(ws, WS_END);
  const f4 zero4 = {0.f, 0.f, 0.f, 0.f};
  f4 acc[4][7];
    unsigned ra[2], rb_[7];
    {
      const unsigned lb = (unsigned)(size_t)(AS3 char*)smem;
      const int f = KV ? (((q4 >> 1) & 1) | ((g & 1) << 1)) : (q4 | ((g & 1) << 2));
#pragma unroll
      for (int s = 0; s < 2; ++s) ra[s] = lb + (wm * 64 + l15) * 128 + (((s * 4 + g) ^ (l15 >> 1)) << 4);
#pragma unroll
      for (int j = 0; j < 7; ++j) {
        const int cc = 2 * j + (p4 >> 1);
        const int pos = KV ? wn * 5 + cc + (cc >= 5 ? 5 : 0) : wn * 6 + cc;
        rb_[j] = lb + (j < JQ ? L_Q + (8 * g + q4) * (NQ * 16) + ((pos ^ ((g & 1) << 1)) << 4) + 8 * (p4 & 1)
                              : L_H + (8 * g + q4) * (NH * 16) + (((wn * (7 - JQ) + j - JQ) ^ f) << 5) + 8 * p4);
      }
    }
#pragma unroll
    for (int i = 0; i < 4; ++i)
#pragma unroll
      for (int j = 0; j < 7; ++j) acc[i][j] = zero4;
    bf8 fa[2][4], fb[2][7];
    s4 hb[2][7][2];
    auto rd1 = [&](int s, int stg, int n) {
      if (n < 4) fa[s][n] = *(const AS3 bf8*)(size_t)(ra[s] + stg * A_BYTES + n * 2048);
      else {
        const int j = (n - 4) >> 1, h = (n - 4) & 1;
        const AS3 char* p = (const AS3 char*)(size_t)rb_[j] + (j < JQ ? stg * QB + (s * 32 + 4 * h) * NQ * 16 : stg * HB + (s * 32 + 4 * h) * NH * 16);
        hb[s][j][h] = __builtin_amdgcn_ds_read_tr16_b64_v4i16((AS3 s4*)p);
        if (h) fb[s][j] = __builtin_bit_cast(bf8, __builtin_shufflevector(hb[s][j][0], hb[s][j][1], 0, 1, 2, 3, 4, 5, 6, 7));
      }
    };
    auto mm1 = [&](int s, int n) {
      const int j = n >> 2, i = n & 3;
      if (KV && j < 5) asm volatile("v_mfma_f32_16x16x32_bf16 %0, %1, %2, %0" : "+v"(acc[i][j]) : "v"(fa[s][i]), "v"(fb[s][j]));
      else asm volatile("v_mfma_f32_16x16x32_bf16 %0, %1, %2, %0" : "+v"(acc[i][j]) : "v"(fb[s][j]), "v"(fa[s][i]));
    };
    auto phase = [&](int sm, int sr, int stg, bool domm, bool dord) {
#pragma unroll
      for (int n = 0; n < 28; ++n) {
        if (domm) mm1(sm, n);
        if (dord && n < 18) rd1(sr, stg, n);
        __builtin_amdgcn_sched_barrier(0);
      }
      if (domm)
        asm volatile("" :: "v"(fa[sm][0]), "v"(fa[sm][1]), "v"(fa[sm][2]), "v"(fa[sm][3]), "v"(fb[sm][0]), "v"(fb[sm][1]), "v"(fb[sm][2]),
                     "v"(fb[sm][3]), "v"(fb[sm][4]), "v"(fb[sm][5]), "v"(fb[sm][6]));
    };
    auto body = [&](int stg, bool first) {
      __builtin_amdgcn_s_barrier();
      __builtin_amdgcn_sched_barrier(0);
      phase(1, 0, stg, !first, true);
      phase(0, 1, stg, true, true);
      WAITL0();
    };
#pragma unroll
    for (int j = 0; j < 7; ++j) asm volatile("" : "+v"(acc[0][j]), "+v"(acc[1][j]), "+v"(acc[2][j]), "+v"(acc[3][j]));
    body(0, true);
#pragma unroll 1
    for (int t = 0; t < 3; ++t) {
      body(1, false);
      body(2, false);
      body(0, false);
    }
    phase(1, 0, 0, true, false);
    asm volatile("s_nop 15" ::: "memory");
    __builtin_amdgcn_sched_barrier(0);
    __builtin_amdgcn_s_barrier();
    const char* sg = smem + L_ROW + 7168 + wm * 512;
    u2 ms[4];
    bf8 am[4];
#pragma unroll
    for (int i = 0; i < 4; ++i) {
      ms[i] = *(const u2*)(sg + (i * 16 + l15) * 8);
      am[i] = __builtin_bit_cast(bf8, u4{ms[i][0], ms[i][0], 0u, 0u});
    }
#pragma unroll
    for (int j = 0; j < 7; ++j) {
      const u2 c = *(const u2*)(smem + L_ROW + (g * 224 + (wn * 7 + j) * 16 + l15) * 8);
      const bf8 bm = __builtin_bit_cast(bf8, u4{c[0], c[1], 0u, 0u});
#pragma unroll
      for (int i = 0; i < 4; ++i) acc[i][j] = (KV && j < 5) ? mfma(am[i], bm, acc[i][j]) : mfma(bm, am[i], acc[i][j]);
    }
    const int rt0 = rb * 8 + wm * 4;
    typedef __attribute__((ext_vector_type(2))) short s2_t;
    auto relu2 = [&](float a, float b) {
      s2_t v = __builtin_bit_cast(s2_t, pk2(a, b));
      return __builtin_bit_cast(unsigned, __builtin_elementwise_max(v, s2_t{0, 0}));
    };
    if (KV) {
      f4 kv[8];
#pragma unroll
      for (int hp = 0; hp < 2; ++hp) {
        u4 fr[6];
#pragma unroll
        for (int ii = 0; ii < 2; ++ii) {
          const int i = hp * 2 + ii;
          const u4 s0 = *(const u4*)(sg + (i * 16 + 4 * g) * 8), s1 = *(const u4*)(sg + (i * 16 + 4 * g) * 8 + 16);
          const f4 rr = __builtin_bit_cast(f4, u4{s0[1], s0[3], s1[1], s1[3]});
#pragma unroll
          for (int j = 0; j < 5; ++j) {
            f4 y = j < 2 ? phi4(acc[i][j], rr) : acc[i][j] * rr;
            if (j == 2) {
              const f4 ph = phi4(acc[i][j], rr);
#pragma unroll
              for (int e = 0; e < 4; ++e) y[e] = l15 < 8 ? ph[e] : y[e];
            }
            fr[j][2 * ii] = pk2(y[0], y[1]);
            fr[j][2 * ii + 1] = pk2(y[2], y[3]);
          }
        }
#pragma unroll
        for (int e = 0; e < 4; ++e) fr[5][e] = l15 < 8 ? 0x3f803f80u : fr[2][e];
        const int ta[8] = {0, 0, 1, 1, 1, 2, 2, 2}, tb[8] = {5, 3, 5, 3, 4, 5, 3, 4};
#pragma unroll
        for (int m = 0; m < 8; ++m) kv[m] = mfma(__builtin_bit_cast(bf8, fr[tb[m]]), __builtin_bit_cast(bf8, fr[ta[m]]), hp ? kv[m] : zero4);
      }
#pragma unroll
      for (int m = 0; m < 8; ++m) *(f4*)(smem + (w * 8 + m) * 1024 + lane * 16) = kv[m];
      WAITL0();
      __builtin_amdgcn_s_barrier();
    }
#pragma unroll
    for (int i = 0; i < 4; ++i) {
      const float rstd = asf(ms[i][1]);
#pragma unroll
      for (int j = JQ; j < 7; j += 2) {
        const int ks = (KV ? 1024 + 64 * ci + 32 * wn : 128 * ci + 64 * wn + 16 * (j - JQ)) >> 5;
        const f4 y0 = acc[i][j] * rstd, y1 = acc[i][j + 1] * rstd;
        pst16(u4{relu2(y0[0], y0[1]), relu2(y0[2], y0[3]), relu2(y1[0], y1[1]), relu2(y1[2], y1[3])}, rs, lane * 16,
              WS_HF + (ks * 128 + rt0 + i) * 1024);
      }
      if (!KV) {
#pragma unroll
        for (int j = 0; j < 3; ++j) {
          if (j == 2 && wn) break;
          const int col = 80 * ci + 48 * wn + 16 * j;
          const f4 y = phi4(acc[i][j], f4{rstd, rstd, rstd, rstd});
          pst8(u2{pk2(y[0], y[1]), pk2(y[2], y[3])}, rs, lane * 16,
               WS_PQ + ((col >> 5) * 128 + rt0 + i) * 1024 + ((col >> 4) & 1) * 8);
        }
      }
    }
}

constexpr int CODE_LINES = 296;
__global__ __launch_bounds__(512, 2) void k1_kernel(const __bf16* __restrict__ x, const __bf16* __restrict__ wqkv,
                                                    const __bf16* __restrict__ w1, const __bf16* __restrict__ w2, char* ws) {
  extern __shared__ __attribute__((aligned(16))) char smem[];
  const int bid = blockIdx.x, xcd = bid & 7, idx = bid >> 3;
  const int tid = threadIdx.x, lane = tid & 63, w = __builtin_amdgcn_readfirstlane(tid >> 6);
  const int rb = (xcd >> 2) * 8 + (idx & 7), cgi = idx >> 3;
  const int ci = (xcd & 3) * 2 + (cgi >> 1);
  const bool kv = !(cgi & 1);
  unsigned pf0;
  {
    const char* pc;
    asm volatile("s_getpc_b64 %0" : "=s"(pc));
    asm volatile("global_load_dword %0, %1, off" : "=&v"(pf0) : "v"(pc + (tid < CODE_LINES ? tid : 0) * 128) : "memory");
  }
  const int g = lane >> 4, l15 = lane & 15, q4 = l15 >> 2, p4 = lane & 3;
  const rsrc_t rs = mkrsrc(ws, WS_END);
  const f4 zero4 = {0.f, 0.f, 0.f, 0.f};
  const unsigned tag = *(const __attribute__((address_space(4))) unsigned*)(ws + WS_EP) + 1u;
  const int jj = bid >> 3;
  if (__builtin_expect(w >= 4, 1)) {
    const int lw = w - 4;
    __builtin_amdgcn_s_setprio(3);
    const rsrc_t rx = mkrsrc(x, S_ * D_ * 2), rq = mkrsrc(wqkv, D_ * N3 * 2), r1 = mkrsrc(w1, D_ * DFF * 2);
    int vo[11];
#pragma unroll
    for (int n = 0; n < 4; ++n) {
      const int row = (n * 4 + lw) * 8 + (lane >> 3);
      vo[n] = (rb * 128 + row) * (D_ * 2) + (((lane & 7) ^ ((row >> 1) & 7)) << 4);
      if (n == 0) { for (int m = 1; m < 4; ++m) vo[m] = vo[0] + m * 32 * D_ * 2; break; }
    }
#define ROT1 3
    const int rot = ROT1 == 1 ? ((jj & 7) + 3 * (jj >> 3)) % 10 : (ROT1 == 2 ? ((jj & 1) * 5 + (jj >> 4) * 2) : (ROT1 == 3 ? ((jj & 3) * 5 / 2 + (jj >> 4)) : 0));
    auto ldA = [&](int stg, int t) {
      t += rot;
      t = t >= 10 ? t - 10 : t;
#pragma unroll
      for (int n = 0; n < 4; ++n) blds16<0>(rx, smem + stg * A_BYTES + n * 4096 + lw * 1024, vo[n], t * 128);
    };
    ldA(0, 0);
    const int nq = kv ? 20 : 12, sq = kv ? 5 : 3, qb = nq * 1024, hb = 28672 - qb;
    auto offs = [&](auto KVc) {
      constexpr bool KV = decltype(KVc)::value;
      constexpr int NQ = KV ? 20 : 12, NH = KV ? 8 : 16;
#pragma unroll
      for (int n = 0; n < 7; ++n) {
        if (n < NQ / 4) {
          const unsigned c = (n * 4 + lw) * 64 + lane, k = (c * (KV ? 3277u : 5462u)) >> 16, ch = (c - k * NQ) ^ ((k >> 2) & 2);
          vo[4 + n] = (k * N3 + 80 * ci + (KV ? (ch >= 10 ? 1200 : 640) + 8 * ch : 8 * (ch >= 10 ? ch - 2 : ch))) * 2;
        } else if (n == NQ / 4) {
          const unsigned c = lw * 64 + lane, k = c / NH;
          const int f = KV ? (((k >> 1) & 1) | ((k >> 2) & 2)) : ((k & 3) | ((k >> 1) & 4));
          vo[4 + n] = (k * DFF + (KV ? 1024 + 64 * ci : 128 * ci) + 8 * ((c - k * NH) ^ (f << 1))) * 2;
        } else vo[4 + n] = vo[4 + NQ / 4] + (n - NQ / 4) * (256 / NH) * DFF * 2;
      }
    };
    if (kv) offs(std::true_type{}); else offs(std::false_type{});
    const rsrc_t rm = mkrsrc(kv ? wqkv : w1, kv ? D_ * N3 * 2 : D_ * DFF * 2);
    auto ldB = [&](int stg, int t) {
      t += rot;
      t = t >= 10 ? t - 10 : t;
      char* dq = smem + L_Q + stg * qb + lw * 1024;
      char* dh = smem + L_Q + 3 * qb + stg * hb + lw * 1024 - sq * 4096;
      const int tq = t * (64 * N3 * 2), th = t * (64 * DFF * 2);
#pragma unroll
      for (int n = 0; n < 7; ++n) {
        if (n < 3) blds16<0>(rq, dq + n * 4096, vo[4 + n], tq);
        else if (n < 5) blds16<0>(rm, (kv ? dq : dh) + n * 4096, vo[4 + n], kv ? tq : th);
        else blds16<0>(r1, dh + n * 4096, vo[4 + n], th);
      }
    };
    ldB(0, 0);
    ldA(1, 1);
    ldB(1, 1);
    int bid2 = bid;
    asm volatile("" : "+s"(bid2));
  const int wks = bid2 / 5, wcb = bid2 - wks * 5;
  const int hf = (bid2 >> 2) & 1, qd = bid2 & 3, sel = (bid2 >> 3) >> 1;
  const bool cjob = (bid2 >> 3) < 27;
  const int cq = (bid2 >> 3) < 24 ? 2 * hf + ((bid2 >> 3) & 1) : (qd & 1) * 2 + hf;
  const int cl = (bid2 >> 3) >= 24 ? 10 * ((bid2 >> 3) - 24) + 5 * (qd >> 1) + 2
                          : (sel < 6 ? 10 * (sel >> 1) + 5 * (qd >> 1) + 3 * (qd & 1) + (sel & 1) : (sel < 10 ? 24 + 4 * qd + sel : 36 + 2 * qd + sel));
    int voG[2];
#pragma unroll
    for (int n = 0; n < 2; ++n) {
      const int c = (n * 4 + lw) * 64 + lane, qq = c / 112, rem = c - qq * 112, tl = rem >> 3, cw = tl >= 7, j = tl - 7 * cw, pr = (rem & 7) * 2;
      int col;
      if (kv) { int cc = j * 16 + pr; col = j < 5 ? 640 + 80 * ci + 40 * cw + (cc >= 40 ? 600 + cc : cc) : N3 + 1024 + 64 * ci + 32 * cw + cc - 80; }
      else col = j < 3 ? 80 * ci + 48 * cw + 16 * (cw && j == 2 ? 1 : j) + pr : N3 + 128 * ci + 64 * cw + 16 * (j - 3) + pr;
      voG[n] = c < 448 ? WS_CG + (qq * 3456 + col) * 8 : WS_SG + rb * 1024 + lane * 16;
    }
    auto side_loads = [&]() {
      const bool q = cl < 30;
      const rsrc_t rc = mkrsrc(q ? wqkv : w1, cjob ? (q ? D_ * N3 * 2 : D_ * DFF * 2) : 0);
      const int st = q ? N3 * 2 : DFF * 2;
      const int vb = (cq * 160 + lw * 8 + (lane >> 3)) * st + (q ? cl : cl - 30) * 128 + (lane & 7) * 16;
#pragma unroll
      for (int n = 0; n < 5; ++n) blds16<0>(rc, smem + L_CB + n * 4096 + lw * 1024, vb, n * 32 * st);
      const rsrc_t r2 = mkrsrc(w2, bid < 240 ? DFF * D_ * 2 : 0);
#pragma unroll
      for (int n = 0; n < 2; ++n) blds16<0>(r2, smem + L_ROW + n * 4096 + lw * 1024, ((wks * 32 + lw * 4 + g) * D_ + wcb * 128 + l15 * 8) * 2, n * 16 * D_ * 2);
    };
    asm volatile("" :: "s"(tag));
    int stg = 2;
#pragma unroll 1
    for (int t = 0; t < 10; ++t) {
      {
        if (t == 1 || t == 2) WAITV(18); else if (t < 9) WAITV(11); else WAITV(0);
        __builtin_amdgcn_s_barrier();
        __builtin_amdgcn_s_setprio(3);
        if (t == 8) {
#pragma unroll
          for (int n = 0; n < 2; ++n) blds16<16>(rs, smem + L_ROW + n * 4096 + lw * 1024, voG[n], 0);
        }
        if (t < 8) { ldA(stg, t + 2); ldB(stg, t + 2); }
        if (t == 0) side_loads();
        stg = stg == 2 ? 0 : stg + 1;
      }
      __builtin_amdgcn_s_setprio(0);
      if (t == 4) {
        const bf8 ones = __builtin_bit_cast(bf8, u4{0x3f803f80u, 0x3f803f80u, 0x3f803f80u, 0x3f803f80u});
        const char* c0 = smem + L_CB + (8 * g + q4) * 128 + lw * 32 + 8 * p4;
        f4 c = zero4;
#pragma unroll
        for (int k = 0; k < 5; ++k) c = mfma(ones, trfrag_asm<512>(c0 + k * 4096), c);
        if (g == 0 && cjob) st8(u2{__builtin_bit_cast(unsigned, c[0]), tag}, rs, l15 * 8, WS_CG + (cq * 3456 + cl * 64 + lw * 16) * 8);
      } else if (t == 5) {
        if (tid == 256) st4(tag, rs, 0, WS_FW + bid * 4);
#pragma unroll
        for (int n = 0; n < 2; ++n) {
          const char* p0 = smem + L_ROW + (4 * g + q4) * 256 + (lw * 2 + n) * 32 + 8 * p4;
          const bf8 f = trfrag_asm<4096>(p0);
          const int col = wcb * 128 + (lw * 2 + n) * 16;
          if (bid < 240) pst16(__builtin_bit_cast(u4, f), rs, lane * 16, WS_W2 + (wks * 40 + (col >> 4)) * 1024);
        }
      }
      WAITL0();
    }
    asm volatile("" :: "v"(pf0));
#pragma unroll
    for (int n = 0; n < 2; ++n) {
#pragma unroll 1
      for (unsigned spin = 0; spin < SPIN; ++spin) {
        const u4 v = *(const u4*)(smem + L_ROW + n * 4096 + lw * 1024 + lane * 16);
        if (__all(v[1] == tag && v[3] == tag)) break;
        __builtin_amdgcn_s_sleep(1);
        asm volatile("" : "+v"(voG[n]) :: "memory");
        *(u4*)(smem + L_ROW + n * 4096 + lw * 1024 + lane * 16) = ld16(rs, voG[n], 0);
      }
      u4 v = *(const u4*)(smem + L_ROW + n * 4096 + lw * 1024 + lane * 16);
#pragma unroll
      for (int e = 0; e < 4; e += 2) {
        const float c = asf(v[e]), r1 = c - bfr(c), r2 = r1 - bfr(r1);
        const unsigned nm = (v[e] & 0xffffu) ^ 0x8000u;
        const bool st = n == 1 && lw == 3;
        v[e + 1] = st ? __builtin_bit_cast(unsigned, __builtin_amdgcn_rcpf(hi16(v[e]))) : pk2(r2, 0.f);
        v[e] = st ? nm | (nm << 16) : pk2(c, r1);
      }
      *(u4*)(smem + L_ROW + n * 4096 + lw * 1024 + lane * 16) = v;
    }
    WAITL0();
    __builtin_amdgcn_s_barrier();
    if (kv) {
      __builtin_amdgcn_s_barrier();
#pragma unroll 1
      for (int it = tid - 256; it < 480; it += 256) {
      const bool isz = it >= 400;
      int hl = isz ? (it - 400) / 20 : it / 100, rem = isz ? (it - 400 - hl * 20) * 5 : it - hl * 100, d = rem / 5, grp = rem - d * 5;
      int hk = hl & 1, kc = 20 * hk + d, a = kc >> 4;
      int pos = isz ? 32 : 40 + 20 * hk + 4 * grp, b = pos >> 4;
      int m = a == 0 ? b - 2 : (a == 1 ? b : b + 3);
      const char* p = smem + ((hl >> 1) * 8 + m) * 1024 + (((pos >> 2) & 3) * 16 + (kc & 15)) * 16;
      f4 v = *(const f4*)p + *(const f4*)(p + 16384);
      if (isz) __builtin_amdgcn_raw_buffer_store_b32(__builtin_bit_cast(unsigned, v[0]), rs, (it - 400) * 4, WS_Z + (rb * 640 + ci * 80) * 4, 0);
      else pst16(__builtin_bit_cast(u4, v), rs, it * 16, WS_KV + (rb * 32 + ci * 4) * 1600);
      }
    }
    if (bid == 0 && w == 7) {
      wait16(rs, lane * 16, WS_FW, tag);
      if (lane == 0) st4(tag, rs, 0, WS_EP);
    }
  } else {
    u4 xa[2], xb[2];
    {
      const int srow = ((bid >> 2) & 1) * 1024 + (bid & 3) * 256 + jj * 8;
      const char* xr = (const char*)x + (srow + w * 2) * (D_ * 2);
      const char *p0 = xr + lane * 16, *p1 = xr + 1024 + l15 * 16;
      asm volatile("global_load_dwordx4 %0, %4, off\n\tglobal_load_dwordx4 %1, %5, off\n\tglobal_load_dwordx4 %2, %4, off offset:1280\n\t"
                   "global_load_dwordx4 %3, %5, off offset:1280"
                   : "=&v"(xa[0]), "=&v"(xb[0]), "=&v"(xa[1]), "=&v"(xb[1]) : "v"(p0), "v"(p1) : "memory");
      asm volatile("s_waitcnt vmcnt(0)" : "+v"(pf0), "+v"(xa[0]), "+v"(xb[0]), "+v"(xa[1]), "+v"(xb[1]) :: "memory");
        float su[2] = {0.f, 0.f}, sq[2] = {0.f, 0.f}, sh[2];
#pragma unroll
        for (int n = 0; n < 2; ++n) {
          sh[n] = lo16(__builtin_amdgcn_readfirstlane(xa[n][0]));
          float s2 = 0.f, q2 = 0.f;
#pragma unroll
          for (int e = 0; e < 4; ++e) {
            const float a0 = lo16(xa[n][e]) - sh[n], a1 = hi16(xa[n][e]) - sh[n], b0 = lo16(xb[n][e]) - sh[n], b1 = hi16(xb[n][e]) - sh[n];
            su[n] += a0 + a1; sq[n] += a0 * a0; sq[n] += a1 * a1; s2 += b0 + b1; q2 += b0 * b0; q2 += b1 * b1;
          }
          if (lane < 16) { su[n] += s2; sq[n] += q2; }
        }
        const float m0 = wave_sum(su[0]) * (1.f / 640.f), m1 = wave_sum(su[1]) * (1.f / 640.f);
        const float e0 = wave_sum(sq[0]) * (1.f / 640.f), e1 = wave_sum(sq[1]) * (1.f / 640.f);
        const float m = lane ? m1 : m0, var = fmaxf((lane ? e1 : e0) - m * m, 0.f);
        const float sd = __builtin_amdgcn_sqrtf(bfr(bfr(var) + 1e-5f));
        if (lane < 2) st8(u2{pk2(m + (lane ? sh[1] : sh[0]), sd), tag}, rs, lane * 8, WS_SG + (srow + w * 2) * 8);
        if (tid == 0) pst16(u4{0u, 0u, 0u, 0u}, rs, 0, WS_F2 + bid * 128);
    }
    if (kv) k1_compute<true>(ws, smem, rb, ci);
    else k1_compute<false>(ws, smem, rb, ci);
  }
}

template <int AUX>
DI bf8 ldfrag(rsrc_t r, int voff, int soff) {
  return __builtin_bit_cast(bf8, __builtin_amdgcn_raw_buffer_load_b128(r, voff, soff, AUX));
}
DI void st8_sc1(char* p, unsigned a, unsigned b) {
  __hip_atomic_store((AS1 unsigned long long*)p, (unsigned long long)a | ((unsigned long long)b << 32), __ATOMIC_RELAXED,
                     __HIP_MEMORY_SCOPE_AGENT);
}
constexpr int L2_ACC = 0;
constexpr int L2_WO = 61440;
constexpr int L2_KV = L2_WO + 5120;
constexpr int L2_Z = L2_KV + 2048;
constexpr int L2_X = L2_Z + 2048;
constexpr int K2_LDS = 98304;

#define RING 3
DI void k2_publish(char* ws, int flag, int lane) {
  WAITV(0);
  if (lane == 0) __hip_atomic_store((AS1 unsigned*)(ws + WS_F2 + flag), 1u, __ATOMIC_RELAXED, __HIP_MEMORY_SCOPE_AGENT);
}
__global__ __launch_bounds__(512, 2) void k2_kernel(const __bf16* __restrict__ x, const __bf16* __restrict__ wo,
                                                    char* ws, __bf16* __restrict__ out) {
  extern __shared__ __attribute__((aligned(16))) char smem[];
  const int tid = threadIdx.x, lane = tid & 63, w = __builtin_amdgcn_readfirstlane(tid >> 6);
  const int g = lane >> 4, l15 = lane & 15;
  const int xcd = blockIdx.x & 7, idx = blockIdx.x >> 3;
  const int r = (xcd >> 1) * 8 + (idx & 7), c = (xcd & 1) * 4 + (idx >> 3);
  unsigned pf0 = 0;
  if (w == 7 || w == 6) {
    const char* pc;
    asm volatile("s_getpc_b64 %0" : "=s"(pc));
    asm volatile("global_load_dword %0, %1, off" : "=&v"(pf0) : "v"(pc + (tid - 384 < 94 ? tid - 384 : 0) * 128) : "memory");
  }
  const rsrc_t rs = mkrsrc(ws, (unsigned)WS_END);
  const int vo = lane * 16;
  const f4 zero4 = {0.f, 0.f, 0.f, 0.f};
  if (__builtin_expect(w >= 4, 0)) {
  __builtin_amdgcn_s_setprio(3);
  if (w == 4) {
    const f4* p = (const f4*)(ws + WS_KV) + r * 100;
    const int i1 = lane < 36 ? lane + 64 : lane;
    f4 v0[16], v1[16];
#pragma unroll
    for (int i = 0; i < 16; ++i) { v0[i] = p[i * 3200 + lane]; v1[i] = p[i * 3200 + i1]; }
    u4 wv[4];
#pragma unroll
    for (int i = 0; i < 4; ++i) {
      int ch = i * 64 + lane, row = ch / 10;
      wv[i] = u4{0u, 0u, 0u, 0u};
      if (ch < 200) wv[i] = *(const u4*)(wo + (20 * r + row) * D_ + 80 * c + (ch - row * 10) * 8);
    }
#pragma unroll
    for (int st = 1; st < 16; st *= 2)
#pragma unroll
      for (int i = 0; i < 16; i += 2 * st) { v0[i] += v0[i + st]; v1[i] += v1[i + st]; }
    *(f4*)(smem + L2_KV + vo) = v0[0];
    *(f4*)(smem + L2_KV + 1024 + vo) = v1[0];
#pragma unroll
    for (int i = 0; i < 4; ++i) *(u4*)(smem + L2_WO + i * 1024 + vo) = wv[i];
    *(u4*)(smem + L2_WO + 4096 + vo) = u4{0u, 0u, 0u, 0u};
    u4 au[2];
#pragma unroll
    for (int dt = 0; dt < 2; ++dt)
#pragma unroll
      for (int hh = 0; hh < 2; ++hh) {
        int d = dt * 16 + l15, grp = 2 * g + hh;
        bool ok = d < 20 && grp < 5;
        f4 v = *(const f4*)(smem + L2_KV + (ok ? d * 5 + grp : 0) * 16);
        au[dt][2 * hh] = ok ? pk2(v[0], v[1]) : 0u;
        au[dt][2 * hh + 1] = ok ? pk2(v[2], v[3]) : 0u;
      }
    const int k0 = 20 * r + 4 * g, k1 = 20 * r + 16;
    const char* p0 = smem + L2_WO + (8 * g + (l15 >> 2)) * 160 + (lane & 3) * 8;
    char* mf = ws + WS_MF + c * 5120 + l15 * 16;
    const int o0 = (k0 >> 5) * 40960 + ((k0 >> 2) & 3) * 256 + ((k0 >> 4) & 1) * 8;
    const int o1 = (k1 >> 5) * 40960 + ((k1 >> 2) & 3) * 256 + ((k1 >> 4) & 1) * 8;
#pragma unroll
    for (int t = 0; t < 5; ++t) {
      bf8 b = trfrag(p0 + t * 32, p0 + t * 32 + 640);
      f4 m0 = mfma(__builtin_bit_cast(bf8, au[0]), b, zero4), m1 = mfma(__builtin_bit_cast(bf8, au[1]), b, zero4);
      st8_sc1(mf + t * 1024 + o0, pk2(m0[0], m0[1]), pk2(m0[2], m0[3]));
      if (g == 0) st8_sc1(mf + t * 1024 + o1, pk2(m1[0], m1[1]), pk2(m1[2], m1[3]));
    }
    k2_publish(ws, (r * 8 + c) * 128, lane);
  } else if (w == 5 || w == 6) {
    const int fo = ((c >> 1) * 5 * 128 + r * 4 + (c & 1) * 2 + w - 5) * 1024;
    const f4* zp = (const f4*)(ws + WS_Z) + (c >> 1) * 40 + (lane < 40 ? lane : 0);
    f4 zv[16];
#pragma unroll
    for (int i = 0; i < 16; ++i) zv[i] = zp[i * 160];
    u4 pq[5];
#pragma unroll
    for (int k = 0; k < 5; ++k) pq[k] = __builtin_bit_cast(u4, ldfrag<0>(rs, vo, (int)WS_PQ + fo + k * 131072));
#pragma unroll
    for (int st = 1; st < 16; st *= 2)
#pragma unroll
      for (int i = 0; i < 16; i += 2 * st) zv[i] += zv[i + st];
    char* zl = smem + L2_Z + (w - 5) * 1024;
    if (lane < 40) *(u2*)(zl + lane * 8) = u2{pk2(zv[0][0], zv[0][1]), pk2(zv[0][2], zv[0][3])};
    f4 den = zero4;
#pragma unroll
    for (int k = 0; k < 5; ++k) {
      u4 zf;
#pragma unroll
      for (int p = 0; p < 2; ++p) {
        int gi = 8 * k + 4 * p + g;
        u2 z2 = *(const u2*)(zl + gi * 8);
        bool mine = gi / 5 == l15;
        zf[2 * p] = mine ? z2[0] : 0u;
        zf[2 * p + 1] = mine ? z2[1] : 0u;
      }
      den = mfma(__builtin_bit_cast(bf8, zf), __builtin_bit_cast(bf8, pq[k]), den);
    }
    f4 rd;
#pragma unroll
    for (int e = 0; e < 4; ++e) rd[e] = __builtin_amdgcn_rcpf(bfr(bfr(den[e]) + 1e-6f));
    if (g < 2) *(f4*)(zl + 512 + l15 * 32 + g * 16) = rd;
#pragma unroll
    for (int k = 0; k < 5; ++k) {
      u4 o;
#pragma unroll
      for (int p = 0; p < 2; ++p) {
        float s = *(const float*)(zl + 512 + l15 * 32 + ((8 * k + 4 * p + g) / 5) * 4);
        o[2 * p] = pk2(lo16(pq[k][2 * p]) * s, hi16(pq[k][2 * p]) * s);
        o[2 * p + 1] = pk2(lo16(pq[k][2 * p + 1]) * s, hi16(pq[k][2 * p + 1]) * s);
      }
      st16(o, rs, vo, WS_AF + fo + k * 131072);
    }
    k2_publish(ws, (r * 8 + c) * 128 + (w - 4) * 4, lane);
  }
    if (w == 7) {
      const rsrc_t rx = mkrsrc(x, S_ * D_ * 2);
#pragma unroll 1
      for (int i = 0; i < 10; ++i) {
        const int ch = i * 64 + lane, row = ch / 10;
        blds16<0>(rx, smem + L2_X + i * 1024, ((r * 64 + row) * D_ + c * 80 + (ch - row * 10) * 8) * 2, 0);
      }
    }
    asm volatile("s_waitcnt vmcnt(0)" : "+v"(pf0) :: "memory");
    __builtin_amdgcn_s_barrier();
    return;
  }
  const int hA = (int)WS_HF + (w * 128 + r * 4) * 1024, hB = (int)WS_W2 + (w * 8 + c) * 5120;
  const int aA = (int)WS_AF + (w * 128 + r * 4) * 1024, aB = (int)WS_MF + (w * 8 + c) * 5120;
#define ROT 0
  const int rot = ROT ? ((idx >> 3) * 3 + (idx & 7) * 5) % 12 : 0, rot2 = ROT ? idx % 5 : 0;
  bf8 fa[RING][4], fb[RING][5];
  auto ld = [&](int i) {
    const int sl = i % RING;
    if (i < 12) {
      int ii = i + rot;
      ii = ii >= 12 ? ii - 12 : ii;
#pragma unroll
      for (int j = 0; j < 5; ++j) fb[sl][j] = ldfrag<LAUX>(rs, vo + j * 1024, hB + ii * 163840);
#pragma unroll
      for (int j = 0; j < 4; ++j) fa[sl][j] = ldfrag<LAUX>(rs, vo + j * 1024, hA + ii * 524288);
    } else {
      int ii = i - 12 + rot2;
      ii = ii >= 5 ? ii - 5 : ii;
#pragma unroll
      for (int j = 0; j < 5; ++j) fb[sl][j] = ldfrag<16>(rs, vo + j * 1024, aB + ii * 163840);
#pragma unroll
      for (int j = 0; j < 4; ++j) fa[sl][j] = ldfrag<16>(rs, vo + j * 1024, aA + ii * 524288);
    }
  };
#pragma unroll
  for (int i = 0; i < RING; ++i) ld(i);
  const int orow = (r * 64 + w * 16 + l15) * D_ + c * 80 + 4 * g;
  const AS1 unsigned* fl = (const AS1 unsigned*)(ws + WS_F2 + (lane < 16 ? (r * 8 + (lane >> 1)) * 128 + 4 + (lane & 1) * 4 : (((lane - 16) & 31) * 8 + c) * 128));
  __builtin_amdgcn_sched_barrier(0);
  f4 acc[4][5];
#pragma unroll
  for (int i = 0; i < 4; ++i)
#pragma unroll
    for (int j = 0; j < 5; ++j) acc[i][j] = zero4;
  unsigned flv = 0;
#pragma unroll
  for (int i = 0; i < 17; ++i) {
    const int sl = i % RING;
#pragma unroll
    for (int j = 0; j < 5; ++j)
#pragma unroll
      for (int m = 0; m < 4; ++m) acc[m][j] = mfma(fb[sl][j], fa[sl][m], acc[m][j]);
    if (i + RING == 11) flv = __hip_atomic_load(fl, __ATOMIC_RELAXED, __HIP_MEMORY_SCOPE_AGENT);
    if (i + RING == 12) {
      for (unsigned spin = 0; !__all(flv == 1u) && spin < (1u << 22); ++spin) {
        __builtin_amdgcn_s_sleep(1);
        flv = __hip_atomic_load(fl, __ATOMIC_RELAXED, __HIP_MEMORY_SCOPE_AGENT);
      }
      __builtin_amdgcn_fence(__ATOMIC_ACQUIRE, "wavefront");
    }
    if (i + RING < 17) ld(i + RING);

    __builtin_amdgcn_sched_barrier(0);
  }
#pragma unroll
  for (int m = 0; m < 4; ++m) {
    if (m != w) {
      int slot = m < w ? m : m - 1;
#pragma unroll
      for (int j = 0; j < 5; ++j) *(f4*)(smem + L2_ACC + ((w * 3 + slot) * 5 + j) * 1024 + vo) = acc[m][j];
    }
  }
  WAITL0();
  __builtin_amdgcn_s_barrier();
  f4 own[5];
#pragma unroll
  for (int j = 0; j < 5; ++j) own[j] = w == 0 ? acc[0][j] : (w == 1 ? acc[1][j] : (w == 2 ? acc[2][j] : acc[3][j]));
  f4 tot[5];
  u2 xr[5];
#pragma unroll
  for (int j = 0; j < 5; ++j) xr[j] = *(const u2*)(smem + L2_X + (w * 16 + l15) * 160 + j * 32 + g * 8);
#pragma unroll
  for (int j = 0; j < 5; ++j) tot[j] = f4{lo16(xr[j][0]), hi16(xr[j][0]), lo16(xr[j][1]), hi16(xr[j][1])};
#pragma unroll
  for (int s = 0; s < 4; ++s) {
    int slot = w < s ? w : w - 1;
#pragma unroll
    for (int j = 0; j < 5; ++j) {
      f4 v = own[j];
      if (s != w) v = *(const f4*)(smem + L2_ACC + ((s * 3 + slot) * 5 + j) * 1024 + vo);
      tot[j] += v;
    }
  }
#pragma unroll
  for (int j = 0; j < 5; ++j) *(u2*)(out + orow + 16 * j) = u2{pk2(tot[j][0], tot[j][1]), pk2(tot[j][2], tot[j][3])};
}

extern "C" void kernel_launch(void* const* d_in, const int* in_sizes, int n_in, void* d_out, int out_size, void* d_ws,
                              size_t ws_size, hipStream_t stream) {
  const __bf16* x = (const __bf16*)d_in[0];
  const __bf16* wqkv = (const __bf16*)d_in[1];
  const __bf16* wo = (const __bf16*)d_in[2];
  const __bf16* w1 = (const __bf16*)d_in[4];
  const __bf16* w2 = (const __bf16*)d_in[5];
  char* ws = (char*)d_ws;
  (void)hipFuncSetAttribute((const void*)k1_kernel, hipFuncAttributeMaxDynamicSharedMemorySize, K1_LDS);
  (void)hipFuncSetAttribute((const void*)k2_kernel, hipFuncAttributeMaxDynamicSharedMemorySize, K2_LDS);
  k1_kernel<<<256, 512, K1_LDS, stream>>>(x, wqkv, w1, w2, ws);
  k2_kernel<<<256, 512, K2_LDS, stream>>>(x, wo, ws, (__bf16*)d_out);
}
```

```cpp
#include <hip/hip_runtime.h>
#include <hip/hip_bf16.h>
#include <stdint.h>
#include <type_traits>

typedef __attribute__((ext_vector_type(8))) __bf16 bf8;
typedef __attribute__((ext_vector_type(4))) __bf16 bf4;
typedef __attribute__((ext_vector_type(2))) __bf16 bf2;
typedef __attribute__((ext_vector_type(4))) short s4;
typedef __attribute__((ext_vector_type(4))) float f4;
typedef __attribute__((ext_vector_type(2))) float f2;
typedef __attribute__((ext_vector_type(4))) unsigned u4;
typedef __attribute__((ext_vector_type(2))) unsigned u2;
#define AS1 __attribute__((address_space(1)))
#define AS3 __attribute__((address_space(3)))
#define DI __device__ __forceinline__

constexpr int S_ = 2048, D_ = 640, DFF = 1536, N3 = 1920;
constexpr int WS_HF = 0;
constexpr int WS_PQ = WS_HF + 128 * 48 * 1024;
constexpr int WS_AF = WS_PQ + 128 * 20 * 1024;
constexpr int WS_W2 = WS_AF + 128 * 20 * 1024;
constexpr int WS_MF = WS_W2 + 8 * 48 * 5 * 1024;
constexpr int WS_KV = WS_MF + 8 * 20 * 5 * 1024;
constexpr int WS_Z = WS_KV + 16 * 32 * 400 * 4;
constexpr int WS_CG = WS_Z + 16 * 640 * 4;
constexpr int WS_SG = WS_CG + 4 * 3456 * 8;
constexpr int WS_FW = WS_SG + 2048 * 8;
constexpr int WS_F1 = WS_FW + 1024;
constexpr int WS_F2 = WS_F1 + 1024;
constexpr int WS_EP = WS_F2 + 256 * 128;
constexpr int WS_END = WS_EP + 128;

DI float bfr(float v) { return (float)(__bf16)v; }
DI unsigned pk2(float a, float b) { f2 v = {a, b}; return __builtin_bit_cast(unsigned, __builtin_convertvector(v, bf2)); }
DI float asf(unsigned u) { return __builtin_bit_cast(float, u); }
DI float lo16(unsigned u) { return __builtin_bit_cast(float, u << 16); }
DI float hi16(unsigned u) { return __builtin_bit_cast(float, u & 0xffff0000u); }
DI f4 mfma(bf8 a, bf8 b, f4 c) { return __builtin_amdgcn_mfma_f32_16x16x32_bf16(a, b, c, 0, 0, 0); }
DI void glds16(const void* g, char* l) {
  __builtin_amdgcn_global_load_lds((const AS1 void*)g, (AS3 void*)l, 16, 0, 0);
}
DI bf8 trfrag(const char* p0, const char* p1) {
  s4 a = __builtin_amdgcn_ds_read_tr16_b64_v4i16((AS3 s4*)p0);
  s4 b = __builtin_amdgcn_ds_read_tr16_b64_v4i16((AS3 s4*)p1);
  return __builtin_bit_cast(bf8, __builtin_shufflevector(a, b, 0, 1, 2, 3, 4, 5, 6, 7));
}
DI bf8 trfrag3(const AS3 char* p0, const AS3 char* p1) {
  s4 a = __builtin_amdgcn_ds_read_tr16_b64_v4i16((AS3 s4*)p0);
  s4 b = __builtin_amdgcn_ds_read_tr16_b64_v4i16((AS3 s4*)p1);
  return __builtin_bit_cast(bf8, __builtin_shufflevector(a, b, 0, 1, 2, 3, 4, 5, 6, 7));
}
template <int OFF>
DI bf8 trfrag_asm(const char* p) {
  u2 a, b;
  asm volatile("ds_read_b64_tr_b16 %0, %2\n\tds_read_b64_tr_b16 %1, %2 offset:%3\n\ts_waitcnt lgkmcnt(0)"
               : "=&v"(a), "=&v"(b) : "v"((unsigned)(size_t)(const AS3 char*)p), "i"(OFF) : "memory");
  return __builtin_bit_cast(bf8, u4{a[0], a[1], b[0], b[1]});
}
DI f4 phi4(f4 a, f4 r) {
  const f4 t = __builtin_elementwise_fma(a, r, f4{1e-6f, 1e-6f, 1e-6f, 1e-6f}), u = a * (r * 1.44269504088896341f);
  f4 m, e;
#pragma unroll
  for (int i = 0; i < 4; ++i) {
    m[i] = __builtin_amdgcn_fmed3f(t[i], 1e-6f, __builtin_inff());
    e[i] = __builtin_amdgcn_fmed3f(__builtin_amdgcn_exp2f(u[i]), 0.f, 1.f);
  }
  return m + e;
}
#define WAITV(n) asm volatile("s_waitcnt vmcnt(" #n ")" ::: "memory")
#define WAITL0() asm volatile("s_waitcnt lgkmcnt(0)" ::: "memory")

#define PAUX 16
#define LAUX 0
typedef __amdgpu_buffer_rsrc_t rsrc_t;
DI rsrc_t mkrsrc(const void* p, unsigned bytes) { return __builtin_amdgcn_make_buffer_rsrc((void*)p, 0, bytes, 0x00020000); }
template <int AUX>
DI void blds16(rsrc_t r, char* l, int voff, int soff) {
  __builtin_amdgcn_raw_ptr_buffer_load_lds(r, (AS3 void*)l, 16, voff, soff, 0, AUX);
}
DI u4 ld16(rsrc_t r, int voff, int soff) { return __builtin_amdgcn_raw_buffer_load_b128(r, voff, soff, 16); }
DI void st16(u4 v, rsrc_t r, int voff, int soff) { __builtin_amdgcn_raw_buffer_store_b128(v, r, voff + soff, 0, 16); }
DI void pst16(u4 v, rsrc_t r, int voff, int soff) { __builtin_amdgcn_raw_buffer_store_b128(v, r, voff + soff, 0, PAUX); }
DI void pst8(u2 v, rsrc_t r, int voff, int soff) { __builtin_amdgcn_raw_buffer_store_b64(v, r, voff, soff, PAUX); }
DI void st8(u2 v, rsrc_t r, int voff, int soff) { __builtin_amdgcn_raw_buffer_store_b64(v, r, voff, soff, 16); }
DI void st4(unsigned v, rsrc_t r, int voff, int soff) { __builtin_amdgcn_raw_buffer_store_b32(v, r, voff, soff, 16); }
DI unsigned ld4(rsrc_t r, int voff, int soff) { return __builtin_amdgcn_raw_buffer_load_b32(r, voff, soff, 16); }
DI float wave_sum(float v) {
#define DPP_ADD(c) v += __builtin_bit_cast(float, __builtin_amdgcn_update_dpp(0, __builtin_bit_cast(int, v), c, 0xf, 0xf, true))
  DPP_ADD(0x111); DPP_ADD(0x112); DPP_ADD(0x114); DPP_ADD(0x118);
#undef DPP_ADD
  const int u = __builtin_bit_cast(int, v);
  return (__builtin_bit_cast(float, __builtin_amdgcn_readlane(u, 15)) + __builtin_bit_cast(float, __builtin_amdgcn_readlane(u, 31))) +
         (__builtin_bit_cast(float, __builtin_amdgcn_readlane(u, 47)) + __builtin_bit_cast(float, __builtin_amdgcn_readlane(u, 63)));
}
constexpr unsigned SPIN = 1u << 20;
#define PSLEEP 8
DI void wait16(rsrc_t rs, int voff, int soff, unsigned tag) {
#pragma unroll 1
  for (unsigned spin = 0; spin < SPIN; ++spin) {
    asm volatile("" : "+v"(voff) :: "memory");
    u4 v = ld16(rs, voff, soff);
    if (__all(v[0] == tag && v[1] == tag && v[2] == tag && v[3] == tag)) break;
    __builtin_amdgcn_s_sleep(PSLEEP);
  }
  asm volatile("" ::: "memory");
  __builtin_amdgcn_fence(__ATOMIC_ACQUIRE, "wavefront");
}
DI void wait4(rsrc_t rs, int voff, int soff, unsigned tag) {
#pragma unroll 1
  for (unsigned spin = 0; spin < SPIN; ++spin) {
    asm volatile("" : "+v"(voff) :: "memory");
    if (__all(ld4(rs, voff, soff) == tag)) break;
    __builtin_amdgcn_s_sleep(PSLEEP);
  }
  asm volatile("" ::: "memory");
  __builtin_amdgcn_fence(__ATOMIC_ACQUIRE, "wavefront");
}

DI void wait_lds(char* p, unsigned lvl) {
#pragma unroll 1
  for (unsigned spin = 0; spin < SPIN; ++spin) {
    if (__hip_atomic_load((AS3 unsigned*)p, __ATOMIC_RELAXED, __HIP_MEMORY_SCOPE_WORKGROUP) >= lvl) break;
    __builtin_amdgcn_s_sleep(1);
  }
  asm volatile("" ::: "memory");
  __builtin_amdgcn_fence(__ATOMIC_ACQUIRE, "wavefront");
}
DI void set_lds(char* p, unsigned lvl) { __hip_atomic_store((AS3 unsigned*)p, lvl, __ATOMIC_RELAXED, __HIP_MEMORY_SCOPE_WORKGROUP); }

constexpr int A_BYTES = 128 * 128;
constexpr int L_Q = 3 * A_BYTES;
constexpr int L_CB = L_Q + 3 * 64 * 448;
constexpr int L_ROW = L_CB + 20480;
constexpr int K1_LDS = L_ROW + 8192;

template <bool KV>
DI void k1_compute(char* ws, char* smem, int rb, int ci) {
  constexpr int NQ = KV ? 20 : 12, NH = KV ? 8 : 16;
  constexpr int QB = 64 * NQ * 16, HB = 64 * NH * 16;
  constexpr int L_H = L_Q + 3 * QB;
  constexpr int JQ = KV ? 5 : 3;
  const int lane = threadIdx.x & 63;
  const int w = __builtin_amdgcn_readfirstlane(threadIdx.x >> 6), wm = (w >> 1) & 1, wn = w & 1;
  const int g = lane >> 4, l15 = lane & 15, q4 = l15 >> 2, p4 = lane & 3;
  const rsrc_t rs = mkrsrc(ws, WS_END);
  const f4 zero4 = {0.f, 0.f, 0.f, 0.f};
  f4 acc[4][7];
    unsigned ra[2], rb_[7];
    {
      const unsigned lb = (unsigned)(size_t)(AS3 char*)smem;
      const int f = KV ? (((q4 >> 1) & 1) | ((g & 1) << 1)) : (q4 | ((g & 1) << 2));
#pragma unroll
      for (int s = 0; s < 2; ++s) ra[s] = lb + (wm * 64 + l15) * 128 + (((s * 4 + g) ^ (l15 >> 1)) << 4);
#pragma unroll
      for (int j = 0; j < 7; ++j) {
        const int cc = 2 * j + (p4 >> 1);
        const int pos = KV ? wn * 5 + cc + (cc >= 5 ? 5 : 0) : wn * 6 + cc;
        rb_[j] = lb + (j < JQ ? L_Q + (8 * g + q4) * (NQ * 16) + ((pos ^ ((g & 1) << 1)) << 4) + 8 * (p4 & 1)
                              : L_H + (8 * g + q4) * (NH * 16) + (((wn * (7 - JQ) + j - JQ) ^ f) << 5) + 8 * p4);
      }
    }
#pragma unroll
    for (int i = 0; i < 4; ++i)
#pragma unroll
      for (int j = 0; j < 7; ++j) acc[i][j] = zero4;
    bf8 fa[2][4], fb[2][7];
    s4 hb[2][7][2];
    auto rd1 = [&](int s, int stg, int n) {
      if (n < 4) fa[s][n] = *(const AS3 bf8*)(size_t)(ra[s] + stg * A_BYTES + n * 2048);
      else {
        const int j = (n - 4) >> 1, h = (n - 4) & 1;
        const AS3 char* p = (const AS3 char*)(size_t)rb_[j] + (j < JQ ? stg * QB + (s * 32 + 4 * h) * NQ * 16 : stg * HB + (s * 32 + 4 * h) * NH * 16);
        hb[s][j][h] = __builtin_amdgcn_ds_read_tr16_b64_v4i16((AS3 s4*)p);
        if (h) fb[s][j] = __builtin_bit_cast(bf8, __builtin_shufflevector(hb[s][j][0], hb[s][j][1], 0, 1, 2, 3, 4, 5, 6, 7));
      }
    };
    auto mm1 = [&](int s, int n) {
      const int j = n >> 2, i = n & 3;
      if (KV && j < 5) asm volatile("v_mfma_f32_16x16x32_bf16 %0, %1, %2, %0" : "+v"(acc[i][j]) : "v"(fa[s][i]), "v"(fb[s][j]));
      else asm volatile("v_mfma_f32_16x16x32_bf16 %0, %1, %2, %0" : "+v"(acc[i][j]) : "v"(fb[s][j]), "v"(fa[s][i]));
    };
    auto phase = [&](int sm, int sr, int stg, bool domm, bool dord) {
#pragma unroll
      for (int n = 0; n < 28; ++n) {
        if (domm) mm1(sm, n);
        if (dord && n < 18) rd1(sr, stg, n);
        __builtin_amdgcn_sched_barrier(0);
      }
      if (domm)
        asm volatile("" :: "v"(fa[sm][0]), "v"(fa[sm][1]), "v"(fa[sm][2]), "v"(fa[sm][3]), "v"(fb[sm][0]), "v"(fb[sm][1]), "v"(fb[sm][2]),
                     "v"(fb[sm][3]), "v"(fb[sm][4]), "v"(fb[sm][5]), "v"(fb[sm][6]));
    };
    auto body = [&](int stg, bool first) {
      __builtin_amdgcn_s_barrier();
      __builtin_amdgcn_sched_barrier(0);
      phase(1, 0, stg, !first, true);
      phase(0, 1, stg, true, true);
      WAITL0();
    };
#pragma unroll
    for (int j = 0; j < 7; ++j) asm volatile("" : "+v"(acc[0][j]), "+v"(acc[1][j]), "+v"(acc[2][j]), "+v"(acc[3][j]));
    body(0, true);
#pragma unroll 1
    for (int t = 0; t < 3; ++t) {
      body(1, false);
      body(2, false);
      body(0, false);
    }
    phase(1, 0, 0, true, false);
    asm volatile("s_nop 15" ::: "memory");
    __builtin_amdgcn_sched_barrier(0);
    __builtin_amdgcn_s_barrier();
    const char* sg = smem + L_ROW + 7168 + wm * 512;
    u2 ms[4];
    bf8 am[4];
#pragma unroll
    for (int i = 0; i < 4; ++i) {
      ms[i] = *(const u2*)(sg + (i * 16 + l15) * 8);
      am[i] = __builtin_bit_cast(bf8, u4{ms[i][0], ms[i][0], 0u, 0u});
    }
#pragma unroll
    for (int j = 0; j < 7; ++j) {
      const u2 c = *(const u2*)(smem + L_ROW + (g * 224 + (wn * 7 + j) * 16 + l15) * 8);
      const bf8 bm = __builtin_bit_cast(bf8, u4{c[0], c[1], 0u, 0u});
#pragma unroll
      for (int i = 0; i < 4; ++i) acc[i][j] = (KV && j < 5) ? mfma(am[i], bm, acc[i][j]) : mfma(bm, am[i], acc[i][j]);
    }
    const int rt0 = rb * 8 + wm * 4;
    typedef __attribute__((ext_vector_type(2))) short s2_t;
    auto relu2 = [&](float a, float b) {
      s2_t v = __builtin_bit_cast(s2_t, pk2(a, b));
      return __builtin_bit_cast(unsigned, __builtin_elementwise_max(v, s2_t{0, 0}));
    };
    if (KV) {
      f4 kv[8];
#pragma unroll
      for (int hp = 0; hp < 2; ++hp) {
        u4 fr[6];
#pragma unroll
        for (int ii = 0; ii < 2; ++ii) {
          const int i = hp * 2 + ii;
          const u4 s0 = *(const u4*)(sg + (i * 16 + 4 * g) * 8), s1 = *(const u4*)(sg + (i * 16 + 4 * g) * 8 + 16);
          const f4 rr = __builtin_bit_cast(f4, u4{s0[1], s0[3], s1[1], s1[3]});
#pragma unroll
          for (int j = 0; j < 5; ++j) {
            f4 y = j < 2 ? phi4(acc[i][j], rr) : acc[i][j] * rr;
            if (j == 2) {
              const f4 ph = phi4(acc[i][j], rr);
#pragma unroll
              for (int e = 0; e < 4; ++e) y[e] = l15 < 8 ? ph[e] : y[e];
            }
            fr[j][2 * ii] = pk2(y[0], y[1]);
            fr[j][2 * ii + 1] = pk2(y[2], y[3]);
          }
        }
#pragma unroll
        for (int e = 0; e < 4; ++e) fr[5][e] = l15 < 8 ? 0x3f803f80u : fr[2][e];
        const int ta[8] = {0, 0, 1, 1, 1, 2, 2, 2}, tb[8] = {5, 3, 5, 3, 4, 5, 3, 4};
#pragma unroll
        for (int m = 0; m < 8; ++m) kv[m] = mfma(__builtin_bit_cast(bf8, fr[tb[m]]), __builtin_bit_cast(bf8, fr[ta[m]]), hp ? kv[m] : zero4);
      }
#pragma unroll
      for (int m = 0; m < 8; ++m) *(f4*)(smem + (w * 8 + m) * 1024 + lane * 16) = kv[m];
      WAITL0();
      __builtin_amdgcn_s_barrier();
    }
#pragma unroll
    for (int i = 0; i < 4; ++i) {
      const float rstd = asf(ms[i][1]);
#pragma unroll
      for (int j = JQ; j < 7; j += 2) {
        const int ks = (KV ? 1024 + 64 * ci + 32 * wn : 128 * ci + 64 * wn + 16 * (j - JQ)) >> 5;
        const f4 y0 = acc[i][j] * rstd, y1 = acc[i][j + 1] * rstd;
        pst16(u4{relu2(y0[0], y0[1]), relu2(y0[2], y0[3]), relu2(y1[0], y1[1]), relu2(y1[2], y1[3])}, rs, lane * 16,
              WS_HF + (ks * 128 + rt0 + i) * 1024);
      }
      if (!KV) {
#pragma unroll
        for (int j = 0; j < 3; ++j) {
          if (j == 2 && wn) break;
          const int col = 80 * ci + 48 * wn + 16 * j;
          const f4 y = phi4(acc[i][j], f4{rstd, rstd, rstd, rstd});
          pst8(u2{pk2(y[0], y[1]), pk2(y[2], y[3])}, rs, lane * 16,
               WS_PQ + ((col >> 5) * 128 + rt0 + i) * 1024 + ((col >> 4) & 1) * 8);
        }
      }
    }
}

constexpr int CODE_LINES = 296;
__global__ __launch_bounds__(512, 2) void k1_kernel(const __bf16* __restrict__ x, const __bf16* __restrict__ wqkv,
                                                    const __bf16* __restrict__ w1, const __bf16* __restrict__ w2, char* ws,
                                                    const __bf16* __restrict__ wo) {
  extern __shared__ __attribute__((aligned(16))) char smem[];
  const int bid = blockIdx.x, xcd = bid & 7, idx = bid >> 3;
  const int tid = threadIdx.x, lane = tid & 63, w = __builtin_amdgcn_readfirstlane(tid >> 6);
  const int rb = (xcd >> 2) * 8 + (idx & 7), cgi = idx >> 3;
  const int ci = (xcd & 3) * 2 + (cgi >> 1);
  const bool kv = !(cgi & 1);
  unsigned pf0;
  {
    const char* pc;
    asm volatile("s_getpc_b64 %0" : "=s"(pc));
    asm volatile("global_load_dword %0, %1, off" : "=&v"(pf0) : "v"(tid < CODE_LINES ? pc + tid * 128 : (const char*)wo + (bid * 25 + (tid - CODE_LINES < 25 ? tid - CODE_LINES : 0)) * 128) : "memory");
  }
  const int g = lane >> 4, l15 = lane & 15, q4 = l15 >> 2, p4 = lane & 3;
  const rsrc_t rs = mkrsrc(ws, WS_END);
  const f4 zero4 = {0.f, 0.f, 0.f, 0.f};
  const unsigned tag = *(const __attribute__((address_space(4))) unsigned*)(ws + WS_EP) + 1u;
  const int jj = bid >> 3;
  if (__builtin_expect(w >= 4, 1)) {
    const int lw = w - 4;
    __builtin_amdgcn_s_setprio(3);
    const rsrc_t rx = mkrsrc(x, S_ * D_ * 2), rq = mkrsrc(wqkv, D_ * N3 * 2), r1 = mkrsrc(w1, D_ * DFF * 2);
    int vo[11];
#pragma unroll
    for (int n = 0; n < 4; ++n) {
      const int row = (n * 4 + lw) * 8 + (lane >> 3);
      vo[n] = (rb * 128 + row) * (D_ * 2) + (((lane & 7) ^ ((row >> 1) & 7)) << 4);
      if (n == 0) { for (int m = 1; m < 4; ++m) vo[m] = vo[0] + m * 32 * D_ * 2; break; }
    }
#define ROT1 3
    const int rot = ROT1 == 1 ? ((jj & 7) + 3 * (jj >> 3)) % 10 : (ROT1 == 2 ? ((jj & 1) * 5 + (jj >> 4) * 2) : (ROT1 == 3 ? ((jj & 3) * 5 / 2 + (jj >> 4)) : 0));
    auto ldA = [&](int stg, int t) {
      t += rot;
      t = t >= 10 ? t - 10 : t;
#pragma unroll
      for (int n = 0; n < 4; ++n) blds16<0>(rx, smem + stg * A_BYTES + n * 4096 + lw * 1024, vo[n], t * 128);
    };
    ldA(0, 0);
    const int nq = kv ? 20 : 12, sq = kv ? 5 : 3, qb = nq * 1024, hb = 28672 - qb;
    auto offs = [&](auto KVc) {
      constexpr bool KV = decltype(KVc)::value;
      constexpr int NQ = KV ? 20 : 12, NH = KV ? 8 : 16;
#pragma unroll
      for (int n = 0; n < 7; ++n) {
        if (n < NQ / 4) {
          const unsigned c = (n * 4 + lw) * 64 + lane, k = (c * (KV ? 3277u : 5462u)) >> 16, ch = (c - k * NQ) ^ ((k >> 2) & 2);
          vo[4 + n] = (k * N3 + 80 * ci + (KV ? (ch >= 10 ? 1200 : 640) + 8 * ch : 8 * (ch >= 10 ? ch - 2 : ch))) * 2;
        } else if (n == NQ / 4) {
          const unsigned c = lw * 64 + lane, k = c / NH;
          const int f = KV ? (((k >> 1) & 1) | ((k >> 2) & 2)) : ((k & 3) | ((k >> 1) & 4));
          vo[4 + n] = (k * DFF + (KV ? 1024 + 64 * ci : 128 * ci) + 8 * ((c - k * NH) ^ (f << 1))) * 2;
        } else vo[4 + n] = vo[4 + NQ / 4] + (n - NQ / 4) * (256 / NH) * DFF * 2;
      }
    };
    if (kv) offs(std::true_type{}); else offs(std::false_type{});
    const rsrc_t rm = mkrsrc(kv ? wqkv : w1, kv ? D_ * N3 * 2 : D_ * DFF * 2);
    auto ldB = [&](int stg, int t) {
      t += rot;
      t = t >= 10 ? t - 10 : t;
      char* dq = smem + L_Q + stg * qb + lw * 1024;
      char* dh = smem + L_Q + 3 * qb + stg * hb + lw * 1024 - sq * 4096;
      const int tq = t * (64 * N3 * 2), th = t * (64 * DFF * 2);
#pragma unroll
      for (int n = 0; n < 7; ++n) {
        if (n < 3) blds16<0>(rq, dq + n * 4096, vo[4 + n], tq);
        else if (n < 5) blds16<0>(rm, (kv ? dq : dh) + n * 4096, vo[4 + n], kv ? tq : th);
        else blds16<0>(r1, dh + n * 4096, vo[4 + n], th);
      }
    };
    ldB(0, 0);
    ldA(1, 1);
    ldB(1, 1);
    int bid2 = bid;
    asm volatile("" : "+s"(bid2));
  const int wks = bid2 / 5, wcb = bid2 - wks * 5;
  const int hf = (bid2 >> 2) & 1, qd = bid2 & 3, sel = (bid2 >> 3) >> 1;
  const bool cjob = (bid2 >> 3) < 27;
  const int cq = (bid2 >> 3) < 24 ? 2 * hf + ((bid2 >> 3) & 1) : (qd & 1) * 2 + hf;
  const int cl = (bid2 >> 3) >= 24 ? 10 * ((bid2 >> 3) - 24) + 5 * (qd >> 1) + 2
                          : (sel < 6 ? 10 * (sel >> 1) + 5 * (qd >> 1) + 3 * (qd & 1) + (sel & 1) : (sel < 10 ? 24 + 4 * qd + sel : 36 + 2 * qd + sel));
    int voG[2];
#pragma unroll
    for (int n = 0; n < 2; ++n) {
      const int c = (n * 4 + lw) * 64 + lane, qq = c / 112, rem = c - qq * 112, tl = rem >> 3, cw = tl >= 7, j = tl - 7 * cw, pr = (rem & 7) * 2;
      int col;
      if (kv) { int cc = j * 16 + pr; col = j < 5 ? 640 + 80 * ci + 40 * cw + (cc >= 40 ? 600 + cc : cc) : N3 + 1024 + 64 * ci + 32 * cw + cc - 80; }
      else col = j < 3 ? 80 * ci + 48 * cw + 16 * (cw && j == 2 ? 1 : j) + pr : N3 + 128 * ci + 64 * cw + 16 * (j - 3) + pr;
      voG[n] = c < 448 ? WS_CG + (qq * 3456 + col) * 8 : WS_SG + rb * 1024 + lane * 16;
    }
    auto side_loads = [&]() {
      const bool q = cl < 30;
      const rsrc_t rc = mkrsrc(q ? wqkv : w1, cjob ? (q ? D_ * N3 * 2 : D_ * DFF * 2) : 0);
      const int st = q ? N3 * 2 : DFF * 2;
      const int vb = (cq * 160 + lw * 8 + (lane >> 3)) * st + (q ? cl : cl - 30) * 128 + (lane & 7) * 16;
#pragma unroll
      for (int n = 0; n < 5; ++n) blds16<0>(rc, smem + L_CB + n * 4096 + lw * 1024, vb, n * 32 * st);
      const rsrc_t r2 = mkrsrc(w2, bid < 240 ? DFF * D_ * 2 : 0);
#pragma unroll
      for (int n = 0; n < 2; ++n) blds16<0>(r2, smem + L_ROW + n * 4096 + lw * 1024, ((wks * 32 + lw * 4 + g) * D_ + wcb * 128 + l15 * 8) * 2, n * 16 * D_ * 2);
    };
    asm volatile("" :: "s"(tag));
    int stg = 2;
#pragma unroll 1
    for (int t = 0; t < 10; ++t) {
      {
        if (t == 1 || t == 2) WAITV(18); else if (t < 9) WAITV(11); else WAITV(0);
        __builtin_amdgcn_s_barrier();
        __builtin_amdgcn_s_setprio(3);
        if (t == 8) {
#pragma unroll
          for (int n = 0; n < 2; ++n) blds16<16>(rs, smem + L_ROW + n * 4096 + lw * 1024, voG[n], 0);
        }
        if (t < 8) { ldA(stg, t + 2); ldB(stg, t + 2); }
        if (t == 0) side_loads();
        stg = stg == 2 ? 0 : stg + 1;
      }
      __builtin_amdgcn_s_setprio(0);
      if (t == 4) {
        const bf8 ones = __builtin_bit_cast(bf8, u4{0x3f803f80u, 0x3f803f80u, 0x3f803f80u, 0x3f803f80u});
        const char* c0 = smem + L_CB + (8 * g + q4) * 128 + lw * 32 + 8 * p4;
        f4 c = zero4;
#pragma unroll
        for (int k = 0; k < 5; ++k) c = mfma(ones, trfrag_asm<512>(c0 + k * 4096), c);
        if (g == 0 && cjob) st8(u2{__builtin_bit_cast(unsigned, c[0]), tag}, rs, l15 * 8, WS_CG + (cq * 3456 + cl * 64 + lw * 16) * 8);
      } else if (t == 5) {
        if (tid == 256) st4(tag, rs, 0, WS_FW + bid * 4);
#pragma unroll
        for (int n = 0; n < 2; ++n) {
          const char* p0 = smem + L_ROW + (4 * g + q4) * 256 + (lw * 2 + n) * 32 + 8 * p4;
          const bf8 f = trfrag_asm<4096>(p0);
          const int col = wcb * 128 + (lw * 2 + n) * 16;
          if (bid < 240) pst16(__builtin_bit_cast(u4, f), rs, lane * 16, WS_W2 + (wks * 40 + (col >> 4)) * 1024);
        }
      }
      WAITL0();
    }
    asm volatile("" :: "v"(pf0));
#pragma unroll
    for (int n = 0; n < 2; ++n) {
#pragma unroll 1
      for (unsigned spin = 0; spin < SPIN; ++spin) {
        const u4 v = *(const u4*)(smem + L_ROW + n * 4096 + lw * 1024 + lane * 16);
        if (__all(v[1] == tag && v[3] == tag)) break;
        __builtin_amdgcn_s_sleep(1);
        asm volatile("" : "+v"(voG[n]) :: "memory");
        *(u4*)(smem + L_ROW + n * 4096 + lw * 1024 + lane * 16) = ld16(rs, voG[n], 0);
      }
      u4 v = *(const u4*)(smem + L_ROW + n * 4096 + lw * 1024 + lane * 16);
#pragma unroll
      for (int e = 0; e < 4; e += 2) {
        const float c = asf(v[e]), r1 = c - bfr(c), r2 = r1 - bfr(r1);
        const unsigned nm = (v[e] & 0xffffu) ^ 0x8000u;
        const bool st = n == 1 && lw == 3;
        v[e + 1] = st ? __builtin_bit_cast(unsigned, __builtin_amdgcn_rcpf(hi16(v[e]))) : pk2(r2, 0.f);
        v[e] = st ? nm | (nm << 16) : pk2(c, r1);
      }
      *(u4*)(smem + L_ROW + n * 4096 + lw * 1024 + lane * 16) = v;
    }
    WAITL0();
    __builtin_amdgcn_s_barrier();
    if (kv) {
      __builtin_amdgcn_s_barrier();
#pragma unroll 1
      for (int it = tid - 256; it < 480; it += 256) {
      const bool isz = it >= 400;
      int hl = isz ? (it - 400) / 20 : it / 100, rem = isz ? (it - 400 - hl * 20) * 5 : it - hl * 100, d = rem / 5, grp = rem - d * 5;
      int hk = hl & 1, kc = 20 * hk + d, a = kc >> 4;
      int pos = isz ? 32 : 40 + 20 * hk + 4 * grp, b = pos >> 4;
      int m = a == 0 ? b - 2 : (a == 1 ? b : b + 3);
      const char* p = smem + ((hl >> 1) * 8 + m) * 1024 + (((pos >> 2) & 3) * 16 + (kc & 15)) * 16;
      f4 v = *(const f4*)p + *(const f4*)(p + 16384);
      if (isz) __builtin_amdgcn_raw_buffer_store_b32(__builtin_bit_cast(unsigned, v[0]), rs, (it - 400) * 4, WS_Z + (rb * 640 + ci * 80) * 4, 0);
      else pst16(__builtin_bit_cast(u4, v), rs, it * 16, WS_KV + (rb * 32 + ci * 4) * 1600);
      }
    }
    if (bid == 0 && w == 7) {
      wait16(rs, lane * 16, WS_FW, tag);
      if (lane == 0) st4(tag, rs, 0, WS_EP);
    }
  } else {
    u4 xa[2], xb[2];
    {
      const int srow = ((bid >> 2) & 1) * 1024 + (bid & 3) * 256 + jj * 8;
      const char* xr = (const char*)x + (srow + w * 2) * (D_ * 2);
      const char *p0 = xr + lane * 16, *p1 = xr + 1024 + l15 * 16;
      asm volatile("global_load_dwordx4 %0, %4, off\n\tglobal_load_dwordx4 %1, %5, off\n\tglobal_load_dwordx4 %2, %4, off offset:1280\n\t"
                   "global_load_dwordx4 %3, %5, off offset:1280"
                   : "=&v"(xa[0]), "=&v"(xb[0]), "=&v"(xa[1]), "=&v"(xb[1]) : "v"(p0), "v"(p1) : "memory");
      asm volatile("s_waitcnt vmcnt(0)" : "+v"(pf0), "+v"(xa[0]), "+v"(xb[0]), "+v"(xa[1]), "+v"(xb[1]) :: "memory");
        float su[2] = {0.f, 0.f}, sq[2] = {0.f, 0.f}, sh[2];
#pragma unroll
        for (int n = 0; n < 2; ++n) {
          sh[n] = lo16(__builtin_amdgcn_readfirstlane(xa[n][0]));
          float s2 = 0.f, q2 = 0.f;
#pragma unroll
          for (int e = 0; e < 4; ++e) {
            const float a0 = lo16(xa[n][e]) - sh[n], a1 = hi16(xa[n][e]) - sh[n], b0 = lo16(xb[n][e]) - sh[n], b1 = hi16(xb[n][e]) - sh[n];
            su[n] += a0 + a1; sq[n] += a0 * a0; sq[n] += a1 * a1; s2 += b0 + b1; q2 += b0 * b0; q2 += b1 * b1;
          }
          if (lane < 16) { su[n] += s2; sq[n] += q2; }
        }
        const float m0 = wave_sum(su[0]) * (1.f / 640.f), m1 = wave_sum(su[1]) * (1.f / 640.f);
        const float e0 = wave_sum(sq[0]) * (1.f / 640.f), e1 = wave_sum(sq[1]) * (1.f / 640.f);
        const float m = lane ? m1 : m0, var = fmaxf((lane ? e1 : e0) - m * m, 0.f);
        const float sd = __builtin_amdgcn_sqrtf(bfr(bfr(var) + 1e-5f));
        if (lane < 2) st8(u2{pk2(m + (lane ? sh[1] : sh[0]), sd), tag}, rs, lane * 8, WS_SG + (srow + w * 2) * 8);
        if (tid == 0) pst16(u4{0u, 0u, 0u, 0u}, rs, 0, WS_F2 + bid * 128);
    }
    if (kv) k1_compute<true>(ws, smem, rb, ci);
    else k1_compute<false>(ws, smem, rb, ci);
  }
}

template <int AUX>
DI bf8 ldfrag(rsrc_t r, int voff, int soff) {
  return __builtin_bit_cast(bf8, __builtin_amdgcn_raw_buffer_load_b128(r, voff, soff, AUX));
}
DI void st8_sc1(char* p, unsigned a, unsigned b) {
  __hip_atomic_store((AS1 unsigned long long*)p, (unsigned long long)a | ((unsigned long long)b << 32), __ATOMIC_RELAXED,
                     __HIP_MEMORY_SCOPE_AGENT);
}
constexpr int L2_ACC = 0;
constexpr int L2_WO = 61440;
constexpr int L2_KV = L2_WO + 5120;
constexpr int L2_Z = L2_KV + 2048;
constexpr int L2_X = L2_Z + 2048;
constexpr int K2_LDS = 98304;

#define RING 3
DI void k2_publish(char* ws, int flag, int lane) {
  WAITV(0);
  if (lane == 0) __hip_atomic_store((AS1 unsigned*)(ws + WS_F2 + flag), 1u, __ATOMIC_RELAXED, __HIP_MEMORY_SCOPE_AGENT);
}
__global__ __launch_bounds__(512, 2) void k2_kernel(const __bf16* __restrict__ x, const __bf16* __restrict__ wo,
                                                    char* ws, __bf16* __restrict__ out) {
  extern __shared__ __attribute__((aligned(16))) char smem[];
  const int tid = threadIdx.x, lane = tid & 63, w = __builtin_amdgcn_readfirstlane(tid >> 6);
  const int g = lane >> 4, l15 = lane & 15;
  const int xcd = blockIdx.x & 7, idx = blockIdx.x >> 3;
  const int r = (xcd >> 1) * 8 + (idx & 7), c = (xcd & 1) * 4 + (idx >> 3);
  unsigned pf0 = 0;
  if (w == 7 || w == 6) {
    const char* pc;
    asm volatile("s_getpc_b64 %0" : "=s"(pc));
    asm volatile("global_load_dword %0, %1, off" : "=&v"(pf0) : "v"(tid - 384 < 94 ? pc + (tid - 384) * 128 : (const char*)(out + (r * 64 + (lane & 1) * 63) * D_ + c * 80)) : "memory");
  }
  const rsrc_t rs = mkrsrc(ws, (unsigned)WS_END);
  const int vo = lane * 16;
  const f4 zero4 = {0.f, 0.f, 0.f, 0.f};
  if (__builtin_expect(w >= 4, 0)) {
  __builtin_amdgcn_s_setprio(3);
  if (w == 4) {
    const f4* p = (const f4*)(ws + WS_KV) + r * 100;
    const int i1 = lane < 36 ? lane + 64 : lane;
    f4 v0[16], v1[16];
#pragma unroll
    for (int i = 0; i < 16; ++i) { v0[i] = p[i * 3200 + lane]; v1[i] = p[i * 3200 + i1]; }
    u4 wv[4];
#pragma unroll
    for (int i = 0; i < 4; ++i) {
      int ch = i * 64 + lane, row = ch / 10;
      wv[i] = u4{0u, 0u, 0u, 0u};
      if (ch < 200) wv[i] = *(const u4*)(wo + (20 * r + row) * D_ + 80 * c + (ch - row * 10) * 8);
    }
#pragma unroll
    for (int st = 1; st < 16; st *= 2)
#pragma unroll
      for (int i = 0; i < 16; i += 2 * st) { v0[i] += v0[i + st]; v1[i] += v1[i + st]; }
    *(f4*)(smem + L2_KV + vo) = v0[0];
    *(f4*)(smem + L2_KV + 1024 + vo) = v1[0];
#pragma unroll
    for (int i = 0; i < 4; ++i) *(u4*)(smem + L2_WO + i * 1024 + vo) = wv[i];
    *(u4*)(smem + L2_WO + 4096 + vo) = u4{0u, 0u, 0u, 0u};
    u4 au[2];
#pragma unroll
    for (int dt = 0; dt < 2; ++dt)
#pragma unroll
      for (int hh = 0; hh < 2; ++hh) {
        int d = dt * 16 + l15, grp = 2 * g + hh;
        bool ok = d < 20 && grp < 5;
        f4 v = *(const f4*)(smem + L2_KV + (ok ? d * 5 + grp : 0) * 16);
        au[dt][2 * hh] = ok ? pk2(v[0], v[1]) : 0u;
        au[dt][2 * hh + 1] = ok ? pk2(v[2], v[3]) : 0u;
      }
    const int k0 = 20 * r + 4 * g, k1 = 20 * r + 16;
    const char* p0 = smem + L2_WO + (8 * g + (l15 >> 2)) * 160 + (lane & 3) * 8;
    char* mf = ws + WS_MF + c * 5120 + l15 * 16;
    const int o0 = (k0 >> 5) * 40960 + ((k0 >> 2) & 3) * 256 + ((k0 >> 4) & 1) * 8;
    const int o1 = (k1 >> 5) * 40960 + ((k1 >> 2) & 3) * 256 + ((k1 >> 4) & 1) * 8;
#pragma unroll
    for (int t = 0; t < 5; ++t) {
      bf8 b = trfrag(p0 + t * 32, p0 + t * 32 + 640);
      f4 m0 = mfma(__builtin_bit_cast(bf8, au[0]), b, zero4), m1 = mfma(__builtin_bit_cast(bf8, au[1]), b, zero4);
      st8_sc1(mf + t * 1024 + o0, pk2(m0[0], m0[1]), pk2(m0[2], m0[3]));
      if (g == 0) st8_sc1(mf + t * 1024 + o1, pk2(m1[0], m1[1]), pk2(m1[2], m1[3]));
    }
    k2_publish(ws, (r * 8 + c) * 128, lane);
  } else if (w == 5 || w == 6) {
    const int fo = ((c >> 1) * 5 * 128 + r * 4 + (c & 1) * 2 + w - 5) * 1024;
    const f4* zp = (const f4*)(ws + WS_Z) + (c >> 1) * 40 + (lane < 40 ? lane : 0);
    f4 zv[16];
#pragma unroll
    for (int i = 0; i < 16; ++i) zv[i] = zp[i * 160];
    u4 pq[5];
#pragma unroll
    for (int k = 0; k < 5; ++k) pq[k] = __builtin_bit_cast(u4, ldfrag<0>(rs, vo, (int)WS_PQ + fo + k * 131072));
#pragma unroll
    for (int st = 1; st < 16; st *= 2)
#pragma unroll
      for (int i = 0; i < 16; i += 2 * st) zv[i] += zv[i + st];
    char* zl = smem + L2_Z + (w - 5) * 1024;
    if (lane < 40) *(u2*)(zl + lane * 8) = u2{pk2(zv[0][0], zv[0][1]), pk2(zv[0][2], zv[0][3])};
    f4 den = zero4;
#pragma unroll
    for (int k = 0; k < 5; ++k) {
      u4 zf;
#pragma unroll
      for (int p = 0; p < 2; ++p) {
        int gi = 8 * k + 4 * p + g;
        u2 z2 = *(const u2*)(zl + gi * 8);
        bool mine = gi / 5 == l15;
        zf[2 * p] = mine ? z2[0] : 0u;
        zf[2 * p + 1] = mine ? z2[1] : 0u;
      }
      den = mfma(__builtin_bit_cast(bf8, zf), __builtin_bit_cast(bf8, pq[k]), den);
    }
    f4 rd;
#pragma unroll
    for (int e = 0; e < 4; ++e) rd[e] = __builtin_amdgcn_rcpf(bfr(bfr(den[e]) + 1e-6f));
    if (g < 2) *(f4*)(zl + 512 + l15 * 32 + g * 16) = rd;
#pragma unroll
    for (int k = 0; k < 5; ++k) {
      u4 o;
#pragma unroll
      for (int p = 0; p < 2; ++p) {
        float s = *(const float*)(zl + 512 + l15 * 32 + ((8 * k + 4 * p + g) / 5) * 4);
        o[2 * p] = pk2(lo16(pq[k][2 * p]) * s, hi16(pq[k][2 * p]) * s);
        o[2 * p + 1] = pk2(lo16(pq[k][2 * p + 1]) * s, hi16(pq[k][2 * p + 1]) * s);
      }
      st16(o, rs, vo, WS_AF + fo + k * 131072);
    }
    k2_publish(ws, (r * 8 + c) * 128 + (w - 4) * 4, lane);
  }
    if (w == 7) {
      const rsrc_t rx = mkrsrc(x, S_ * D_ * 2);
#pragma unroll 1
      for (int i = 0; i < 10; ++i) {
        const int ch = i * 64 + lane, row = ch / 10;
        blds16<0>(rx, smem + L2_X + i * 1024, ((r * 64 + row) * D_ + c * 80 + (ch - row * 10) * 8) * 2, 0);
      }
    }
    asm volatile("s_waitcnt vmcnt(0)" : "+v"(pf0) :: "memory");
    __builtin_amdgcn_s_barrier();
    return;
  }
  const int hA = (int)WS_HF + (w * 128 + r * 4) * 1024, hB = (int)WS_W2 + (w * 8 + c) * 5120;
  const int aA = (int)WS_AF + (w * 128 + r * 4) * 1024, aB = (int)WS_MF + (w * 8 + c) * 5120;
#define ROT 0
  const int rot = ROT ? ((idx >> 3) * 3 + (idx & 7) * 5) % 12 : 0, rot2 = ROT ? idx % 5 : 0;
  bf8 fa[RING][4], fb[RING][5];
  auto ld = [&](int i) {
    const int sl = i % RING;
    if (i < 12) {
      int ii = i + rot;
      ii = ii >= 12 ? ii - 12 : ii;
#pragma unroll
      for (int j = 0; j < 5; ++j) fb[sl][j] = ldfrag<LAUX>(rs, vo + j * 1024, hB + ii * 163840);
#pragma unroll
      for (int j = 0; j < 4; ++j) fa[sl][j] = ldfrag<LAUX>(rs, vo + j * 1024, hA + ii * 524288);
    } else {
      int ii = i - 12 + rot2;
      ii = ii >= 5 ? ii - 5 : ii;
#pragma unroll
      for (int j = 0; j < 5; ++j) fb[sl][j] = ldfrag<16>(rs, vo + j * 1024, aB + ii * 163840);
#pragma unroll
      for (int j = 0; j < 4; ++j) fa[sl][j] = ldfrag<16>(rs, vo + j * 1024, aA + ii * 524288);
    }
  };
#pragma unroll
  for (int i = 0; i < RING; ++i) ld(i);
  const int orow = (r * 64 + w * 16 + l15) * D_ + c * 80 + 4 * g;
  const AS1 unsigned* fl = (const AS1 unsigned*)(ws + WS_F2 + (lane < 16 ? (r * 8 + (lane >> 1)) * 128 + 4 + (lane & 1) * 4 : (((lane - 16) & 31) * 8 + c) * 128));
  __builtin_amdgcn_sched_barrier(0);
  f4 acc[4][5];
#pragma unroll
  for (int i = 0; i < 4; ++i)
#pragma unroll
    for (int j = 0; j < 5; ++j) acc[i][j] = zero4;
  unsigned flv = 0;
#pragma unroll
  for (int i = 0; i < 17; ++i) {
    const int sl = i % RING;
#pragma unroll
    for (int j = 0; j < 5; ++j)
#pragma unroll
      for (int m = 0; m < 4; ++m) acc[m][j] = mfma(fb[sl][j], fa[sl][m], acc[m][j]);
    if (i + RING == 11) flv = __hip_atomic_load(fl, __ATOMIC_RELAXED, __HIP_MEMORY_SCOPE_AGENT);
    if (i + RING == 12) {
      for (unsigned spin = 0; !__all(flv == 1u) && spin < (1u << 22); ++spin) {
        __builtin_amdgcn_s_sleep(1);
        flv = __hip_atomic_load(fl, __ATOMIC_RELAXED, __HIP_MEMORY_SCOPE_AGENT);
      }
      __builtin_amdgcn_fence(__ATOMIC_ACQUIRE, "wavefront");
    }
    if (i + RING < 17) ld(i + RING);

    __builtin_amdgcn_sched_barrier(0);
  }
#pragma unroll
  for (int m = 0; m < 4; ++m) {
    if (m != w) {
      int slot = m < w ? m : m - 1;
#pragma unroll
      for (int j = 0; j < 5; ++j) *(f4*)(smem + L2_ACC + ((w * 3 + slot) * 5 + j) * 1024 + vo) = acc[m][j];
    }
  }
  WAITL0();
  __builtin_amdgcn_s_barrier();
  f4 own[5];
#pragma unroll
  for (int j = 0; j < 5; ++j) own[j] = w == 0 ? acc[0][j] : (w == 1 ? acc[1][j] : (w == 2 ? acc[2][j] : acc[3][j]));
  f4 tot[5];
  u2 xr[5];
#pragma unroll
  for (int j = 0; j < 5; ++j) xr[j] = *(const u2*)(smem + L2_X + (w * 16 + l15) * 160 + j * 32 + g * 8);
#pragma unroll
  for (int j = 0; j < 5; ++j) tot[j] = f4{lo16(xr[j][0]), hi16(xr[j][0]), lo16(xr[j][1]), hi16(xr[j][1])};
#pragma unroll
  for (int s = 0; s < 4; ++s) {
    int slot = w < s ? w : w - 1;
#pragma unroll
    for (int j = 0; j < 5; ++j) {
      f4 v = own[j];
      if (s != w) v = *(const f4*)(smem + L2_ACC + ((s * 3 + slot) * 5 + j) * 1024 + vo);
      tot[j] += v;
    }
  }
#pragma unroll
  for (int j = 0; j < 5; ++j) *(u2*)(out + orow + 16 * j) = u2{pk2(tot[j][0], tot[j][1]), pk2(tot[j][2], tot[j][3])};
}

extern "C" void kernel_launch(void* const* d_in, const int* in_sizes, int n_in, void* d_out, int out_size, void* d_ws,
                              size_t ws_size, hipStream_t stream) {
  const __bf16* x = (const __bf16*)d_in[0];
  const __bf16* wqkv = (const __bf16*)d_in[1];
  const __bf16* wo = (const __bf16*)d_in[2];
  const __bf16* w1 = (const __bf16*)d_in[4];
  const __bf16* w2 = (const __bf16*)d_in[5];
  char* ws = (char*)d_ws;
  (void)hipFuncSetAttribute((const void*)k1_kernel, hipFuncAttributeMaxDynamicSharedMemorySize, K1_LDS);
  (void)hipFuncSetAttribute((const void*)k2_kernel, hipFuncAttributeMaxDynamicSharedMemorySize, K2_LDS);
  k1_kernel<<<256, 512, K1_LDS, stream>>>(x, wqkv, w1, w2, ws, wo);
  k2_kernel<<<256, 512, K2_LDS, stream>>>(x, wo, ws, (__bf16*)d_out);
}
```
